```python
import torch

def _softmax(x, axis=-1):
    return torch.softmax(x, dim=axis)
def _gelu(x):
    return torch.nn.functional.gelu(x, approximate="tanh")
def _silu(x):
    return torch.nn.functional.silu(x)
def _sigmoid(x):
    return torch.sigmoid(x)
def _layernorm(x, axis=-1, eps=1e-5):
    m = x.mean(dim=axis, keepdim=True)
    v = x.var(dim=axis, keepdim=True, unbiased=False)
    return (x - m) / torch.sqrt(v + eps)
def _scatter_add(tgt, idx, src, axis=0):
    return tgt.clone().index_add_(axis, idx, src)
def _segment_sum(src, seg, n):
    out = torch.zeros((n,) + src.shape[1:], dtype=src.dtype)
    return out.index_add_(0, seg, src)
def _pad(x, pw):
    flat = tuple(v for pair in reversed(pw) for v in pair)
    return torch.nn.functional.pad(x, flat)

kernel_name = 'interleave_layers__linear_kernel_hedgehog__grouped_kv_mqa__prompt'

def setup_inputs():

    _w = (lambda a: (lambda t: t.to(torch.bfloat16) if t.is_floating_point() else t)(torch.as_tensor(a)))
    def _A_setup():
        B, S, D, H = 1, 2048, 1024, 32
        Dh = D // H
        return {
            "x": _w((lambda *s: torch.randn(*s, dtype=torch.bfloat16) * 0.05)(B, S, D)),
            "wqkv": _w((lambda *s: torch.randn(*s, dtype=torch.bfloat16) * 0.2)(D, 3 * D)),
            "w_phi": _w((lambda *s: torch.randn(*s, dtype=torch.bfloat16) * 0.2)(H, Dh, Dh) * (Dh ** -0.5)),
            "wo": _w((lambda *s: torch.randn(*s, dtype=torch.bfloat16) * 0.05)(D, D)),
        }
    _A_in = _A_setup()

    def _B_setup():
        B, S, D, H = 1, 2048, 1024, 32
        Dh = D // H
        return {
            "x": _w((lambda *s: torch.randn(*s, dtype=torch.bfloat16) * 0.05)(B, S, D)),
            "wq": _w((lambda *s: torch.randn(*s, dtype=torch.bfloat16) * 0.1)(D, H * Dh)),
            "wk": _w((lambda *s: torch.randn(*s, dtype=torch.bfloat16) * 0.1)(D, Dh)),
            "wv": _w((lambda *s: torch.randn(*s, dtype=torch.bfloat16) * 0.05)(D, Dh)),
            "wo": _w((lambda *s: torch.randn(*s, dtype=torch.bfloat16) * 0.05)(H * Dh, D)),
        }
    _B_in = _B_setup()

    return {
        "A_x": _A_in["x"],
        "A_wqkv": _A_in["wqkv"],
        "A_w_phi": _A_in["w_phi"],
        "A_wo": _A_in["wo"],
        "B_x": _B_in["x"],
        "B_wq": _B_in["wq"],
        "B_wk": _B_in["wk"],
        "B_wv": _B_in["wv"],
        "B_wo": _B_in["wo"],
    }

def reference(A_x, A_wqkv, A_w_phi, A_wo, B_x, B_wq, B_wk, B_wv, B_wo):
    def _A_ref(x, wqkv, w_phi, wo):
        B, S, D = x.shape
        H = 32; Dh = D // H
        qkv = (lambda x, *d: x.permute(*d))((x @ wqkv).reshape(B, S, 3, H, Dh), 2, 0, 3, 1, 4)
        q, k, v = qkv[0], qkv[1], qkv[2]
        pq = torch.einsum("bhsd,hdf->bhsf", q, w_phi)
        pk = torch.einsum("bhsd,hdf->bhsf", k, w_phi)
        phi_q = torch.concatenate([_softmax(pq, axis=-1), _softmax(-pq, axis=-1)], axis=-1)
        phi_k = torch.concatenate([_softmax(pk, axis=-1), _softmax(-pk, axis=-1)], axis=-1)
        kv = torch.einsum("bhsf,bhsd->bhfd", phi_k, v)
        z  = torch.einsum("bhsf->bhf", phi_k)
        num = torch.einsum("bhqf,bhfd->bhqd", phi_q, kv)
        den = torch.einsum("bhqf,bhf->bhq", phi_q, z)[..., None] + 1e-6
        o = num / den
        return (lambda x, *d: x.permute(*d))(o, 0, 2, 1, 3).reshape(B, S, D) @ wo

    def _B_ref(x, wq, wk, wv, wo):
        B, S, D = x.shape
        H = 32
        Dh = D // H
        q = (lambda x, *d: x.permute(*d))((x @ wq).reshape(B, S, H, Dh), 0, 2, 1, 3)
        k = x @ wk
        v = x @ wv
        scores = torch.einsum("bhqd,bkd->bhqk", q, k) * (Dh ** -0.5)
        p = _softmax(scores, axis=-1)
        o = torch.einsum("bhqk,bkd->bhqd", p, v)
        return (lambda x, *d: x.permute(*d))(o, 0, 2, 1, 3).reshape(B, S, H * Dh) @ wo

    x = A_x
    x = x + _A_ref(_layernorm(x, axis=-1), A_wqkv, A_w_phi, A_wo)
    x = x + _B_ref(_layernorm(x, axis=-1), B_wq, B_wk, B_wv, B_wo)
    x = x + _A_ref(_layernorm(x, axis=-1), A_wqkv, A_w_phi, A_wo)
    x = x + _B_ref(_layernorm(x, axis=-1), B_wq, B_wk, B_wv, B_wo)
    return x
```

```cpp
#include <hip/hip_runtime.h>
#include <hip/hip_bf16.h>
typedef __hip_bfloat16 bf16;
constexpr int S=2048, D=1024, H=32, DH=32;
__device__ inline float b2f(bf16 x){return __bfloat162float(x);}
__device__ inline bf16 f2b(float x){return __float2bfloat16(x);}

__global__ void k_ln(const bf16* x, bf16* y){
  int row=blockIdx.x; __shared__ float red[8];
  float v[4]; float s=0;
  for(int i=0;i<4;i++){v[i]=b2f(x[row*D+threadIdx.x+256*i]); s+=v[i];}
  for(int o=32;o;o>>=1) s+=__shfl_xor(s,o);
  if((threadIdx.x&63)==0) red[threadIdx.x>>6]=s; __syncthreads();
  float m=(red[0]+red[1]+red[2]+red[3])/D; __syncthreads();
  float q=0; for(int i=0;i<4;i++){q+=(v[i]-m)*(v[i]-m);}
  for(int o=32;o;o>>=1) q+=__shfl_xor(q,o);
  if((threadIdx.x&63)==0) red[threadIdx.x>>6]=q; __syncthreads();
  float var=(red[0]+red[1]+red[2]+red[3])/D;
  float r=rsqrtf(var+1e-5f);
  for(int i=0;i<4;i++) y[row*D+threadIdx.x+256*i]=f2b((v[i]-m)*r);
}
__global__ void k_gemm(const bf16* A,const bf16* W,bf16* C,const bf16* res,int N,int K,int ldc){
  __shared__ float As[32][33], Ws[32][33];
  int tx=threadIdx.x&31, ty=threadIdx.x>>5;
  int r0=blockIdx.y*32, c0=blockIdx.x*32; float acc[4]={0,0,0,0};
  for(int k0=0;k0<K;k0+=32){
    for(int i=0;i<4;i++){As[ty+8*i][tx]=b2f(A[(r0+ty+8*i)*K+k0+tx]); Ws[ty+8*i][tx]=b2f(W[(k0+ty+8*i)*N+c0+tx]);}
    __syncthreads();
    for(int k=0;k<32;k++) for(int i=0;i<4;i++) acc[i]+=As[ty+8*i][k]*Ws[k][tx];
    __syncthreads();
  }
  for(int i=0;i<4;i++){int r=r0+ty+8*i,c=c0+tx; float v=acc[i]; if(res){ v=b2f(f2b(v))+b2f(res[r*ldc+c]);} C[r*ldc+c]=f2b(v);}
}
__global__ void k_phi(const bf16* qkv,const bf16* wphi,bf16* phi){
  int s=blockIdx.x, which=blockIdx.y; int h=threadIdx.x>>5, f=threadIdx.x&31;
  float p=0; for(int d=0;d<32;d++) p+=b2f(qkv[s*3072+which*1024+h*32+d])*b2f(wphi[h*1024+d*32+f]);
  p=b2f(f2b(p));
  float mx=p,mn=p; for(int o=16;o;o>>=1){mx=fmaxf(mx,__shfl_xor(mx,o)); mn=fminf(mn,__shfl_xor(mn,o));}
  float e1=__expf(p-mx), e2=__expf(mn-p); float s1=e1,s2=e2;
  for(int o=16;o;o>>=1){s1+=__shfl_xor(s1,o); s2+=__shfl_xor(s2,o);}
  phi[((size_t)which*S+s)*2048+h*64+f]=f2b(e1/s1); phi[((size_t)which*S+s)*2048+h*64+32+f]=f2b(e2/s2);
}
__global__ void k_kv(const bf16* phik,const bf16* qkv,float* kv,float* z){
  int h=blockIdx.x; int f=blockIdx.y*8+(threadIdx.x>>5), d=threadIdx.x&31; float a=0,zz=0;
  for(int s=0;s<S;s++){float p=b2f(phik[(size_t)s*2048+h*64+f]); a+=p*b2f(qkv[s*3072+2048+h*32+d]); zz+=p;}
  kv[(h*64+f)*32+d]=b2f(f2b(a)); if(d==0) z[h*64+f]=b2f(f2b(zz));
}
__global__ void k_o(const bf16* phiq,const float* kv,const float* z,bf16* o){
  int s=blockIdx.x; int h=threadIdx.x>>5,d=threadIdx.x&31; float n=0,de=0;
  for(int f=0;f<64;f++){float p=b2f(phiq[(size_t)s*2048+h*64+f]); n+=p*kv[(h*64+f)*32+d]; de+=p*z[h*64+f];}
  o[s*D+h*32+d]=f2b(n/(de+1e-6f));
}
__global__ void k_attn(const bf16* q,const bf16* k,const bf16* v,bf16* o){
  int idx=blockIdx.x*blockDim.x+threadIdx.x; int s=idx>>5,h=idx&31;
  float qq[32],acc[32]; for(int d=0;d<32;d++){qq[d]=b2f(q[s*D+h*32+d])*0.17677669529663687f; acc[d]=0;}
  float m=-1e30f,l=0;
  __shared__ float ks[64][32], vs[64][32];
  for(int j0=0;j0<S;j0+=64){
    __syncthreads();
    for(int i=threadIdx.x;i<2048;i+=blockDim.x){ks[i>>5][i&31]=b2f(k[(j0+(i>>5))*32+(i&31)]); vs[i>>5][i&31]=b2f(v[(j0+(i>>5))*32+(i&31)]);}
    __syncthreads();
    for(int j=0;j<64;j++){
      float sc=0; for(int d=0;d<32;d++) sc+=qq[d]*ks[j][d];
      float mn=fmaxf(m,sc); float al=__expf(m-mn), p=__expf(sc-mn); l=l*al+p;
      for(int d=0;d<32;d++) acc[d]=acc[d]*al+p*vs[j][d];
      m=mn;
    }
  }
  for(int d=0;d<32;d++) o[s*D+h*32+d]=f2b(acc[d]/l);
}
extern "C" void kernel_launch(void* const* d_in, const int* in_sizes, int n_in, void* d_out, int out_size, void* d_ws, size_t ws_size, hipStream_t stream){
  const bf16* x0=(const bf16*)d_in[0]; const bf16* wqkv=(const bf16*)d_in[1]; const bf16* wphi=(const bf16*)d_in[2]; const bf16* woA=(const bf16*)d_in[3];
  const bf16* wq=(const bf16*)d_in[5]; const bf16* wk=(const bf16*)d_in[6]; const bf16* wv=(const bf16*)d_in[7]; const bf16* woB=(const bf16*)d_in[8];
  char* ws=(char*)d_ws; size_t MB=1<<20;
  bf16* ln=(bf16*)(ws); bf16* qkv=(bf16*)(ws+8*MB); bf16* phi=(bf16*)(ws+24*MB); float* kv=(float*)(ws+48*MB); float* z=(float*)(ws+49*MB);
  bf16* o=(bf16*)(ws+50*MB); bf16* xa=(bf16*)(ws+56*MB); bf16* xb=(bf16*)(ws+62*MB); bf16* kk=(bf16*)(ws+68*MB); bf16* vv=(bf16*)(ws+69*MB);
  const bf16* x=x0;
  for(int L=0;L<2;L++){
    k_ln<<<S,256,0,stream>>>(x,ln);
    k_gemm<<<dim3(96,64),256,0,stream>>>(ln,wqkv,qkv,nullptr,3072,1024,3072);
    k_phi<<<dim3(S,2),1024,0,stream>>>(qkv,wphi,phi);
    k_kv<<<dim3(32,8),256,0,stream>>>(phi+(size_t)S*2048,qkv,kv,z);
    k_o<<<S,1024,0,stream>>>(phi,kv,z,o);
    k_gemm<<<dim3(32,64),256,0,stream>>>(o,woA,xa,x,1024,1024,1024);
    k_ln<<<S,256,0,stream>>>(xa,ln);
    k_gemm<<<dim3(32,64),256,0,stream>>>(ln,wq,qkv,nullptr,1024,1024,1024);
    k_gemm<<<dim3(1,64),256,0,stream>>>(ln,wk,kk,nullptr,32,1024,32);
    k_gemm<<<dim3(1,64),256,0,stream>>>(ln,wv,vv,nullptr,32,1024,32);
    k_attn<<<S*32/256,256,0,stream>>>(qkv,kk,vv,o);
    bf16* xo = L==0? xb : (bf16*)d_out;
    k_gemm<<<dim3(32,64),256,0,stream>>>(o,woB,xo,xa,1024,1024,1024);
    x=xo;
  }
}
```

```cpp
#include <hip/hip_runtime.h>
#include <hip/hip_bf16.h>
#include <stdint.h>

typedef unsigned short u16;
using bf16x8 = __attribute__((ext_vector_type(8))) short;
using s16x4 = __attribute__((ext_vector_type(4))) short;
using f32x16 = __attribute__((ext_vector_type(16))) float;
using f32x4 = __attribute__((ext_vector_type(4))) float;
using f32x2 = __attribute__((ext_vector_type(2))) float;
using u32x4 = __attribute__((ext_vector_type(4))) unsigned;
using u32x2 = __attribute__((ext_vector_type(2))) unsigned;
typedef __bf16 bf16x2_t __attribute__((ext_vector_type(2)));
#define LAS __attribute__((address_space(3)))
#define GAS __attribute__((address_space(1)))
typedef LAS char* lds_ptr;
typedef short v4i16_t __attribute__((ext_vector_type(4)));
#define DI __device__ __forceinline__
#define MFMA32(a, b, c) __builtin_amdgcn_mfma_f32_32x32x16_bf16((a), (b), (c), 0, 0, 0)
#define MFMA16(a, b, c) __builtin_amdgcn_mfma_f32_16x16x32_bf16((a), (b), (c), 0, 0, 0)
#define SB() __builtin_amdgcn_sched_barrier(0)
#define RLX_AGENT __ATOMIC_RELAXED, __HIP_MEMORY_SCOPE_AGENT
#define UNROLL _Pragma("unroll")

constexpr float LOG2E = 1.4426950408889634f;
constexpr unsigned SPIN_CAP = 1u << 20;

constexpr size_t MBy = 1u << 20;
constexpr size_t OFF_FLAGA = 0;
constexpr size_t OFF_FLAGB = 8192;
constexpr size_t FLAG_BYTES = 12288;
constexpr size_t OFF_CS = 64 * 1024;
constexpr size_t OFF_WKVF = 128 * 1024;
constexpr size_t OFF_WPHIF = 256 * 1024;
constexpr size_t OFF_ST = 1 * MBy;
constexpr size_t OFF_KVF = 2 * MBy;
constexpr size_t OFF_SLAB = 4 * MBy;
constexpr size_t SLAB_B = 8448;
constexpr size_t OFF_ACT = 16 * MBy;

DI void glds16(const void* sbase, unsigned voff, unsigned lds_base) {
  unsigned sv;
  asm volatile("s_mov_b32 %0, m0\n\ts_mov_b32 m0, %3\n\ts_nop 0\n\tglobal_load_lds_dwordx4 %1, %2\n\ts_mov_b32 m0, %0"
               : "=&s"(sv) : "v"(voff), "s"(sbase), "s"(lds_base) : "memory");
}
DI void glds16_sc1(const void* sbase, unsigned voff, unsigned lds_base) {
  unsigned sv;
  asm volatile("s_mov_b32 %0, m0\n\ts_mov_b32 m0, %3\n\ts_nop 0\n\tglobal_load_lds_dwordx4 %1, %2 sc1\n\ts_mov_b32 m0, %0"
               : "=&s"(sv) : "v"(voff), "s"(sbase), "s"(lds_base) : "memory");
}
template <int N> DI void wait_vm_bar() { asm volatile("s_waitcnt vmcnt(%0)\n\ts_barrier" ::"n"(N) : "memory"); }
DI void bar_only() { asm volatile("s_barrier" ::: "memory"); }
DI void lgkm_bar() { asm volatile("s_waitcnt lgkmcnt(0)\n\ts_barrier" ::: "memory"); }
DI void drain_vm() { asm volatile("s_waitcnt vmcnt(0)" ::: "memory"); }
DI s16x4 lds_tr(lds_ptr p) { return __builtin_bit_cast(s16x4, __builtin_amdgcn_ds_read_tr16_b64_v4i16((LAS v4i16_t*)p)); }
template <int E> DI bf16x8 lds_trfrag(lds_ptr p) { s16x4 lo = lds_tr(p), hi = lds_tr(p + E); return __builtin_shufflevector(lo, hi, 0, 1, 2, 3, 4, 5, 6, 7); }
DI bf16x8 lds_b128(lds_ptr p) { return *(LAS bf16x8*)p; }
DI unsigned cvtpk(float lo, float hi) {
  bf16x2_t v = __builtin_convertvector((f32x2){lo, hi}, bf16x2_t);
  return __builtin_bit_cast(unsigned, v);
}
DI float bflo(unsigned u) { return __uint_as_float(u << 16); }
DI float bfhi(unsigned u) { return __uint_as_float(u & 0xffff0000u); }
DI float dot2(unsigned a, unsigned b, float c) {
  return __builtin_amdgcn_fdot2_f32_bf16(__builtin_bit_cast(bf16x2_t, a), __builtin_bit_cast(bf16x2_t, b), c, false);
}
DI float half_add(float x) { auto r = __builtin_amdgcn_permlane32_swap(__float_as_uint(x), __float_as_uint(x), false, false); return __uint_as_float(r[0]) + __uint_as_float(r[1]); }
DI float half_max(float x) { auto r = __builtin_amdgcn_permlane32_swap(__float_as_uint(x), __float_as_uint(x), false, false); return fmaxf(__uint_as_float(r[0]), __uint_as_float(r[1])); }
DI float half_min(float x) { auto r = __builtin_amdgcn_permlane32_swap(__float_as_uint(x), __float_as_uint(x), false, false); return fminf(__uint_as_float(r[0]), __uint_as_float(r[1])); }
DI bf16x8 pack8(const f32x16& x, int s) {
  u32x4 p = {cvtpk(x[8 * s], x[8 * s + 1]), cvtpk(x[8 * s + 2], x[8 * s + 3]), cvtpk(x[8 * s + 4], x[8 * s + 5]), cvtpk(x[8 * s + 6], x[8 * s + 7])};
  return __builtin_bit_cast(bf16x8, p);
}
DI void gather8(const f32x16& a, int gp, float (&v)[8]) {
  UNROLL for (int j = 0; j < 4; j++) {
    auto s = __builtin_amdgcn_permlane32_swap(__float_as_uint(a[8 * gp + j]), __float_as_uint(a[8 * gp + 4 + j]), false, false);
    v[j] = __uint_as_float(s[0]); v[4 + j] = __uint_as_float(s[1]);
  }
}
DI void st16_sc1(void* p, u32x4 v) { asm volatile("global_store_dwordx4 %0, %1, off sc1\n\ts_nop 1" ::"v"(p), "v"(v) : "memory"); }
DI f32x4 ld16_sc1(const void* base, unsigned off) {
  auto rs = __builtin_amdgcn_make_buffer_rsrc((void*)base, 0, 0x7fffffff, 0x00020000);
  return __builtin_bit_cast(f32x4, __builtin_amdgcn_raw_buffer_load_b128(rs, off, 0, 16));
}
DI void load_stats(const f32x2* ST, int rt, int lane, float& mean, float& rstd) {
  const GAS f32x2* p = (const GAS f32x2*)ST + (rt * 16 + (lane >> 5) * 8) * 32 + (lane & 31);
  f32x2 v[8];
  UNROLL for (int i = 0; i < 8; i++) v[i] = p[i * 32];
  float s = ((v[0].x + v[1].x) + (v[2].x + v[3].x)) + ((v[4].x + v[5].x) + (v[6].x + v[7].x));
  float q = ((v[0].y + v[1].y) + (v[2].y + v[3].y)) + ((v[4].y + v[5].y) + (v[6].y + v[7].y));
  s = half_add(s); q = half_add(q);
  mean = s * (1.f / 1024.f);
  float var = fmaxf(q * (1.f / 1024.f) - mean * mean, 0.f);
  rstd = __builtin_amdgcn_rsqf(var + 1e-5f);
}
DI f32x16 init_acc(const float* cs, int hi, float negmean) {
  f32x16 a;
  UNROLL for (int g = 0; g < 4; g++) {
    f32x4 c = *(const GAS f32x4*)(cs + 8 * g + 4 * hi);
    UNROLL for (int j = 0; j < 4; j++) a[4 * g + j] = c[j] * negmean;
  }
  return a;
}
DI void dual_softmax(const f32x16& p, f32x16& ep, f32x16& em) {
  float mx = p[0], mn = p[0];
  UNROLL for (int i = 1; i < 16; i++) { mx = fmaxf(mx, p[i]); mn = fminf(mn, p[i]); }
  mx = half_max(mx); mn = half_min(mn);
  const float a = -mx * LOG2E, b = mn * LOG2E;
  float sp = 0.f, sm = 0.f;
  UNROLL for (int i = 0; i < 16; i++) {
    ep[i] = __builtin_amdgcn_exp2f(__builtin_fmaf(p[i], LOG2E, a));
    em[i] = __builtin_amdgcn_exp2f(__builtin_fmaf(p[i], -LOG2E, b));
    sp += ep[i]; sm += em[i];
  }
  sp = __builtin_amdgcn_rcpf(half_add(sp)); sm = __builtin_amdgcn_rcpf(half_add(sm));
  UNROLL for (int i = 0; i < 16; i++) { ep[i] *= sp; em[i] *= sm; }
}
DI bf16x8 sel_frag(int lane) {
  short v = (((lane >> 2) & 1) == ((lane >> 4) & 1)) ? (short)0x3f80 : (short)0;
  return (bf16x8){v, v, v, v, v, v, v, v};
}

__global__ __launch_bounds__(512) void k_prep(const char* __restrict__ x0, const char* __restrict__ wqkv, const u16* __restrict__ wphi,
                                              const char* __restrict__ wq, const u16* __restrict__ wk, const u16* __restrict__ wv, char* __restrict__ ws) {
  __shared__ float red[8][64];
  const int tid = threadIdx.x, lane = tid & 63, wid = tid >> 6, bid = blockIdx.x;
  if (bid < 64) {
    const char* W = bid < 48 ? wqkv + bid * 128 : wq + (bid - 48) * 128;
    const int ld = bid < 48 ? 6144 : 2048;
    float a[8] = {0, 0, 0, 0, 0, 0, 0, 0};
    u32x4 v[16];
    UNROLL for (int i = 0; i < 16; i++) v[i] = *(const GAS u32x4*)(W + (size_t)(wid * 128 + i * 8 + (lane >> 3)) * ld + (lane & 7) * 16);
    UNROLL for (int i = 0; i < 16; i++) UNROLL for (int j = 0; j < 4; j++) { a[2 * j] += bflo(v[i][j]); a[2 * j + 1] += bfhi(v[i][j]); }
    UNROLL for (int j = 0; j < 8; j++) { a[j] += __shfl_xor(a[j], 8); a[j] += __shfl_xor(a[j], 16); a[j] += __shfl_xor(a[j], 32); }
    if (lane < 8) UNROLL for (int j = 0; j < 8; j++) red[wid][lane * 8 + j] = a[j];
    __syncthreads();
    if (tid < 64) {
      float s = 0;
      UNROLL for (int w = 0; w < 8; w++) s += red[w][tid];
      ((float*)(ws + OFF_CS))[bid * 64 + tid] = s;
    }
  } else if (bid < 192) {
    const int task = (bid - 64) * 8 + wid, rt = task >> 4, ct = task & 15, r = lane & 31, hi = lane >> 5;
    u32x4 v[4];
    UNROLL for (int i = 0; i < 4; i++) v[i] = *(const GAS u32x4*)(x0 + (size_t)(rt * 32 + r) * 2048 + ct * 128 + i * 32 + hi * 16);
    float s = 0, q = 0;
    UNROLL for (int i = 0; i < 4; i++) {
      *(GAS u32x4*)(ws + OFF_ACT + (size_t)rt * 65536 + (ct * 4 + i) * 1024 + lane * 16) = v[i];
      UNROLL for (int j = 0; j < 4; j++) { s = dot2(v[i][j], 0x3f803f80u, s); q = dot2(v[i][j], v[i][j], q); }
    }
    s = half_add(s); q = half_add(q);
    if (hi == 0) ((f32x2*)(ws + OFF_ST))[(rt * 16 + ct) * 32 + r] = (f32x2){s, q};
  } else if (bid < 200) {
    const int t = (bid - 192) * 512 + tid, h = t >> 7, s = (t >> 6) & 1, f = lane & 31, hi = lane >> 5;
    bf16x8 o;
    UNROLL for (int j = 0; j < 8; j++) o[j] = (short)wphi[h * 1024 + (16 * s + 8 * (j >> 2) + 4 * hi + (j & 3)) * 32 + f];
    *(GAS bf16x8*)(ws + OFF_WPHIF + (size_t)t * 16) = o;
  } else if (bid < 216) {
    const int t = (bid - 200) * 512 + tid, type = t >> 12, kk = (t >> 7) & 31, u = (t >> 6) & 1;
    const u16* W = type ? wv : wk;
    bf16x8 o;
    UNROLL for (int j = 0; j < 8; j++) o[j] = (short)W[(32 * kk + 8 * (lane >> 4) + j) * 32 + 16 * u + (lane & 15)];
    *(GAS bf16x8*)(ws + OFF_WKVF + (size_t)t * 16) = o;
  } else if (bid == 216) {
    const u16* W = (lane >> 5) ? wv : wk;
    float s = 0;
    for (int k = 0; k < 128; k++) s += __uint_as_float((unsigned)W[(wid * 128 + k) * 32 + (lane & 31)] << 16);
    red[wid][lane] = s;
    __syncthreads();
    if (tid < 64) {
      float t = 0;
      UNROLL for (int w = 0; w < 8; w++) t += red[w][tid];
      ((float*)(ws + OFF_CS))[4096 + tid] = t;
    }
  } else if (bid == 217) {
    for (int i = tid; i < (int)(FLAG_BYTES / 4); i += 512) ((unsigned*)ws)[i] = 0u;
  }
}

constexpr int A_SLOT = 24576, A_IMG = 2 * A_SLOT, A_ROW = 200, A_KVL = A_IMG + 256 * A_ROW, A_ZL = A_KVL + 4096, A_LDS = 6 * A_SLOT;
__global__ __launch_bounds__(512, 2) void k_a(const char* __restrict__ XF, const f32x2* __restrict__ ST, const char* __restrict__ W,
                                              const float* __restrict__ CS, const char* __restrict__ WPHIF, char* SLAB, unsigned* FLAG,
                                              char* __restrict__ OF) {
  extern __shared__ __attribute__((aligned(16))) char lds_raw[];
  const lds_ptr lds = (lds_ptr)lds_raw;
  const unsigned l0 = (unsigned)(uintptr_t)lds;
  const int tid = threadIdx.x, lane = tid & 63, wid = __builtin_amdgcn_readfirstlane(tid >> 6);
  const int bid = blockIdx.x, xcd = bid & 7, idx = bid >> 3;
  const int tt = (xcd & 1) * 4 + (idx & 3), h = (xcd >> 1) * 8 + (idx >> 2);
  const int rot = idx * 5 + xcd * 3;
  const int r = lane & 31, hi = lane >> 5, rt = tt * 8 + wid;
  constexpr int NC = 8, SPC = 8, NSTEP = 64, LOOK = 3, PD = 8, BD = 1, BR = BD + 1;
  const char* Wb = W + h * 64 + (size_t)wid * 16 * 6144;
  const unsigned boff = (lane >> 2) * 6144 + (lane & 3) * 16;
  auto dma = [&](int c) {
    const int cc = (c + rot) & (NC - 1);
    UNROLL for (int i = 0; i < 3; i++) glds16(Wb + (size_t)cc * 128 * 6144 + i * 2048, boff, l0 + (c % 6) * A_SLOT + i * 8192 + wid * 1024);
  };
  const char* Aw = XF + (size_t)rt * 65536 + lane * 16;
  auto aload = [&](int s) { const int cc = ((s / SPC) + rot) & (NC - 1); return *(const GAS bf16x8*)(Aw + cc * 8192 + (s % SPC) * 1024); };
  dma(0);
  float mean, rstd;
  bf16x8 a[PD];
  UNROLL for (int s = 0; s < PD; s++) a[s] = aload(s);
  UNROLL for (int c = 1; c < LOOK; c++) dma(c);
  load_stats(ST, rt, lane, mean, rstd);
  f32x16 acc[3];
  UNROLL for (int i = 0; i < 3; i++) acc[i] = init_acc(CS + i * 1024 + h * 32, hi, -mean);
  bf16x8 wph[2];
  UNROLL for (int s = 0; s < 2; s++) wph[s] = *(const GAS bf16x8*)(WPHIF + (h * 2 + s) * 1024 + lane * 16);
  const int q4 = (lane & 15) >> 2, cbp = ((lane >> 4) & 1) * 32 + (lane & 3) * 8;
  const lds_ptr bb = lds + (8 * hi + q4) * 64 + cbp;
  bf16x8 bf[BR][3];
  auto chunk_sync = [&](int c) {
    if (c < LOOK) wait_vm_bar<0>();
    else if (c * SPC + PD <= NSTEP) wait_vm_bar<LOOK * SPC>();
    else wait_vm_bar<0>();
    if (c + LOOK < NC) dma(c + LOOK);
  };
  auto chunk_sync_early = [&](int c) {
    if (c + LOOK < NC) dma(c + LOOK);
  };
  auto bread = [&](int s) {
    const lds_ptr p = bb + ((s / SPC) % 6) * A_SLOT + (s % SPC) * 1024;
    UNROLL for (int i = 0; i < 3; i++) bf[s % BR][i] = lds_trfrag<256>(p + i * 8192);
  };
  chunk_sync(0);
  UNROLL for (int s = 0; s < BD; s++) bread(s);
  UNROLL for (int s = 0; s < NSTEP; s++) {
    if (s + BD < NSTEP) {
      if ((s + BD) % SPC == 0) { const int c = (s + BD) / SPC; if (c < LOOK) chunk_sync_early(c); else chunk_sync(c); }
      bread(s + BD);
    }
    SB();
    const bf16x8 av = a[s % PD];
    UNROLL for (int i = 0; i < 3; i++) acc[i] = MFMA32(bf[s % BR][i], av, acc[i]);
    SB();
    if (s + PD < NSTEP) a[s % PD] = aload(s + PD);
    SB();
  }
  const f32x16 zero = {};
  {
    UNROLL for (int i = 0; i < 16; i++) { acc[1][i] *= rstd; acc[2][i] *= rstd; }
    f32x16 p = MFMA32(wph[0], pack8(acc[1], 0), zero);
    p = MFMA32(wph[1], pack8(acc[1], 1), p);
    const lds_ptr row = lds + A_IMG + (wid * 32 + r) * A_ROW + hi * 8;
    UNROLL for (int g = 0; g < 4; g++) *(LAS u32x2*)(row + 128 + g * 16) = (u32x2){cvtpk(acc[2][4 * g], acc[2][4 * g + 1]), cvtpk(acc[2][4 * g + 2], acc[2][4 * g + 3])};
    f32x16 ep, em;
    dual_softmax(p, ep, em);
    UNROLL for (int g = 0; g < 4; g++) {
      *(LAS u32x2*)(row + g * 16) = (u32x2){cvtpk(ep[4 * g], ep[4 * g + 1]), cvtpk(ep[4 * g + 2], ep[4 * g + 3])};
      *(LAS u32x2*)(row + 64 + g * 16) = (u32x2){cvtpk(em[4 * g], em[4 * g + 1]), cvtpk(em[4 * g + 2], em[4 * g + 3])};
    }
  }
  lgkm_bar();
  char* slab = SLAB + (size_t)(h * 8) * SLAB_B;
  GAS unsigned* flag = (GAS unsigned*)FLAG + h * 32;
  if (wid < 4) {
    const int fh = wid & 1;
    const lds_ptr ip = lds + A_IMG + (8 * hi + q4) * A_ROW + cbp;
    if (wid < 2) {
      f32x16 kv = zero;
      UNROLL for (int u = 0; u < 16; u++) {
        const bf16x8 pf = lds_trfrag<4 * A_ROW>(ip + u * 16 * A_ROW + fh * 64), vf = lds_trfrag<4 * A_ROW>(ip + u * 16 * A_ROW + 128);
        kv = MFMA32(pf, vf, kv);
      }
      UNROLL for (int g = 0; g < 4; g++)
        st16_sc1(slab + tt * SLAB_B + fh * 4096 + g * 1024 + lane * 16,
                 (u32x4){__float_as_uint(kv[4 * g]), __float_as_uint(kv[4 * g + 1]), __float_as_uint(kv[4 * g + 2]), __float_as_uint(kv[4 * g + 3])});
    } else {
      const bf16x8 sel = sel_frag(lane);
      f32x4 z = {};
      UNROLL for (int u = 0; u < 16; u++) z = MFMA16(sel, lds_trfrag<4 * A_ROW>(ip + u * 16 * A_ROW + fh * 64), z);
      if (lane < 32) __hip_atomic_store((GAS unsigned*)(slab + tt * SLAB_B + 8192 + fh * 128 + lane * 4), __float_as_uint(z[0]), RLX_AGENT);
    }
    drain_vm();
    if (lane == 0) __hip_atomic_store(flag + tt * 4 + wid, 1u, RLX_AGENT);
  }
  bf16x8 phq[4];
  {
    UNROLL for (int i = 0; i < 16; i++) acc[0][i] *= rstd;
    f32x16 p = MFMA32(wph[0], pack8(acc[0], 0), zero);
    p = MFMA32(wph[1], pack8(acc[0], 1), p);
    f32x16 ep, em;
    dual_softmax(p, ep, em);
    phq[0] = pack8(ep, 0); phq[1] = pack8(ep, 1); phq[2] = pack8(em, 0); phq[3] = pack8(em, 1);
  }
  for (unsigned spins = 0; spins < SPIN_CAP; spins++) {
    const unsigned v = __hip_atomic_load(flag + (lane & 31), RLX_AGENT);
    if (__all(v == 1u)) break;
    __builtin_amdgcn_s_sleep(1);
  }
  asm volatile("" ::: "memory");
  {
    f32x4 v[8];
    UNROLL for (int t = 0; t < 8; t++) v[t] = ld16_sc1(slab, t * SLAB_B + wid * 1024 + lane * 16);
    float zs = 0.f;
    if (wid == 0) {
      UNROLL for (int t = 0; t < 8; t++) zs += __uint_as_float(__hip_atomic_load((GAS unsigned*)(slab + t * SLAB_B + 8192 + lane * 4), RLX_AGENT));
    }
    const f32x4 s = ((v[0] + v[1]) + (v[2] + v[3])) + ((v[4] + v[5]) + (v[6] + v[7]));
    *(LAS u32x2*)(lds + A_KVL + (wid >> 1) * 1024 + lane * 16 + (wid & 1) * 8) = (u32x2){cvtpk(s[0], s[1]), cvtpk(s[2], s[3])};
    if (wid == 0) *(LAS u16*)(lds + A_ZL + lane * 2) = (u16)cvtpk(zs, zs);
  }
  lgkm_bar();
  {
    f32x16 num = zero, den = zero;
    UNROLL for (int fs = 0; fs < 4; fs++) {
      const bf16x8 kf = lds_b128(lds + A_KVL + fs * 1024 + lane * 16);
      const s16x4 z0 = *(LAS s16x4*)(lds + A_ZL + fs * 32 + hi * 8), z1 = *(LAS s16x4*)(lds + A_ZL + fs * 32 + hi * 8 + 16);
      num = MFMA32(kf, phq[fs], num);
      den = MFMA32(__builtin_shufflevector(z0, z1, 0, 1, 2, 3, 4, 5, 6, 7), phq[fs], den);
    }
    const float rd = 1.f / (den[0] + 1e-6f);
    UNROLL for (int gp = 0; gp < 2; gp++) {
      float v[8];
      gather8(num, gp, v);
      *(GAS u32x4*)(OF + (size_t)rt * 65536 + (h * 2 + gp) * 1024 + lane * 16) =
          (u32x4){cvtpk(v[0] * rd, v[1] * rd), cvtpk(v[2] * rd, v[3] * rd), cvtpk(v[4] * rd, v[5] * rd), cvtpk(v[6] * rd, v[7] * rd)};
    }
  }
}

constexpr int G_NC = 8, G_SPC = 8, G_NSTEP = 64, G_LOOK = 2, G_PD = 16, G_BD = 2, G_BR = G_BD + 1;
DI void gemm64_loader(const char* Wb, unsigned l0, int lw, int lane, int rot) {
  const unsigned boff = (lane >> 3) * 2048 + (((lane & 7) ^ (((lane >> 4) & 1) << 2)) << 4);
  auto dma = [&](int c) {
    const int cc = (c + rot) & (G_NC - 1);
    UNROLL for (int i = 0; i < 8; i++) glds16(Wb + (size_t)(cc * 128 + (lw * 8 + i) * 8) * 2048, boff, l0 + c * 16384 + (lw * 8 + i) * 1024);
  };
  UNROLL for (int c = 0; c < G_LOOK; c++) dma(c);
  UNROLL for (int c = 0; c < G_NC; c++) {
    if (c + G_LOOK <= G_NC) wait_vm_bar<(G_LOOK - 1) * 8>(); else wait_vm_bar<0>();
    if (c + G_LOOK < G_NC) dma(c + G_LOOK);
  }
}
DI bf16x8 gemm64_aload(const char* Aw, int s, int rot) {
  const int cc = ((s / G_SPC) + rot) & (G_NC - 1);
  return *(const GAS bf16x8*)(Aw + cc * 8192 + (s % G_SPC) * 1024);
}
template <class Hook>
DI void gemm64_compute(const char* Aw, lds_ptr lds, int lane, int rot, bf16x8 (&a)[G_PD], f32x16 (&acc)[2], Hook hook) {
  const int hi = lane >> 5, q4 = (lane & 15) >> 2;
  lds_ptr bb[2];
  UNROLL for (int n = 0; n < 2; n++) bb[n] = lds + (8 * hi + q4) * 128 + ((n ^ (q4 >> 1)) << 6) + ((lane >> 4) & 1) * 32 + (lane & 3) * 8;
  bf16x8 bf[G_BR][2];
  auto bread = [&](int s) { UNROLL for (int n = 0; n < 2; n++) bf[s % G_BR][n] = lds_trfrag<512>(bb[n] + s * 2048); };
  lgkm_bar();
  UNROLL for (int s = 0; s < G_BD; s++) bread(s);
  UNROLL for (int s = 0; s < G_NSTEP; s++) {
    if (s + G_BD < G_NSTEP) {
      if ((s + G_BD) % G_SPC == 0) bar_only();
      bread(s + G_BD);
    }
    hook(s);
    SB();
    const bf16x8 av = a[s % G_PD];
    acc[0] = MFMA32(bf[s % G_BR][0], av, acc[0]);
    acc[1] = MFMA32(bf[s % G_BR][1], av, acc[1]);
    SB();
    if (s + G_PD < G_NSTEP) a[s % G_PD] = gemm64_aload(Aw, s + G_PD, rot);
    SB();
  }
}

template <bool FINAL>
__global__ __launch_bounds__(384) void k_out(const char* __restrict__ AF, const char* __restrict__ W, const char* __restrict__ XinF,
                                             char* __restrict__ Xout, f32x2* __restrict__ ST) {
  extern __shared__ __attribute__((aligned(16))) char lds_raw[];
  const lds_ptr lds = (lds_ptr)lds_raw;
  const int tid = threadIdx.x, lane = tid & 63, wid = __builtin_amdgcn_readfirstlane(tid >> 6);
  const int bid = blockIdx.x, xcd = bid & 7, idx = bid >> 3;
  const int pm = (xcd >> 1) * 4 + (idx & 3), pn = (xcd & 1) * 8 + (idx >> 2);
  const int rot = idx * 5 + xcd * 3;
  if (wid >= 4) { gemm64_loader(W + pn * 128, (unsigned)(uintptr_t)lds, wid - 4, lane, rot); return; }
  const int rt = pm * 4 + wid, r = lane & 31, hi = lane >> 5;
  const char* Aw = AF + (size_t)rt * 65536 + lane * 16;
  bf16x8 a[G_PD];
  UNROLL for (int s = 0; s < G_PD; s++) a[s] = gemm64_aload(Aw, s, rot);
  f32x16 acc[2] = {{}, {}};
  u32x4 res[4];
  const size_t fo = (size_t)rt * 65536 + pn * 4096 + lane * 16;
  gemm64_compute(Aw, lds, lane, rot, a, acc, [&](int s) {
    if (s == G_NSTEP - 8) UNROLL for (int i = 0; i < 4; i++) res[i] = *(const GAS u32x4*)(XinF + fo + i * 1024);
  });
  float ss = 0.f, sq = 0.f;
  UNROLL for (int i = 0; i < 4; i++) {
    float v[8];
    gather8(acc[i >> 1], i & 1, v);
    u32x4 o;
    UNROLL for (int j = 0; j < 4; j++) o[j] = cvtpk(v[2 * j] + bflo(res[i][j]), v[2 * j + 1] + bfhi(res[i][j]));
    if (FINAL) *(GAS u32x4*)(Xout + (size_t)(rt * 32 + r) * 2048 + pn * 128 + i * 32 + hi * 16) = o;
    else {
      *(GAS u32x4*)(Xout + fo + i * 1024) = o;
      UNROLL for (int j = 0; j < 4; j++) { ss = dot2(o[j], 0x3f803f80u, ss); sq = dot2(o[j], o[j], sq); }
    }
  }
  if (!FINAL) {
    ss = half_add(ss); sq = half_add(sq);
    if (hi == 0) ST[(rt * 16 + pn) * 32 + r] = (f32x2){ss, sq};
  }
}

constexpr int B_SPARE = 131072, B_QX = 65536, B_LDS = 163840;
constexpr float QSCALE = 0.17677669529663687f * LOG2E;
__global__ __launch_bounds__(512, 2) void k_b(const char* __restrict__ XF, const f32x2* __restrict__ ST, const char* __restrict__ W,
                                              const float* __restrict__ CS, const char* __restrict__ WKVF, char* KVF, unsigned* FLAG,
                                              char* __restrict__ OF) {
  extern __shared__ __attribute__((aligned(16))) char lds_raw[];
  const lds_ptr lds = (lds_ptr)lds_raw;
  const unsigned l0 = (unsigned)(uintptr_t)lds;
  const int tid = threadIdx.x, lane = tid & 63, wid = __builtin_amdgcn_readfirstlane(tid >> 6);
  const int bid = blockIdx.x, xcd = bid & 7, idx = bid >> 3;
  const int pm = (xcd >> 1) * 4 + (idx & 3), pj = (xcd & 1) * 8 + (idx >> 2);
  const int rot = idx * 5 + xcd * 3;
  const int hi = lane >> 5, g4 = lane >> 4, c16 = lane & 15;
  const int rt = pm * 4 + (wid & 3), head = pj * 2 + (wid >> 2);
  const int krt = pm * 4 + (pj >> 2), kth = (pj >> 1) & 1, ktype = pj & 1;
  bf16x8 kx[4], kw[4][2];
  UNROLL for (int i = 0; i < 4; i++) {
    const int kk = wid * 4 + i;
    kx[i] = *(const GAS bf16x8*)(XF + (size_t)krt * 65536 + (2 * kk + (g4 >> 1)) * 1024 + (16 * kth + c16 + 32 * (g4 & 1)) * 16);
    UNROLL for (int u = 0; u < 2; u++) kw[i][u] = *(const GAS bf16x8*)(WKVF + ((ktype * 32 + kk) * 2 + u) * 1024 + lane * 16);
  }
  auto kv_partial = [&]() {
    f32x4 d[2] = {{}, {}};
    if (ktype == 0) { UNROLL for (int i = 0; i < 4; i++) UNROLL for (int u = 0; u < 2; u++) d[u] = MFMA16(kw[i][u], kx[i], d[u]); }
    else { UNROLL for (int i = 0; i < 4; i++) UNROLL for (int u = 0; u < 2; u++) d[u] = MFMA16(kx[i], kw[i][u], d[u]); }
    UNROLL for (int u = 0; u < 2; u++) *(LAS f32x4*)(lds + B_SPARE + (wid * 2 + u) * 1024 + lane * 16) = d[u];
  };
  auto kv_dma = [&](int ch, int first, int n) {
    const int cc = (ch + rot) & 7;
    for (int i = 0; i < n; i++) glds16_sc1(KVF + cc * 32768 + (first + i) * 1024, lane * 16, l0 + ((ch + 4) % 5) * 32768 + (first + i) * 1024);
  };
  bf16x8 qf[2];
  if (wid < 4) {
    const char* Aw = XF + (size_t)rt * 65536 + lane * 16;
    bf16x8 a[G_PD];
    UNROLL for (int s = 0; s < G_PD; s++) a[s] = gemm64_aload(Aw, s, rot);
    float mean, rstd;
    load_stats(ST, rt, lane, mean, rstd);
    f32x16 acc[2];
    UNROLL for (int n = 0; n < 2; n++) acc[n] = init_acc(CS + 3072 + pj * 64 + n * 32, hi, -mean);
    kv_partial();
    gemm64_compute(Aw, lds, lane, rot, a, acc, [&](int) {});
    const float sc = rstd * QSCALE;
    UNROLL for (int i = 0; i < 16; i++) { acc[0][i] *= sc; acc[1][i] *= sc; }
    qf[0] = pack8(acc[0], 0); qf[1] = pack8(acc[0], 1);
    UNROLL for (int s = 0; s < 2; s++) *(LAS bf16x8*)(lds + B_QX + (wid * 2 + s) * 1024 + lane * 16) = pack8(acc[1], s);
    lgkm_bar();
  } else {
    if (wid < 6) {
      kv_partial();
      asm volatile("s_waitcnt lgkmcnt(0)" ::: "memory");
      gemm64_loader(W + pj * 128, l0, wid - 4, lane, rot);
    } else if (wid == 6) {
      const GAS f32x2* sp = (const GAS f32x2*)ST + (krt * 16 + g4 * 4) * 32 + 16 * kth + c16;
      f32x2 sv[4];
      UNROLL for (int i = 0; i < 4; i++) sv[i] = sp[i * 32];
      f32x4 cs[2];
      UNROLL for (int u = 0; u < 2; u++) {
        if (ktype == 0) cs[u] = *(const GAS f32x4*)(CS + 4096 + 16 * u + 4 * g4);
        else { const float c = CS[4096 + 32 + 16 * u + c16]; cs[u] = (f32x4){c, c, c, c}; }
      }
      kv_partial();
      float s = (sv[0].x + sv[1].x) + (sv[2].x + sv[3].x), q = (sv[0].y + sv[1].y) + (sv[2].y + sv[3].y);
      s += __shfl_xor(s, 16); q += __shfl_xor(q, 16);
      s = half_add(s); q = half_add(q);
      const float mean = s * (1.f / 1024.f);
      const float rstd = __builtin_amdgcn_rsqf(fmaxf(q * (1.f / 1024.f) - mean * mean, 0.f) + 1e-5f);
      f32x4 m4, r4;
      UNROLL for (int j = 0; j < 4; j++) {
        const float mj = __shfl(mean, 4 * g4 + j), rj = __shfl(rstd, 4 * g4 + j);
        m4[j] = ktype ? mj : mean; r4[j] = ktype ? rj : rstd;
      }
      lgkm_bar();
      UNROLL for (int u = 0; u < 2; u++) {
        f32x4 d = {};
        UNROLL for (int w = 0; w < 8; w++) d += *(LAS f32x4*)(lds + B_SPARE + (w * 2 + u) * 1024 + lane * 16);
        d = (d - m4 * cs[u]) * r4;
        const int frag = ktype ? 2 + kth : u, lp = (ktype ? 16 * u : 16 * kth) + c16 + 32 * (g4 & 1);
        const unsigned long long w64 = (unsigned long long)cvtpk(d[0], d[1]) | ((unsigned long long)cvtpk(d[2], d[3]) << 32);
        __hip_atomic_store((GAS unsigned long long*)(KVF + krt * 4096 + frag * 1024 + lp * 16 + (g4 >> 1) * 8), w64, RLX_AGENT);
      }
      bar_only();
      bar_only();
      drain_vm();
      if (lane == 0) __hip_atomic_store((GAS unsigned*)FLAG + pm * 16 + pj, 1u, RLX_AGENT);
      UNROLL for (int c = 3; c < G_NC; c++) bar_only();
    } else {
      kv_partial();
      lgkm_bar();
      UNROLL for (int c = 1; c < G_NC; c++) bar_only();
      for (unsigned spins = 0; spins < SPIN_CAP; spins++) {
        const u32x4 v = __builtin_bit_cast(u32x4, ld16_sc1(FLAG, lane * 16));
        if (__all(v[0] == 1u && v[1] == 1u && v[2] == 1u && v[3] == 1u)) break;
        __builtin_amdgcn_s_sleep(1);
      }
      __builtin_amdgcn_fence(__ATOMIC_ACQUIRE, "agent");
      drain_vm();
      kv_dma(0, 0, 32);
      drain_vm();
    }
    bar_only();
    UNROLL for (int s = 0; s < 2; s++) qf[s] = lds_b128(lds + B_QX + ((wid & 3) * 2 + s) * 1024 + lane * 16);
  }
  kv_dma(1, wid * 4, 4);
  kv_dma(2, wid * 4, 4);
  const bf16x8 sel = sel_frag(lane);
  const f32x16 zero = {};
  f32x16 O = zero, negm, SA, SB_;
  f32x4 L = {};
  bf16x8 kf[2], vf[2];
  lds_ptr cur = lds + 4 * 32768 + lane * 16;
  kf[0] = lds_b128(cur); kf[1] = lds_b128(cur + 1024);
  SA = MFMA32(kf[0], qf[0], zero); SA = MFMA32(kf[1], qf[1], SA);
  kf[0] = lds_b128(cur + 4096); kf[1] = lds_b128(cur + 4096 + 1024);
  vf[0] = lds_b128(cur + 2048); vf[1] = lds_b128(cur + 3072);
  {
    float m = SA[0];
    UNROLL for (int i = 1; i < 16; i++) m = fmaxf(m, SA[i]);
    m = half_max(m);
    UNROLL for (int i = 0; i < 16; i++) { negm[i] = -m; SA[i] -= m; }
  }
  auto step = [&](f32x16& Sc, f32x16& Sn, lds_ptr kp, lds_ptr vp) {
    Sn = MFMA32(kf[0], qf[0], negm);
    Sn = MFMA32(kf[1], qf[1], Sn);
    kf[0] = lds_b128(kp); kf[1] = lds_b128(kp + 1024);
    UNROLL for (int i = 0; i < 8; i++) Sc[i] = __builtin_amdgcn_exp2f(Sc[i]);
    const bf16x8 p0 = pack8(Sc, 0);
    O = MFMA32(vf[0], p0, O);
    L = MFMA16(sel, p0, L);
    UNROLL for (int i = 8; i < 16; i++) Sc[i] = __builtin_amdgcn_exp2f(Sc[i]);
    const bf16x8 p1 = pack8(Sc, 1);
    O = MFMA32(vf[1], p1, O);
    L = MFMA16(sel, p1, L);
    vf[0] = lds_b128(vp + 2048); vf[1] = lds_b128(vp + 3072);
  };
  for (int ch = 0; ch < 8; ch++) {
    const lds_ptr nxt = lds + ((ch + 5) % 5) * 32768 + lane * 16;
    UNROLL for (int t = 0; t < 8; t += 2) {
      if (t == 6) {
        if (ch < 6) wait_vm_bar<4>(); else wait_vm_bar<0>();
        if (ch + 3 < 8) kv_dma(ch + 3, wid * 4, 4);
      }
      step(SA, SB_, t + 2 < 8 ? cur + (t + 2) * 4096 : nxt, cur + (t + 1) * 4096);
      step(SB_, SA, t + 3 < 8 ? cur + (t + 3) * 4096 : nxt + 4096, t + 2 < 8 ? cur + (t + 2) * 4096 : nxt);
    }
    cur = nxt;
  }
  float l = L[0];
  {
    float chk = l;
    UNROLL for (int i = 0; i < 16; i++) chk = fmaxf(chk, fabsf(O[i]) * 0x1p-27f);
    bool bad = !(chk < 0x1p100f);
    UNROLL for (int i = 0; i < 16; i++) bad |= (O[i] != O[i]);
    if (__any(bad)) {
      O = zero; l = 0.f;
      float m = -__builtin_inff();
      for (int t = 0; t < 64; t++) {
        bf16x8 f[4];
        UNROLL for (int i = 0; i < 4; i++) f[i] = __builtin_bit_cast(bf16x8, ld16_sc1(KVF, t * 4096 + i * 1024 + lane * 16));
        f32x16 s = MFMA32(f[0], qf[0], zero);
        s = MFMA32(f[1], qf[1], s);
        float mt = s[0];
        UNROLL for (int i = 1; i < 16; i++) mt = fmaxf(mt, s[i]);
        const float mn = fmaxf(m, half_max(mt));
        const float al = __builtin_amdgcn_exp2f(m - mn);
        m = mn;
        UNROLL for (int i = 0; i < 16; i++) { s[i] = __builtin_amdgcn_exp2f(s[i] - mn); O[i] *= al; }
        const bf16x8 p0 = pack8(s, 0), p1 = pack8(s, 1);
        f32x4 lt = MFMA16(sel, p0, ((f32x4){0.f, 0.f, 0.f, 0.f}));
        lt = MFMA16(sel, p1, lt);
        l = l * al + lt[0];
        O = MFMA32(f[2], p0, O);
        O = MFMA32(f[3], p1, O);
      }
    }
  }
  const float rl = 1.f / l;
  UNROLL for (int gp = 0; gp < 2; gp++) {
    float v[8];
    gather8(O, gp, v);
    *(GAS u32x4*)(OF + (size_t)rt * 65536 + (head * 2 + gp) * 1024 + lane * 16) =
        (u32x4){cvtpk(v[0] * rl, v[1] * rl), cvtpk(v[2] * rl, v[3] * rl), cvtpk(v[4] * rl, v[5] * rl), cvtpk(v[6] * rl, v[7] * rl)};
  }
}

extern "C" void kernel_launch(void* const* d_in, const int* in_sizes, int n_in, void* d_out, int out_size, void* d_ws, size_t ws_size,
                              hipStream_t stream) {
  const char* x0 = (const char*)d_in[0];
  const char* wqkv = (const char*)d_in[1];
  const u16* wphi = (const u16*)d_in[2];
  const char* woA = (const char*)d_in[3];
  const char* wq = (const char*)d_in[5];
  const u16* wk = (const u16*)d_in[6];
  const u16* wv = (const u16*)d_in[7];
  const char* woB = (const char*)d_in[8];
  char* ws = (char*)d_ws;
  (void)hipFuncSetAttribute((const void*)k_a, hipFuncAttributeMaxDynamicSharedMemorySize, A_LDS);
  (void)hipFuncSetAttribute((const void*)k_b, hipFuncAttributeMaxDynamicSharedMemorySize, B_LDS);
  (void)hipFuncSetAttribute((const void*)k_out<false>, hipFuncAttributeMaxDynamicSharedMemorySize, 131072);
  (void)hipFuncSetAttribute((const void*)k_out<true>, hipFuncAttributeMaxDynamicSharedMemorySize, 131072);
  const float* CS = (const float*)(ws + OFF_CS);
  auto act = [&](int i) { return ws + OFF_ACT + (size_t)i * 4 * MBy; };
  auto st = [&](int i) { return (f32x2*)(ws + OFF_ST + (size_t)i * 262144); };
  k_prep<<<218, 512, 0, stream>>>(x0, wqkv, wphi, wq, wk, wv, ws);
  for (int L = 0; L < 2; L++) {
    char* xin = act(4 * L);
    k_a<<<256, 512, A_LDS, stream>>>(xin, st(2 * L), wqkv, CS, ws + OFF_WPHIF, ws + OFF_SLAB + (size_t)L * 256 * SLAB_B,
                                     (unsigned*)(ws + OFF_FLAGA + L * 4096), act(4 * L + 1));
    k_out<false><<<256, 384, 131072, stream>>>(act(4 * L + 1), woA, xin, act(4 * L + 2), st(2 * L + 1));
    k_b<<<256, 512, B_LDS, stream>>>(act(4 * L + 2), st(2 * L + 1), wq, CS, ws + OFF_WKVF, ws + OFF_KVF + (size_t)L * 262144,
                                     (unsigned*)(ws + OFF_FLAGB + L * 1024), act(4 * L + 3));
    if (L == 0) k_out<false><<<256, 384, 131072, stream>>>(act(3), woB, act(2), act(4), st(2));
    else k_out<true><<<256, 384, 131072, stream>>>(act(7), woB, act(6), (char*)d_out, st(3));
  }
}
```

```cpp
#include <hip/hip_runtime.h>
#include <hip/hip_bf16.h>
#include <stdint.h>

typedef unsigned short u16;
using bf16x8 = __attribute__((ext_vector_type(8))) short;
using s16x4 = __attribute__((ext_vector_type(4))) short;
using f32x16 = __attribute__((ext_vector_type(16))) float;
using f32x4 = __attribute__((ext_vector_type(4))) float;
using f32x2 = __attribute__((ext_vector_type(2))) float;
using u32x4 = __attribute__((ext_vector_type(4))) unsigned;
using u32x2 = __attribute__((ext_vector_type(2))) unsigned;
typedef __bf16 bf16x2_t __attribute__((ext_vector_type(2)));
#define LAS __attribute__((address_space(3)))
#define GAS __attribute__((address_space(1)))
typedef LAS char* lds_ptr;
typedef short v4i16_t __attribute__((ext_vector_type(4)));
#define DI __device__ __forceinline__
#define MFMA32(a, b, c) __builtin_amdgcn_mfma_f32_32x32x16_bf16((a), (b), (c), 0, 0, 0)
#define MFMA16(a, b, c) __builtin_amdgcn_mfma_f32_16x16x32_bf16((a), (b), (c), 0, 0, 0)
#define SB() __builtin_amdgcn_sched_barrier(0)
#define RLX_AGENT __ATOMIC_RELAXED, __HIP_MEMORY_SCOPE_AGENT
#define UNROLL _Pragma("unroll")

constexpr float LOG2E = 1.4426950408889634f;
constexpr unsigned SPIN_CAP = 1u << 20;

constexpr unsigned MBy = 1u << 20;
constexpr unsigned OFF_FLAGA = 0;
constexpr unsigned OFF_FLAGX = 16384;
constexpr unsigned OFF_FLAGB = 81920;
constexpr unsigned FLAG_BYTES = 81920 + 32768;
constexpr unsigned OFF_CSKV = 120 * 1024;
constexpr unsigned OFF_CS = 512 * 1024;
constexpr unsigned OFF_WKVF = 128 * 1024;
constexpr unsigned OFF_WPHIF = 256 * 1024;
constexpr unsigned OFF_ST = 1 * MBy;
constexpr unsigned OFF_KVF = 2 * MBy;
constexpr unsigned OFF_SLAB = 4 * MBy;
constexpr unsigned SLAB_B = 8448;
constexpr unsigned OFF_ACT = 16 * MBy;
constexpr unsigned OFF_WTA = 48 * MBy;
constexpr unsigned OFF_WT64 = 54 * MBy;
constexpr unsigned WS_USED = 60 * MBy;

#define STAMP(k, i)
#define STAMPC(k, i)
DI void glds16(const void* sbase, unsigned voff, unsigned lds_base) {
  unsigned sv;
  asm volatile("s_mov_b32 %0, m0\n\ts_mov_b32 m0, %3\n\ts_nop 0\n\tglobal_load_lds_dwordx4 %1, %2\n\ts_mov_b32 m0, %0"
               : "=&s"(sv) : "v"(voff), "s"(sbase), "s"(lds_base) : "memory");
}
DI void glds4(const void* sbase, unsigned voff, unsigned lds_base) {
  unsigned sv;
  asm volatile("s_mov_b32 %0, m0\n\ts_mov_b32 m0, %3\n\ts_nop 0\n\tglobal_load_lds_dword %1, %2\n\ts_mov_b32 m0, %0"
               : "=&s"(sv) : "v"(voff), "s"(sbase), "s"(lds_base) : "memory");
}
#define GL_(o, sc) "global_load_lds_dwordx4 %1, %2 offset:" #o sc "\n\t"
#define GLDS_N(NAME, SC)                                                                                                   \
  template <int N> DI void NAME(const void* sbase, unsigned voff, unsigned lds_base) {                                     \
    unsigned sv;                                                                                                           \
    if constexpr (N == 2)                                                                                                  \
      asm volatile("s_mov_b32 %0, m0\n\ts_mov_b32 m0, %3\n\ts_nop 0\n\t" GL_(0, SC) GL_(1024, SC) "s_mov_b32 m0, %0"       \
                   : "=&s"(sv) : "v"(voff), "s"(sbase), "s"(lds_base) : "memory");                                         \
    else if constexpr (N == 3)                                                                                             \
      asm volatile("s_mov_b32 %0, m0\n\ts_mov_b32 m0, %3\n\ts_nop 0\n\t" GL_(0, SC) GL_(1024, SC) GL_(2048, SC) "s_mov_b32 m0, %0" \
                   : "=&s"(sv) : "v"(voff), "s"(sbase), "s"(lds_base) : "memory");                                         \
    else                                                                                                                   \
      asm volatile("s_mov_b32 %0, m0\n\ts_mov_b32 m0, %3\n\ts_nop 0\n\t" GL_(0, SC) GL_(1024, SC) GL_(2048, SC) GL_(3072, SC) "s_mov_b32 m0, %0" \
                   : "=&s"(sv) : "v"(voff), "s"(sbase), "s"(lds_base) : "memory");                                         \
  }
GLDS_N(gldsN, "")
GLDS_N(gldsN_sc1, " sc1")
DI void glds16_sc1(const void* sbase, unsigned voff, unsigned lds_base) {
  unsigned sv;
  asm volatile("s_mov_b32 %0, m0\n\ts_mov_b32 m0, %3\n\ts_nop 0\n\tglobal_load_lds_dwordx4 %1, %2 sc1\n\ts_mov_b32 m0, %0"
               : "=&s"(sv) : "v"(voff), "s"(sbase), "s"(lds_base) : "memory");
}
template <int N> DI void wait_vm_bar() { asm volatile("s_waitcnt vmcnt(%0)\n\ts_barrier" ::"n"(N) : "memory"); }
DI void bar_only() { asm volatile("s_barrier" ::: "memory"); }
DI void lgkm_bar() { asm volatile("s_waitcnt lgkmcnt(0)\n\ts_barrier" ::: "memory"); }
DI void drain_vm() { asm volatile("s_waitcnt vmcnt(0)" ::: "memory"); }
DI s16x4 lds_tr(lds_ptr p) { return __builtin_bit_cast(s16x4, __builtin_amdgcn_ds_read_tr16_b64_v4i16((LAS v4i16_t*)p)); }
template <int E> DI bf16x8 lds_trfrag(lds_ptr p) { s16x4 lo = lds_tr(p), hi = lds_tr(p + E); return __builtin_shufflevector(lo, hi, 0, 1, 2, 3, 4, 5, 6, 7); }
DI bf16x8 lds_b128(lds_ptr p) { return *(LAS bf16x8*)p; }
DI unsigned cvtpk(float lo, float hi) {
  bf16x2_t v = __builtin_convertvector((f32x2){lo, hi}, bf16x2_t);
  return __builtin_bit_cast(unsigned, v);
}
DI float bflo(unsigned u) { return __uint_as_float(u << 16); }
DI float bfhi(unsigned u) { return __uint_as_float(u & 0xffff0000u); }
DI float dot2(unsigned a, unsigned b, float c) {
  return __builtin_amdgcn_fdot2_f32_bf16(__builtin_bit_cast(bf16x2_t, a), __builtin_bit_cast(bf16x2_t, b), c, false);
}
DI float half_add(float x) { auto r = __builtin_amdgcn_permlane32_swap(__float_as_uint(x), __float_as_uint(x), false, false); return __uint_as_float(r[0]) + __uint_as_float(r[1]); }
DI float half_max(float x) { auto r = __builtin_amdgcn_permlane32_swap(__float_as_uint(x), __float_as_uint(x), false, false); return fmaxf(__uint_as_float(r[0]), __uint_as_float(r[1])); }
DI float half_min(float x) { auto r = __builtin_amdgcn_permlane32_swap(__float_as_uint(x), __float_as_uint(x), false, false); return fminf(__uint_as_float(r[0]), __uint_as_float(r[1])); }
DI bf16x8 pack8(const f32x16& x, int s) {
  u32x4 p = {cvtpk(x[8 * s], x[8 * s + 1]), cvtpk(x[8 * s + 2], x[8 * s + 3]), cvtpk(x[8 * s + 4], x[8 * s + 5]), cvtpk(x[8 * s + 6], x[8 * s + 7])};
  return __builtin_bit_cast(bf16x8, p);
}
DI void gather8(const f32x16& a, int gp, float (&v)[8]) {
  UNROLL for (int j = 0; j < 4; j++) {
    auto s = __builtin_amdgcn_permlane32_swap(__float_as_uint(a[8 * gp + j]), __float_as_uint(a[8 * gp + 4 + j]), false, false);
    v[j] = __uint_as_float(s[0]); v[4 + j] = __uint_as_float(s[1]);
  }
}
typedef __amdgpu_buffer_rsrc_t rsrc_t;
DI u32x4 ld16(rsrc_t rs, unsigned voff, unsigned soff) { return __builtin_amdgcn_raw_buffer_load_b128(rs, voff, soff, 16); }
DI unsigned ld4(rsrc_t rs, unsigned voff, unsigned soff) { return __builtin_amdgcn_raw_buffer_load_b32(rs, voff, soff, 16); }
DI void st16(rsrc_t rs, u32x4 v, unsigned voff, unsigned soff) { __builtin_amdgcn_raw_buffer_store_b128(v, rs, voff + soff, 0, 16); }
DI void st8(rsrc_t rs, u32x2 v, unsigned voff, unsigned soff) { __builtin_amdgcn_raw_buffer_store_b64(v, rs, voff, soff, 16); }
DI void st4(rsrc_t rs, unsigned v, unsigned voff, unsigned soff) { __builtin_amdgcn_raw_buffer_store_b32(v, rs, voff, soff, 16); }
DI void publish(char* ws, unsigned off, int lane) {
  drain_vm();
  if (lane == 0) __hip_atomic_store((GAS unsigned*)(ws + off), 1u, RLX_AGENT);
}
DI void wait_flags(char* ws, unsigned voff) {
  for (unsigned spins = 0; spins < SPIN_CAP; spins++) {
    const unsigned v = __hip_atomic_load((GAS unsigned*)(ws + voff), RLX_AGENT);
    if (__all(v == 1u)) break;
    __builtin_amdgcn_s_sleep(4);
  }
  asm volatile("" ::: "memory");
}
DI void stats_finish(const u32x4 (&v)[4], float& mean, float& rstd) {
  float s = 0.f, q = 0.f;
  UNROLL for (int i = 0; i < 4; i++) { s += __uint_as_float(v[i][0]) + __uint_as_float(v[i][2]); q += __uint_as_float(v[i][1]) + __uint_as_float(v[i][3]); }
  s = half_add(s); q = half_add(q);
  mean = s * (1.f / 1024.f);
  rstd = __builtin_amdgcn_rsqf(fmaxf(q * (1.f / 1024.f) - mean * mean, 0.f) + 1e-5f);
}
DI void split3(float x, unsigned& a, unsigned& b, unsigned& c) {
  a = cvtpk(x, x) & 0xffffu; x -= bflo(a);
  b = cvtpk(x, x) & 0xffffu; x -= bflo(b);
  c = cvtpk(x, x) & 0xffffu;
}
DI bf16x8 cs_frag(float cs, int hi) {
  unsigned a, b, c; split3(cs, a, b, c);
  u32x4 p = {a | (b << 16), a | (c << 16), b | (a << 16), 0u};
  if (hi) p = (u32x4){0u, 0u, 0u, 0u};
  return __builtin_bit_cast(bf16x8, p);
}
DI bf16x8 mean_frag(float mean) {
  unsigned a, b, c; split3(-mean, a, b, c);
  u32x4 p = {a | (a << 16), b | (a << 16), b | (c << 16), 0u};
  return __builtin_bit_cast(bf16x8, p);
}
template <int V> struct IC { static constexpr int value = V; };
DI void dual_softmax(const f32x16& p, f32x16& ep, f32x16& em) {
  float mx = p[0], mn = p[0];
  UNROLL for (int i = 1; i < 16; i++) { mx = fmaxf(mx, p[i]); mn = fminf(mn, p[i]); }
  mx = half_max(mx); mn = half_min(mn);
  const float a = -mx * LOG2E, b = mn * LOG2E;
  float sp = 0.f, sm = 0.f;
  UNROLL for (int i = 0; i < 16; i++) {
    ep[i] = __builtin_amdgcn_exp2f(__builtin_fmaf(p[i], LOG2E, a));
    em[i] = __builtin_amdgcn_exp2f(__builtin_fmaf(p[i], -LOG2E, b));
    sp += ep[i]; sm += em[i];
  }
  sp = __builtin_amdgcn_rcpf(half_add(sp)); sm = __builtin_amdgcn_rcpf(half_add(sm));
  UNROLL for (int i = 0; i < 16; i++) { ep[i] *= sp; em[i] *= sm; }
}
DI bf16x8 sel_frag(int lane) {
  short v = ((lane & 3) == 0 && ((lane >> 2) & 1) == ((lane >> 4) & 1)) ? (short)0x3f80 : (short)0;
  return (bf16x8){v, v, v, v, v, v, v, v};
}

__global__ __launch_bounds__(512) void k_prep(const char* __restrict__ x0, const char* __restrict__ wqkv, const u16* __restrict__ wphi,
                                              const char* __restrict__ woA, const char* __restrict__ wq, const u16* __restrict__ wk,
                                              const u16* __restrict__ wv, const char* __restrict__ woB, char* __restrict__ ws) {
  extern __shared__ __attribute__((aligned(16))) char lds_raw[];
  const lds_ptr lds = (lds_ptr)lds_raw;
  const int tid = threadIdx.x, lane = tid & 63, wid = __builtin_amdgcn_readfirstlane(tid >> 6), bid = blockIdx.x;
  const int r = lane & 31, hi = lane >> 5;
  if (bid < 192) {
    int kb, cb, ld, which;
    const char* W;
    if (bid < 96) { const int t = bid >> 3, g = bid & 7; cb = (t % 3) * 4 + (g >> 1); kb = (t / 3) * 2 + (g & 1); ld = 6144; W = wqkv; which = -1; }
    else { const int t = bid - 96; which = t >> 5; kb = (t >> 2) & 7; cb = t & 3; ld = 2048; W = which == 0 ? woA : (which == 1 ? wq : woB); }
    const unsigned l0 = (unsigned)(uintptr_t)lds + wid * 8192;
    UNROLL for (int i = 0; i < 8; i++) glds16(W + (size_t)(kb * 128 + wid * 16 + i * 2) * ld + cb * 512, hi * ld + r * 16, l0 + i * 1024);
    drain_vm();
    const int step = kb * 8 + wid;
    const lds_ptr p = lds + wid * 8192 + (8 * hi + ((lane & 15) >> 2)) * 512 + ((lane >> 4) & 1) * 32 + (lane & 3) * 8;
    float cs[8];
    UNROLL for (int j = 0; j < 8; j++) {
      const u32x4 fr = __builtin_bit_cast(u32x4, lds_trfrag<2048>(p + j * 64));
      unsigned dst;
      if (which < 0) dst = OFF_WTA + (((cb & 3) * 8 + j) * 192 + step * 3 + (cb >> 2)) * 1024;
      else dst = OFF_WT64 + which * 2 * MBy + ((cb * 4 + (j >> 1)) * 128 + step * 2 + (j & 1)) * 1024;
      *(GAS u32x4*)(ws + dst + lane * 16) = fr;
      float c = 0.f;
      UNROLL for (int e = 0; e < 4; e++) c = dot2(fr[e], 0x3f803f80u, c);
      cs[j] = half_add(c);
    }
    if (which < 0 || which == 1) {
      LAS float* red = (LAS float*)(lds + 65536);
      if (hi == 0) UNROLL for (int j = 0; j < 8; j++) red[wid * 256 + j * 32 + r] = cs[j];
      __syncthreads();
      if (tid < 256) {
        float t = 0.f;
        UNROLL for (int w = 0; w < 8; w++) t += red[w * 256 + tid];
        ((float*)(ws + OFF_CS))[kb * 4096 + (which < 0 ? 0 : 3072) + cb * 256 + tid] = t;
      }
    }
  } else if (bid < 256) {
    const int t = bid - 192, rt = (t & 1) * 32 + (t >> 1);
    u32x4 v[2][4];
    UNROLL for (int c = 0; c < 2; c++) UNROLL for (int i = 0; i < 4; i++)
      v[c][i] = *(const GAS u32x4*)(x0 + (size_t)(rt * 32 + r) * 2048 + (wid * 2 + c) * 128 + i * 32 + hi * 16);
    f32x4 st;
    UNROLL for (int c = 0; c < 2; c++) {
      float s = 0, q = 0;
      UNROLL for (int i = 0; i < 4; i++) {
        *(GAS u32x4*)(ws + OFF_ACT + (size_t)rt * 65536 + ((wid * 2 + c) * 4 + i) * 1024 + lane * 16) = v[c][i];
        UNROLL for (int j = 0; j < 4; j++) { s = dot2(v[c][i][j], 0x3f803f80u, s); q = dot2(v[c][i][j], v[c][i][j], q); }
      }
      st[2 * c] = half_add(s); st[2 * c + 1] = half_add(q);
    }
    if (hi == 0) *(GAS f32x4*)(ws + OFF_ST + rt * 4096 + wid * 512 + r * 16) = st;
  } else if (bid < 264) {
    const int t = (bid - 256) * 512 + tid, h = t >> 7, s = (t >> 6) & 1;
    bf16x8 o;
    UNROLL for (int j = 0; j < 8; j++) o[j] = (short)wphi[h * 1024 + (16 * s + 8 * (j >> 2) + 4 * hi + (j & 3)) * 32 + r];
    *(GAS bf16x8*)(ws + OFF_WPHIF + (size_t)t * 16) = o;
  } else if (bid < 280) {
    const int t = (bid - 264) * 512 + tid, type = t >> 12, kk = (t >> 7) & 31, u = (t >> 6) & 1;
    const u16* W = type ? wv : wk;
    bf16x8 o;
    UNROLL for (int j = 0; j < 8; j++) o[j] = (short)W[(32 * kk + 8 * (lane >> 4) + j) * 32 + 16 * u + (lane & 15)];
    *(GAS bf16x8*)(ws + OFF_WKVF + (size_t)t * 16) = o;
  } else if (bid < 282) {
    const char* W = (const char*)(bid == 280 ? wk : wv);
    float a[8] = {0, 0, 0, 0, 0, 0, 0, 0};
    u32x4 v[8];
    UNROLL for (int i = 0; i < 8; i++) v[i] = *(const GAS u32x4*)(W + (wid * 8 + i) * 1024 + lane * 16);
    UNROLL for (int i = 0; i < 8; i++) UNROLL for (int j = 0; j < 4; j++) { a[2 * j] += bflo(v[i][j]); a[2 * j + 1] += bfhi(v[i][j]); }
    UNROLL for (int j = 0; j < 8; j++) { a[j] += __shfl_xor(a[j], 4); a[j] += __shfl_xor(a[j], 8); a[j] += __shfl_xor(a[j], 16); a[j] += __shfl_xor(a[j], 32); }
    LAS float* red = (LAS float*)lds;
    if (lane < 4) UNROLL for (int j = 0; j < 8; j++) red[wid * 32 + lane * 8 + j] = a[j];
    __syncthreads();
    if (tid < 32) {
      float t = 0.f;
      UNROLL for (int w = 0; w < 8; w++) t += red[w * 32 + tid];
      ((float*)(ws + OFF_CSKV))[(bid - 280) * 32 + tid] = t;
    }
  } else {
    *(GAS u32x4*)(ws + (bid - 282) * 8192 + tid * 16) = (u32x4){0u, 0u, 0u, 0u};
  }
}
constexpr int PREP_LDS = 65536 + 8192;

struct Ctx {
  char* ws; rsrc_t rs; lds_ptr lds; unsigned l0;
  int lane, wid, grp, idx, hi;
};
DI Ctx fresh(const Ctx& x) {
  Ctx y = x;
  asm volatile("" : "+s"(y.wid), "+s"(y.grp), "+s"(y.idx), "+v"(y.lane));
  y.hi = y.lane >> 5;
  return y;
}
constexpr int LDS_ALL = 163840;
constexpr int A_SLOT = 24576, A_ROW = 200, A_IMG = 106496, A_KVL = A_IMG + 256 * A_ROW, A_ZL = A_KVL + 4096;
static_assert(A_ZL + 128 <= LDS_ALL, "");
DI unsigned act_off(int i) { return OFF_ACT + i * 4 * MBy; }
DI unsigned flagx(int seam, int rt) { return OFF_FLAGX + (seam * 64 + rt) * 128; }

DI int a_head(const Ctx& x) { return (x.grp >> 1) * 8 + (x.idx >> 2); }
DI int a_tile(const Ctx& x) { return (x.grp & 1) * 4 + (x.idx & 3); }
DI int a_rot(const Ctx& x) { return (x.idx >> 3) * 2 + (x.idx & 3) * 3 + x.grp; }
DI unsigned touch(const char* p) { return *(const GAS unsigned*)p; }
DI void keep(unsigned v) { asm volatile("" ::"v"(v)); }
DI void dma_wqkv(const Ctx& x, int c, int pg) {
  const int cc = (c + a_rot(x)) & 7;
  gldsN<3>(x.ws + OFF_WTA + a_head(x) * 196608 + cc * A_SLOT + pg * 3072, x.lane * 16, x.l0 + (c % 6) * A_SLOT + pg * 3072);
}
DI int rot64(const Ctx& x) { return (x.idx >> 1) + x.grp; }
constexpr int w64_at(int which, int c) {
  constexpr int t[3][8] = {{48, 64, 80, 0, 16, 32, 96, 112}, {128, 144, 48, 64, 80, 0, 16, 32}, {32, 48, 64, 80, 96, 112, 128, 144}};
  return t[which][c] * 1024;
}
template <int WHICH, int N> DI void dma_w64(const Ctx& x, int c, int p) {
  const int cc = (c + rot64(x)) & 7;
  gldsN<N>(x.ws + OFF_WT64 + WHICH * 2 * MBy + (x.idx >> 1) * 131072 + cc * 16384 + p * 1024, x.lane * 16, x.l0 + w64_at(WHICH, c) + p * 1024);
}
DI float colsum(const Ctx& x, int col) {
  const GAS float* p = (const GAS float*)(x.ws + OFF_CS) + col;
  return ((p[0] + p[4096]) + (p[2 * 4096] + p[3 * 4096])) + ((p[4 * 4096] + p[5 * 4096]) + (p[6 * 4096] + p[7 * 4096]));
}

DI void phase_a(const Ctx& x, int L) {
  char* ws = x.ws;
  const int lane = x.lane, wid = x.wid, hi = x.hi, h = a_head(x), tt = a_tile(x), r = lane & 31, rt = tt * 8 + wid;
  const lds_ptr lds = x.lds;
  constexpr int SPC = 8, NSTEP = 64, PD = 16;
  const int rot = a_rot(x);
  STAMP(4 * L, 0);
  float csv[3];
  UNROLL for (int i = 0; i < 3; i++) csv[i] = colsum(x, i * 1024 + h * 32 + r);
  bf16x8 wph[2];
  UNROLL for (int s = 0; s < 2; s++) wph[s] = *(const GAS bf16x8*)(ws + OFF_WPHIF + (h * 2 + s) * 1024 + lane * 16);
  if (L) wait_flags(ws, flagx(4 * L, rt) + (lane & 15) * 4);
  STAMP(4 * L, 10);
  const unsigned abase = act_off(4 * L) + rt * 65536;
  auto aload = [&](int s) { return __builtin_bit_cast(bf16x8, ld16(x.rs, lane * 16 + (s & 3) * 1024, abase + (((s / SPC) + rot) & 7) * 8192 + (s & 4) * 1024)); };
  bf16x8 a[PD];
  UNROLL for (int s = 0; s < PD; s++) a[s] = aload(s);
  f32x16 acc[3] = {{}, {}, {}};
  const int q4 = (lane & 15) >> 2, cbp = ((lane >> 4) & 1) * 32 + (lane & 3) * 8;
  bf16x8 bf[2][3];
  auto bread = [&](int s) { UNROLL for (int i = 0; i < 3; i++) bf[s & 1][i] = lds_b128(lds + lane * 16 + ((s % 48) * 3 + i) * 1024); };
  u32x4 stv[4];
  unsigned tch[2];
  if (L == 0) UNROLL for (int c = 2; c < 4; c++) dma_wqkv(x, c, wid);
  UNROLL for (int c = 4; c < 6; c++) dma_wqkv(x, c, wid);
  if (L == 0) wait_vm_bar<PD + 12>(); else wait_vm_bar<PD + 6>();
  STAMP(4 * L, 1); STAMPC(4 * L, 14);
  bread(0);
  UNROLL for (int s = 0; s < NSTEP; s++) {
    if (s == 15) { wait_vm_bar<15>(); dma_wqkv(x, 6, wid); }
    if (s == 25) { bar_only(); dma_wqkv(x, 7, wid); dma_w64<0, 2>(x, 0, wid * 2); }
    if (s == 47) {
      asm volatile("s_waitcnt vmcnt(22) lgkmcnt(0)\n\ts_barrier" ::: "memory");
      dma_w64<0, 2>(x, 1, wid * 2); dma_w64<0, 2>(x, 2, wid * 2);
    }
    if (s == 48) {
      UNROLL for (int i = 0; i < 4; i++) stv[i] = ld16(x.rs, (hi * 4 + i) * 512 + r * 16, OFF_ST + 2 * L * 262144 + rt * 4096);
      tch[0] = touch(ws + OFF_WT64 + (x.idx * 8 + wid) * 8192 + lane * 128);
      tch[1] = touch(ws + OFF_WT64 + 2 * MBy + (x.idx * 8 + wid) * 8192 + lane * 128);
    }
    if (s + 1 < NSTEP) bread(s + 1);
    SB();
    const bf16x8 av = a[s % PD];
    UNROLL for (int i = 0; i < 3; i++) acc[i] = MFMA32(bf[s & 1][i], av, acc[i]);
    SB();
    if (s + PD < NSTEP) a[s % PD] = aload(s + PD);
    SB();
  }
  STAMP(4 * L, 2); STAMPC(4 * L, 15);
  float mean, rstd;
  stats_finish(stv, mean, rstd);
  {
    const bf16x8 mf = mean_frag(mean);
    acc[1] = MFMA32(cs_frag(csv[1], hi), mf, acc[1]);
    acc[2] = MFMA32(cs_frag(csv[2], hi), mf, acc[2]);
    acc[0] = MFMA32(cs_frag(csv[0], hi), mf, acc[0]);
  }
  const f32x16 zero = {};
  {
    UNROLL for (int i = 0; i < 16; i++) { acc[1][i] *= rstd; acc[2][i] *= rstd; }
    f32x16 p = MFMA32(wph[0], pack8(acc[1], 0), zero);
    p = MFMA32(wph[1], pack8(acc[1], 1), p);
    const lds_ptr row = lds + A_IMG + (wid * 32 + r) * A_ROW + hi * 8;
    UNROLL for (int g = 0; g < 4; g++) *(LAS u32x2*)(row + 128 + g * 16) = (u32x2){cvtpk(acc[2][4 * g], acc[2][4 * g + 1]), cvtpk(acc[2][4 * g + 2], acc[2][4 * g + 3])};
    f32x16 ep, em;
    dual_softmax(p, ep, em);
    UNROLL for (int g = 0; g < 4; g++) {
      *(LAS u32x2*)(row + g * 16) = (u32x2){cvtpk(ep[4 * g], ep[4 * g + 1]), cvtpk(ep[4 * g + 2], ep[4 * g + 3])};
      *(LAS u32x2*)(row + 64 + g * 16) = (u32x2){cvtpk(em[4 * g], em[4 * g + 1]), cvtpk(em[4 * g + 2], em[4 * g + 3])};
    }
  }
  STAMP(4 * L, 3);
  lgkm_bar();
  STAMP(4 * L, 4);
  const unsigned slab = OFF_SLAB + (L * 256 + h * 8) * SLAB_B, flag = OFF_FLAGA + L * 4096 + h * 128;
  if (wid < 4) {
    const int fh = wid & 1;
    const lds_ptr ip = lds + A_IMG + (8 * hi + q4) * A_ROW + cbp;
    if (wid < 2) {
      f32x16 kv = zero;
      UNROLL for (int u = 0; u < 16; u++) {
        const bf16x8 pf = lds_trfrag<4 * A_ROW>(ip + u * 16 * A_ROW + fh * 64), vf = lds_trfrag<4 * A_ROW>(ip + u * 16 * A_ROW + 128);
        kv = MFMA32(pf, vf, kv);
      }
      UNROLL for (int g = 0; g < 4; g++)
        st16(x.rs, (u32x4){__float_as_uint(kv[4 * g]), __float_as_uint(kv[4 * g + 1]), __float_as_uint(kv[4 * g + 2]), __float_as_uint(kv[4 * g + 3])},
             g * 1024 + lane * 16, slab + tt * SLAB_B + fh * 4096);
    } else {
      const bf16x8 sel = sel_frag(lane);
      f32x4 z = {};
      UNROLL for (int u = 0; u < 16; u++) z = MFMA16(sel, lds_trfrag<4 * A_ROW>(ip + u * 16 * A_ROW + fh * 64), z);
      if (lane < 32) st4(x.rs, __float_as_uint(z[0]), lane * 4, slab + tt * SLAB_B + 8192 + fh * 128);
    }
    publish(ws, flag + (tt * 4 + wid) * 4, lane);
  } else {
    UNROLL for (int c = 3; c < 6; c++) dma_w64<0, 4>(x, c, (wid - 4) * 4);
  }
  STAMP(4 * L, 5);
  bf16x8 phq[4];
  {
    UNROLL for (int i = 0; i < 16; i++) acc[0][i] *= rstd;
    f32x16 p = MFMA32(wph[0], pack8(acc[0], 0), zero);
    p = MFMA32(wph[1], pack8(acc[0], 1), p);
    f32x16 ep, em;
    dual_softmax(p, ep, em);
    phq[0] = pack8(ep, 0); phq[1] = pack8(ep, 1); phq[2] = pack8(em, 0); phq[3] = pack8(em, 1);
  }
  keep(tch[0]); keep(tch[1]);
  STAMP(4 * L, 6);
  wait_flags(ws, flag + (lane & 31) * 4);
  STAMP(4 * L, 7);
  {
    f32x4 v[8];
    UNROLL for (int t = 0; t < 8; t++) v[t] = __builtin_bit_cast(f32x4, ld16(x.rs, lane * 16, slab + t * SLAB_B + wid * 1024));
    float zs = 0.f;
    if (wid == 0) UNROLL for (int t = 0; t < 8; t++) zs += __uint_as_float(ld4(x.rs, lane * 4, slab + t * SLAB_B + 8192));
    const f32x4 s = ((v[0] + v[1]) + (v[2] + v[3])) + ((v[4] + v[5]) + (v[6] + v[7]));
    *(LAS u32x2*)(lds + A_KVL + (wid >> 1) * 1024 + lane * 16 + (wid & 1) * 8) = (u32x2){cvtpk(s[0], s[1]), cvtpk(s[2], s[3])};
    if (wid == 0) *(LAS u16*)(lds + A_ZL + lane * 2) = (u16)cvtpk(zs, zs);
  }
  lgkm_bar();
  STAMP(4 * L, 8);
  {
    f32x16 num = zero, den = zero;
    UNROLL for (int fs = 0; fs < 4; fs++) {
      const bf16x8 kf = lds_b128(lds + A_KVL + fs * 1024 + lane * 16);
      const s16x4 z0 = *(LAS s16x4*)(lds + A_ZL + fs * 32 + hi * 8), z1 = *(LAS s16x4*)(lds + A_ZL + fs * 32 + hi * 8 + 16);
      num = MFMA32(kf, phq[fs], num);
      den = MFMA32(__builtin_shufflevector(z0, z1, 0, 1, 2, 3, 4, 5, 6, 7), phq[fs], den);
    }
    const float rd = 1.f / (den[0] + 1e-6f);
    UNROLL for (int gp = 0; gp < 2; gp++) {
      float v[8];
      gather8(num, gp, v);
      st16(x.rs, (u32x4){cvtpk(v[0] * rd, v[1] * rd), cvtpk(v[2] * rd, v[3] * rd), cvtpk(v[4] * rd, v[5] * rd), cvtpk(v[6] * rd, v[7] * rd)},
           gp * 1024 + lane * 16, act_off(4 * L + 1) + rt * 65536 + h * 2048);
    }
  }
  publish(ws, flagx(4 * L + 1, rt) + h * 4, lane);
  STAMP(4 * L, 9);
}

constexpr int G_PD = 16;
template <int WHICH, class Hook>
DI void gemm64(const Ctx& x, unsigned abase, f32x16 (&acc)[2], Hook hook) {
  const int lane = x.lane, rot = rot64(x);
  auto aload = [&](int s) { return __builtin_bit_cast(bf16x8, ld16(x.rs, lane * 16 + (s & 3) * 1024, abase + (((s >> 3) + rot) & 7) * 8192 + (s & 4) * 1024)); };
  bf16x8 a[G_PD];
  UNROLL for (int s = 0; s < G_PD; s++) a[s] = aload(s);
  bf16x8 bf[3][2];
  auto bread = [&](int s) { UNROLL for (int n = 0; n < 2; n++) bf[s % 3][n] = lds_b128(x.lds + lane * 16 + w64_at(WHICH, s >> 3) + (s & 7) * 2048 + n * 1024); };
  hook(-1);
  bread(0); bread(1);
  UNROLL for (int s = 0; s < 64; s++) {
    hook(s);
    if (s + 2 < 64) bread(s + 2);
    SB();
    const bf16x8 av = a[s % G_PD];
    acc[0] = MFMA32(bf[s % 3][0], av, acc[0]);
    acc[1] = MFMA32(bf[s % 3][1], av, acc[1]);
    SB();
    if (s + G_PD < 64) a[s % G_PD] = aload(s + G_PD);
    SB();
  }
}

template <bool AFTER_B>
DI void out_compute(const Ctx& x, int L, u32x4 (&res)[4], char* out) {
  char* ws = x.ws;
  const int lane = x.lane, hi = x.hi, ph = 4 * L + (AFTER_B ? 3 : 1);
  const bool last = AFTER_B && L == 1;
  const int pn = x.idx >> 1, rt = (x.grp * 2 + (x.idx & 1)) * 4 + x.wid, r = lane & 31;
  STAMP(ph, 0);
  bar_only();
  wait_flags(ws, flagx(ph, rt) + (lane & 31) * 4);
  STAMP(ph, 3);
  f32x16 acc[2] = {{}, {}};
  gemm64<AFTER_B ? 2 : 0>(x, act_off(ph) + rt * 65536, acc, [&](int s) { if (s == 46) bar_only(); });
  STAMP(ph, 1);
  float ss = 0.f, sq = 0.f;
  UNROLL for (int i = 0; i < 4; i++) {
    float v[8];
    gather8(acc[i >> 1], i & 1, v);
    UNROLL for (int j = 0; j < 4; j++) res[i][j] = cvtpk(v[2 * j] + bflo(res[i][j]), v[2 * j + 1] + bfhi(res[i][j]));
    if (last) *(GAS u32x4*)(out + (size_t)(rt * 32 + r) * 2048 + pn * 128 + i * 32 + hi * 16) = res[i];
    else st16(x.rs, res[i], i * 1024 + lane * 16, act_off(ph + 1) + rt * 65536 + pn * 4096);
    UNROLL for (int j = 0; j < 4; j++) { ss = dot2(res[i][j], 0x3f803f80u, ss); sq = dot2(res[i][j], res[i][j], sq); }
  }
  if (!last) {
    ss = half_add(ss); sq = half_add(sq);
    if (hi == 0) st8(x.rs, (u32x2){__float_as_uint(ss), __float_as_uint(sq)}, r * 16, OFF_ST + ((ph + 1) >> 1) * 262144 + rt * 4096 + (pn >> 1) * 512 + (pn & 1) * 8);
  }
  bar_only();
  if (!last) publish(ws, flagx(ph + 1, rt) + pn * 4, lane);
  STAMP(ph, 2);
}
DI void phase_out_b(const Ctx& x, int L, u32x4 (&res)[4], char* out) {
  if (x.wid < 4) { out_compute<true>(x, L, res, out); return; }
  const int w4 = x.wid - 4;
  bar_only();
  if (L == 0) UNROLL for (int i = 0; i < 2; i++) dma_wqkv(x, 0, w4 * 2 + i);
  bar_only();
  if (L == 0) UNROLL for (int c = 1; c < 4; c++) UNROLL for (int i = 0; i < 2; i++) dma_wqkv(x, c, w4 * 2 + i);
  bar_only();
}

constexpr int B_QX = 65536, B_PART = 98304;
constexpr float QSCALE = 0.17677669529663687f * LOG2E;
DI void phase_out_a_b(const Ctx& x, int L, u32x4 (&res)[4]) {
  char* ws = x.ws;
  const lds_ptr lds = x.lds;
  const int lane = x.lane, wid = x.wid, hi = x.hi, g4 = lane >> 4, c16 = lane & 15, ph = 4 * L + 2;
  const int pm = x.grp * 2 + (x.idx & 1), pj = x.idx >> 1;
  const int rt = pm * 4 + (wid & 3), head = pj * 2 + (wid >> 2);
  const unsigned stb = OFF_ST + (2 * L + 1) * 262144, kvf = OFF_KVF + L * 262144, flagb = OFF_FLAGB + L * 16384;
  auto kv_dma = [&](int ch, int first) {
    gldsN_sc1<4>(ws + kvf + ((ch + x.grp) & 7) * 32768 + first * 1024, lane * 16, x.l0 + ((ch + 3) % 5) * 32768 + first * 1024);
  };
  auto kv_all = [&]() {
    const u32x4 v = ld16(x.rs, lane * 16, flagb + x.grp * 2048) & ld16(x.rs, lane * 16 + 1024, flagb + x.grp * 2048);
    return __all((v[0] & v[1] & v[2] & v[3]) == 1u);
  };
  bool kv_ok = true;
  bf16x8 qf[2];
  if (wid < 4) {
    out_compute<false>(x, L, res, nullptr);
    STAMP(ph, 0);
    float csv[2];
    UNROLL for (int n = 0; n < 2; n++) csv[n] = colsum(x, 3072 + pj * 64 + n * 32 + (lane & 31));
    bar_only();
    wait_flags(ws, flagx(ph, rt) + c16 * 4);
    STAMP(ph, 1);
    f32x16 acc[2] = {{}, {}};
    u32x4 stv[4];
    gemm64<1>(x, act_off(ph) + rt * 65536, acc, [&](int s) {
      if (s == 48) UNROLL for (int i = 0; i < 4; i++) stv[i] = ld16(x.rs, (hi * 4 + i) * 512 + (lane & 31) * 16, stb + rt * 4096);
    });
    STAMP(ph, 2);
    float mean, rstd;
    stats_finish(stv, mean, rstd);
    const bf16x8 mf = mean_frag(mean);
    UNROLL for (int n = 0; n < 2; n++) acc[n] = MFMA32(cs_frag(csv[n], hi), mf, acc[n]);
    const float sc = rstd * QSCALE;
    UNROLL for (int i = 0; i < 16; i++) { acc[0][i] *= sc; acc[1][i] *= sc; }
    qf[0] = pack8(acc[0], 0); qf[1] = pack8(acc[0], 1);
    bar_only();
    UNROLL for (int s = 0; s < 2; s++) *(LAS bf16x8*)(lds + B_QX + (wid * 2 + s) * 1024 + lane * 16) = pack8(acc[1], s);
    lgkm_bar();
  } else {
    const int w4 = wid - 4, krt = pm * 4 + (pj >> 2), kth = (pj >> 1) & 1, ktype = pj & 1, u = wid & 1, kh = wid < 6;
    UNROLL for (int c = 6; c < 8; c++) dma_w64<0, 4>(x, c, w4 * 4);
    bar_only();
    UNROLL for (int c = 0; c < 2; c++) dma_w64<1, 4>(x, c, w4 * 4);
    bf16x8 kx[16], kw[16];
    UNROLL for (int i = 0; i < 16; i++) kw[i] = *(const GAS bf16x8*)(ws + OFF_WKVF + ((ktype * 32 + kh * 16 + i) * 2 + u) * 1024 + lane * 16);
    f32x4 cs;
    {
      const GAS float* CS = (const GAS float*)(ws + OFF_CSKV);
      if (ktype == 0) cs = *(const GAS f32x4*)(CS + 16 * u + 4 * g4);
      else { const float c = CS[32 + 16 * u + c16]; cs = (f32x4){c, c, c, c}; }
    }
    wait_vm_bar<0>();
    UNROLL for (int c = 2; c < 7; c++) dma_w64<1, 4>(x, c, w4 * 4);
    bar_only();
    dma_w64<1, 4>(x, 7, w4 * 4);
    volatile LAS unsigned* pflag = (volatile LAS unsigned*)(lds + B_PART + 2048 + u * 64);
    if (!kh) *pflag = 0u;
    STAMP(ph, 0);
    asm volatile("s_waitcnt vmcnt(0) lgkmcnt(0)\n\ts_barrier" ::: "memory");
    __builtin_amdgcn_s_setprio(2);
    wait_flags(ws, flagx(ph, krt) + c16 * 4);
    UNROLL for (int i = 0; i < 16; i++)
      kx[i] = __builtin_bit_cast(bf16x8, ld16(x.rs, (g4 >> 1) * 1024 + (16 * kth + c16 + 32 * (g4 & 1)) * 16 + (i & 1) * 2048,
                                              act_off(ph) + krt * 65536 + kh * 32768 + (i >> 1) * 4096));
    u32x4 sv[2];
    UNROLL for (int i = 0; i < 2; i++) sv[i] = ld16(x.rs, (g4 * 2 + i) * 512 + (16 * kth + c16) * 16, stb + krt * 4096);
    f32x4 d[2] = {{}, {}};
    if (ktype == 0) { UNROLL for (int i = 0; i < 16; i++) d[i & 1] = MFMA16(kw[i], kx[i], d[i & 1]); }
    else { UNROLL for (int i = 0; i < 16; i++) d[i & 1] = MFMA16(kx[i], kw[i], d[i & 1]); }
    d[0] += d[1];
    if (kh) {
      *(LAS f32x4*)(lds + B_PART + u * 1024 + lane * 16) = d[0];
      asm volatile("s_waitcnt lgkmcnt(0)" ::: "memory");
      *pflag = 1u;
    } else {
      float s = (__uint_as_float(sv[0][0]) + __uint_as_float(sv[0][2])) + (__uint_as_float(sv[1][0]) + __uint_as_float(sv[1][2]));
      float q = (__uint_as_float(sv[0][1]) + __uint_as_float(sv[0][3])) + (__uint_as_float(sv[1][1]) + __uint_as_float(sv[1][3]));
      s += __shfl_xor(s, 16); q += __shfl_xor(q, 16);
      s = half_add(s); q = half_add(q);
      const float mean = s * (1.f / 1024.f);
      const float rstd = __builtin_amdgcn_rsqf(fmaxf(q * (1.f / 1024.f) - mean * mean, 0.f) + 1e-5f);
      f32x4 m4, r4;
      UNROLL for (int j = 0; j < 4; j++) {
        const float mj = __shfl(mean, 4 * g4 + j), rj = __shfl(rstd, 4 * g4 + j);
        m4[j] = ktype ? mj : mean; r4[j] = ktype ? rj : rstd;
      }
      for (unsigned spins = 0; spins < SPIN_CAP * 16 && *pflag != 1u; spins++) {}
      asm volatile("" ::: "memory");
      const f32x4 e = (d[0] + *(LAS f32x4*)(lds + B_PART + u * 1024 + lane * 16) - m4 * cs) * r4;
      const int frag = ktype ? 2 + kth : u, lp = (ktype ? 16 * u : 16 * kth) + c16 + 32 * (g4 & 1);
      st8(x.rs, (u32x2){cvtpk(e[0], e[1]), cvtpk(e[2], e[3])}, frag * 1024 + lp * 16 + (g4 >> 1) * 8, kvf + krt * 4096);
      drain_vm();
      if (lane < 8) __hip_atomic_store((GAS unsigned*)(ws + flagb + lane * 2048 + x.grp * 256 + (x.idx * 2 + u) * 4), 1u, RLX_AGENT);
      STAMP(ph, 1);
      if (wid == 7) {
        wait_flags(ws, flagb + x.grp * 2048 + x.grp * 256 + lane * 4);
        STAMP(ph, 2);
        UNROLL for (int i = 0; i < 8; i++) kv_dma(0, i * 4);
        kv_ok = kv_all();
        drain_vm();
        STAMP(ph, 7);
      }
    }
    __builtin_amdgcn_s_setprio(0);
    bar_only();
    bar_only();
    UNROLL for (int s = 0; s < 2; s++) qf[s] = lds_b128(lds + B_QX + ((wid & 3) * 2 + s) * 1024 + lane * 16);
  }
  STAMP(ph, 3); STAMPC(ph, 14);
  auto kv_next2 = [&]() { UNROLL for (int i = 0; i < 16; i++) kv_dma(1 + (i >> 3), (i & 7) * 4); };
  if (wid == 7 && kv_ok) kv_next2();
  const bf16x8 sel = sel_frag(lane);
  const f32x16 zero = {};
  f32x16 O = zero, negm, SA, SB_;
  f32x4 LS = {};
  bf16x8 kf[2], vf[2];
  lds_ptr cur = lds + 3 * 32768 + lane * 16;
  kf[0] = lds_b128(cur); kf[1] = lds_b128(cur + 1024);
  SA = MFMA32(kf[0], qf[0], zero); SA = MFMA32(kf[1], qf[1], SA);
  kf[0] = lds_b128(cur + 4096); kf[1] = lds_b128(cur + 4096 + 1024);
  vf[0] = lds_b128(cur + 2048); vf[1] = lds_b128(cur + 3072);
  {
    float m = SA[0];
    UNROLL for (int i = 1; i < 16; i++) m = fmaxf(m, SA[i]);
    m = half_max(m);
    UNROLL for (int i = 0; i < 16; i++) { negm[i] = -m; SA[i] -= m; }
  }
#define EX(i) Sc[i] = __builtin_amdgcn_exp2f(Sc[i])
#define PIN(x) asm volatile("" : "+v"(x))
  u32x4 p0, p1 = {0u, 0u, 0u, 0u};
  auto step = [&](f32x16& Sc, f32x16& Sn, lds_ptr kp, lds_ptr vc, lds_ptr vp) {
    SB();
    Sn = MFMA32(kf[0], qf[0], negm);
    SB();
    kf[0] = lds_b128(kp);
    EX(0); EX(1); EX(2); PIN(Sc);
    SB();
    O = MFMA32(vf[1], __builtin_bit_cast(bf16x8, p1), O);
    SB();
    vf[1] = lds_b128(vc + 3072);
    EX(3); EX(4); EX(5); PIN(Sc);
    SB();
    Sn = MFMA32(kf[1], qf[1], Sn);
    SB();
    kf[1] = lds_b128(kp + 1024);
    EX(6); EX(7);
    p0[0] = cvtpk(Sc[0], Sc[1]); p0[1] = cvtpk(Sc[2], Sc[3]); p0[2] = cvtpk(Sc[4], Sc[5]); p0[3] = cvtpk(Sc[6], Sc[7]); PIN(p0);
    SB();
    LS = MFMA16(sel, __builtin_bit_cast(bf16x8, p1), LS);
    SB();
    EX(8); EX(9); PIN(Sc);
    SB();
    O = MFMA32(vf[0], __builtin_bit_cast(bf16x8, p0), O);
    SB();
    vf[0] = lds_b128(vp + 2048);
    EX(10); EX(11); EX(12); PIN(Sc);
    SB();
    LS = MFMA16(sel, __builtin_bit_cast(bf16x8, p0), LS);
    SB();
    EX(13); EX(14); EX(15);
    p1[0] = cvtpk(Sc[8], Sc[9]); p1[1] = cvtpk(Sc[10], Sc[11]); p1[2] = cvtpk(Sc[12], Sc[13]); p1[3] = cvtpk(Sc[14], Sc[15]); PIN(p1);
    SB();
  };
  for (int ch = 0; ch < 8; ch++) {
    const lds_ptr nxt = lds + ((ch + 4) % 5) * 32768 + lane * 16;
    UNROLL for (int t = 0; t < 8; t += 2) {
      if (t == 2 && !kv_ok) {
        for (unsigned spins = 0; spins < SPIN_CAP && !kv_all(); spins++) asm volatile("s_sleep 4" ::: "memory");
        kv_next2();
        kv_ok = true;
      }
      if (t == 6) {
        if (ch == 0) wait_vm_bar<32>(); else if (ch < 6) wait_vm_bar<4>(); else wait_vm_bar<8>();
        if (ch == 4 && wid < 6)
          glds4(ws + OFF_WTA + (x.grp >> 1) * 8 * 196608 + (x.idx * 6 + wid) * 8192, lane * 128, x.l0 + 32768 + wid * 256);
        if (ch + 3 < 8) kv_dma(ch + 3, wid * 4);
        else if (ch == 5) { UNROLL for (int c = 0; c < 4; c++) dma_w64<2, 2>(x, c, wid * 2); }
        else if (ch == 6) { UNROLL for (int c = 4; c < 6; c++) dma_w64<2, 2>(x, c, wid * 2); }
        else { UNROLL for (int c = 6; c < 8; c++) dma_w64<2, 2>(x, c, wid * 2); }
      }
      step(SA, SB_, t + 2 < 8 ? cur + (t + 2) * 4096 : nxt, cur + t * 4096, cur + (t + 1) * 4096);
      step(SB_, SA, t + 3 < 8 ? cur + (t + 3) * 4096 : nxt + 4096, cur + (t + 1) * 4096, t + 2 < 8 ? cur + (t + 2) * 4096 : nxt);
    }
    cur = nxt;
  }
  O = MFMA32(vf[1], __builtin_bit_cast(bf16x8, p1), O);
  LS = MFMA16(sel, __builtin_bit_cast(bf16x8, p1), LS);
  STAMP(ph, 4); STAMPC(ph, 15);
  float l = LS[0];
  {
    float chk = l;
    UNROLL for (int i = 0; i < 16; i++) chk = fmaxf(chk, fabsf(O[i]) * 0x1p-27f);
    bool bad = !(chk < 0x1p100f);
    UNROLL for (int i = 0; i < 16; i++) bad |= (O[i] != O[i]);
    if (__any(bad)) {
      O = zero; l = 0.f;
      float m = -__builtin_inff();
      for (int t = 0; t < 64; t++) {
        bf16x8 f[4];
        UNROLL for (int i = 0; i < 4; i++) f[i] = __builtin_bit_cast(bf16x8, ld16(x.rs, i * 1024 + lane * 16, kvf + t * 4096));
        f32x16 s = MFMA32(f[0], qf[0], zero);
        s = MFMA32(f[1], qf[1], s);
        float mt = s[0];
        UNROLL for (int i = 1; i < 16; i++) mt = fmaxf(mt, s[i]);
        const float mn = fmaxf(m, half_max(mt));
        const float al = __builtin_amdgcn_exp2f(m - mn);
        m = mn;
        UNROLL for (int i = 0; i < 16; i++) { s[i] = __builtin_amdgcn_exp2f(s[i] - mn); O[i] *= al; }
        const bf16x8 p0 = pack8(s, 0), p1 = pack8(s, 1);
        f32x4 lt = MFMA16(sel, p0, ((f32x4){0.f, 0.f, 0.f, 0.f}));
        lt = MFMA16(sel, p1, lt);
        l = l * al + lt[0];
        O = MFMA32(f[2], p0, O);
        O = MFMA32(f[3], p1, O);
      }
    }
  }
  const float rl = 1.f / l;
  UNROLL for (int gp = 0; gp < 2; gp++) {
    float v[8];
    gather8(O, gp, v);
    st16(x.rs, (u32x4){cvtpk(v[0] * rl, v[1] * rl), cvtpk(v[2] * rl, v[3] * rl), cvtpk(v[4] * rl, v[5] * rl), cvtpk(v[6] * rl, v[7] * rl)},
         gp * 1024 + lane * 16, act_off(ph + 1) + rt * 65536 + head * 2048);
  }
  publish(ws, flagx(ph + 1, rt) + head * 4, lane);
  asm volatile("s_waitcnt lgkmcnt(0)" ::: "memory");
  STAMP(ph, 5);
}

__global__ __launch_bounds__(512, 2) void k_main(char* ws, char* out) {
  extern __shared__ __attribute__((aligned(16))) char lds_raw[];
  Ctx x;
  x.ws = ws;
  x.rs = __builtin_amdgcn_make_buffer_rsrc(ws, 0, WS_USED, 0x00020000);
  x.lds = (lds_ptr)lds_raw;
  x.l0 = (unsigned)(uintptr_t)x.lds;
  x.lane = threadIdx.x & 63;
  x.wid = __builtin_amdgcn_readfirstlane(threadIdx.x >> 6);
  x.grp = blockIdx.x & 7;
  x.idx = blockIdx.x >> 3;
  x.hi = x.lane >> 5;
  UNROLL for (int c = 0; c < 2; c++) dma_wqkv(x, c, x.wid);
  u32x4 res[4];
  UNROLL for (int i = 0; i < 4; i++)
    res[i] = ld16(x.rs, i * 1024 + x.lane * 16, act_off(0) + ((x.grp * 2 + (x.idx & 1)) * 4 + (x.wid & 3)) * 65536 + (x.idx >> 1) * 4096);
  for (int L = 0; L < 2; L++) {
    phase_a(fresh(x), L);
    phase_out_a_b(fresh(x), L, res);
    phase_out_b(fresh(x), L, res, out);
  }
}

extern "C" void kernel_launch(void* const* d_in, const int* in_sizes, int n_in, void* d_out, int out_size, void* d_ws, size_t ws_size,
                              hipStream_t stream) {
  char* ws = (char*)d_ws;
  (void)hipFuncSetAttribute((const void*)k_main, hipFuncAttributeMaxDynamicSharedMemorySize, LDS_ALL);
  (void)hipFuncSetAttribute((const void*)k_prep, hipFuncAttributeMaxDynamicSharedMemorySize, PREP_LDS);
  k_prep<<<296, 512, PREP_LDS, stream>>>((const char*)d_in[0], (const char*)d_in[1], (const u16*)d_in[2], (const char*)d_in[3], (const char*)d_in[5],
                                         (const u16*)d_in[6], (const u16*)d_in[7], (const char*)d_in[8], ws);
  k_main<<<256, 512, LDS_ALL, stream>>>(ws, (char*)d_out);
}
```

```cpp
#include <hip/hip_runtime.h>
#include <hip/hip_bf16.h>
#include <stdint.h>

typedef unsigned short u16;
using bf16x8 = __attribute__((ext_vector_type(8))) short;
using s16x4 = __attribute__((ext_vector_type(4))) short;
using f32x16 = __attribute__((ext_vector_type(16))) float;
using f32x4 = __attribute__((ext_vector_type(4))) float;
using f32x2 = __attribute__((ext_vector_type(2))) float;
using u32x4 = __attribute__((ext_vector_type(4))) unsigned;
using u32x2 = __attribute__((ext_vector_type(2))) unsigned;
typedef __bf16 bf16x2_t __attribute__((ext_vector_type(2)));
#define LAS __attribute__((address_space(3)))
#define GAS __attribute__((address_space(1)))
typedef LAS char* lds_ptr;
typedef short v4i16_t __attribute__((ext_vector_type(4)));
#define DI __device__ __forceinline__
#define MFMA32(a, b, c) __builtin_amdgcn_mfma_f32_32x32x16_bf16((a), (b), (c), 0, 0, 0)
#define MFMA16(a, b, c) __builtin_amdgcn_mfma_f32_16x16x32_bf16((a), (b), (c), 0, 0, 0)
#define SB() __builtin_amdgcn_sched_barrier(0)
#define RLX_AGENT __ATOMIC_RELAXED, __HIP_MEMORY_SCOPE_AGENT
#define UNROLL _Pragma("unroll")
#define AI __attribute__((always_inline))

constexpr float LOG2E = 1.4426950408889634f;
constexpr unsigned SPIN_CAP = 1u << 20;

constexpr unsigned MBy = 1u << 20;
constexpr unsigned OFF_FL = 768 * 1024;
constexpr unsigned OFF_CSKV = 120 * 1024;
constexpr unsigned OFF_CS = 512 * 1024;
constexpr unsigned OFF_WKVF = 128 * 1024;
constexpr unsigned OFF_WPHIF = 256 * 1024;
constexpr unsigned OFF_ST = 1 * MBy;
constexpr unsigned OFF_KVF = 2 * MBy;
constexpr unsigned OFF_SLAB = 4 * MBy;
constexpr unsigned SLAB_B = 8448;
constexpr unsigned OFF_ACT = 16 * MBy;
constexpr unsigned OFF_WTA = 48 * MBy;
constexpr unsigned OFF_WT64 = 54 * MBy;
constexpr unsigned WS_USED = 60 * MBy;

#define STAMP(k, i)
#define STAMPC(k, i)
DI void glds16(const void* sbase, unsigned voff, unsigned lds_base) {
  unsigned sv;
  asm volatile("s_mov_b32 %0, m0\n\ts_mov_b32 m0, %3\n\ts_nop 0\n\tglobal_load_lds_dwordx4 %1, %2\n\ts_mov_b32 m0, %0"
               : "=&s"(sv) : "v"(voff), "s"(sbase), "s"(lds_base) : "memory");
}
DI void glds4(const void* sbase, unsigned voff, unsigned lds_base) {
  unsigned sv;
  asm volatile("s_mov_b32 %0, m0\n\ts_mov_b32 m0, %3\n\ts_nop 0\n\tglobal_load_lds_dword %1, %2\n\ts_mov_b32 m0, %0"
               : "=&s"(sv) : "v"(voff), "s"(sbase), "s"(lds_base) : "memory");
}
#define GL_(o, sc) "global_load_lds_dwordx4 %1, %2 offset:" #o sc "\n\t"
#define GLDS_N(NAME, SC)                                                                                                   \
  template <int N> DI void NAME(const void* sbase, unsigned voff, unsigned lds_base) {                                     \
    unsigned sv;                                                                                                           \
    if constexpr (N == 1)                                                                                                  \
      asm volatile("s_mov_b32 %0, m0\n\ts_mov_b32 m0, %3\n\ts_nop 0\n\t" GL_(0, SC) "s_mov_b32 m0, %0"                    \
                   : "=&s"(sv) : "v"(voff), "s"(sbase), "s"(lds_base) : "memory");                                         \
    else if constexpr (N == 2)                                                                                                  \
      asm volatile("s_mov_b32 %0, m0\n\ts_mov_b32 m0, %3\n\ts_nop 0\n\t" GL_(0, SC) GL_(1024, SC) "s_mov_b32 m0, %0"       \
                   : "=&s"(sv) : "v"(voff), "s"(sbase), "s"(lds_base) : "memory");                                         \
    else if constexpr (N == 3)                                                                                             \
      asm volatile("s_mov_b32 %0, m0\n\ts_mov_b32 m0, %3\n\ts_nop 0\n\t" GL_(0, SC) GL_(1024, SC) GL_(2048, SC) "s_mov_b32 m0, %0" \
                   : "=&s"(sv) : "v"(voff), "s"(sbase), "s"(lds_base) : "memory");                                         \
    else                                                                                                                   \
      asm volatile("s_mov_b32 %0, m0\n\ts_mov_b32 m0, %3\n\ts_nop 0\n\t" GL_(0, SC) GL_(1024, SC) GL_(2048, SC) GL_(3072, SC) "s_mov_b32 m0, %0" \
                   : "=&s"(sv) : "v"(voff), "s"(sbase), "s"(lds_base) : "memory");                                         \
  }
GLDS_N(gldsN, "")
GLDS_N(gldsN_sc1, " sc1")
DI void glds16_sc1(const void* sbase, unsigned voff, unsigned lds_base) {
  unsigned sv;
  asm volatile("s_mov_b32 %0, m0\n\ts_mov_b32 m0, %3\n\ts_nop 0\n\tglobal_load_lds_dwordx4 %1, %2 sc1\n\ts_mov_b32 m0, %0"
               : "=&s"(sv) : "v"(voff), "s"(sbase), "s"(lds_base) : "memory");
}
template <int N> DI void wait_vm_bar() { asm volatile("s_waitcnt vmcnt(%0)\n\ts_barrier" ::"n"(N) : "memory"); }
DI void bar_only() { asm volatile("s_barrier" ::: "memory"); }
DI void lgkm_bar() { asm volatile("s_waitcnt lgkmcnt(0)\n\ts_barrier" ::: "memory"); }
DI void drain_vm() { asm volatile("s_waitcnt vmcnt(0)" ::: "memory"); }
DI s16x4 lds_tr(lds_ptr p) { return __builtin_bit_cast(s16x4, __builtin_amdgcn_ds_read_tr16_b64_v4i16((LAS v4i16_t*)p)); }
template <int E> DI bf16x8 lds_trfrag(lds_ptr p) { s16x4 lo = lds_tr(p), hi = lds_tr(p + E); return __builtin_shufflevector(lo, hi, 0, 1, 2, 3, 4, 5, 6, 7); }
DI bf16x8 lds_b128(lds_ptr p) { return *(LAS bf16x8*)p; }
DI unsigned cvtpk(float lo, float hi) {
  bf16x2_t v = __builtin_convertvector((f32x2){lo, hi}, bf16x2_t);
  return __builtin_bit_cast(unsigned, v);
}
DI float bflo(unsigned u) { return __uint_as_float(u << 16); }
DI float bfhi(unsigned u) { return __uint_as_float(u & 0xffff0000u); }
DI float dot2(unsigned a, unsigned b, float c) {
  return __builtin_amdgcn_fdot2_f32_bf16(__builtin_bit_cast(bf16x2_t, a), __builtin_bit_cast(bf16x2_t, b), c, false);
}
DI float half_add(float x) { auto r = __builtin_amdgcn_permlane32_swap(__float_as_uint(x), __float_as_uint(x), false, false); return __uint_as_float(r[0]) + __uint_as_float(r[1]); }
DI float half_max(float x) { auto r = __builtin_amdgcn_permlane32_swap(__float_as_uint(x), __float_as_uint(x), false, false); return fmaxf(__uint_as_float(r[0]), __uint_as_float(r[1])); }
DI float half_min(float x) { auto r = __builtin_amdgcn_permlane32_swap(__float_as_uint(x), __float_as_uint(x), false, false); return fminf(__uint_as_float(r[0]), __uint_as_float(r[1])); }
DI bf16x8 pack8(const f32x16& x, int s) {
  u32x4 p = {cvtpk(x[8 * s], x[8 * s + 1]), cvtpk(x[8 * s + 2], x[8 * s + 3]), cvtpk(x[8 * s + 4], x[8 * s + 5]), cvtpk(x[8 * s + 6], x[8 * s + 7])};
  return __builtin_bit_cast(bf16x8, p);
}
DI void gather8(const f32x16& a, int gp, float (&v)[8]) {
  UNROLL for (int j = 0; j < 4; j++) {
    auto s = __builtin_amdgcn_permlane32_swap(__float_as_uint(a[8 * gp + j]), __float_as_uint(a[8 * gp + 4 + j]), false, false);
    v[j] = __uint_as_float(s[0]); v[4 + j] = __uint_as_float(s[1]);
  }
}
typedef __amdgpu_buffer_rsrc_t rsrc_t;
DI u32x4 ld16(rsrc_t rs, unsigned voff, unsigned soff) { return __builtin_amdgcn_raw_buffer_load_b128(rs, voff, soff, 16); }
DI unsigned ld4(rsrc_t rs, unsigned voff, unsigned soff) { return __builtin_amdgcn_raw_buffer_load_b32(rs, voff, soff, 16); }
DI void st16(rsrc_t rs, u32x4 v, unsigned voff, unsigned soff) { __builtin_amdgcn_raw_buffer_store_b128(v, rs, voff + soff, 0, 16); }
DI void st8(rsrc_t rs, u32x2 v, unsigned voff, unsigned soff) { __builtin_amdgcn_raw_buffer_store_b64(v, rs, voff, soff, 16); }
DI void st4(rsrc_t rs, unsigned v, unsigned voff, unsigned soff) { __builtin_amdgcn_raw_buffer_store_b32(v, rs, voff, soff, 16); }
DI unsigned flag_at(int slot, int id) { return OFF_FL + (id >> 2) * 4096 + slot * 128 + (id & 3) * 4; }
DI void set_flag(char* ws, int slot, int id, int lane) {
  if (lane == 0) __hip_atomic_store((GAS unsigned*)(ws + flag_at(slot, id)), 1u, RLX_AGENT);
}
DI void wait_flags(char* ws, int slot, int id) {
  const unsigned off = flag_at(slot, id);
  for (unsigned spins = 0; spins < SPIN_CAP; spins++) {
    const unsigned v = __hip_atomic_load((GAS unsigned*)(ws + off), RLX_AGENT);
    if (__all(v == 1u)) break;
    __builtin_amdgcn_s_sleep(2);
  }
  asm volatile("" ::: "memory");
}
DI bool last_arriver(lds_ptr cnt, unsigned n, int lane) {
  drain_vm();
  unsigned old = 0;
  if (lane == 0) old = __hip_atomic_fetch_add((LAS unsigned*)cnt, 1u, __ATOMIC_RELAXED, __HIP_MEMORY_SCOPE_WORKGROUP);
  return (unsigned)__builtin_amdgcn_readfirstlane(old) == n - 1;
}
DI void stats_load(rsrc_t rs, unsigned base, int lane, u32x2 (&v)[8]) {
  UNROLL for (int i = 0; i < 8; i++) v[i] = __builtin_amdgcn_raw_buffer_load_b64(rs, ((lane >> 5) * 8 + i) * 256 + (lane & 31) * 8, base, 16);
}
DI u32x4 stats_pair(float s, float q) {
  return (u32x4){__float_as_uint(s), __float_as_uint(q), (unsigned)__builtin_amdgcn_mov_dpp(__float_as_int(s), 0xB1, 0xf, 0xf, true),
                 (unsigned)__builtin_amdgcn_mov_dpp(__float_as_int(q), 0xB1, 0xf, 0xf, true)};
}
DI void stats_finish(const u32x2 (&v)[8], float& mean, float& rstd) {
  float s = 0.f, q = 0.f;
  UNROLL for (int i = 0; i < 8; i++) { s += __uint_as_float(v[i][0]); q += __uint_as_float(v[i][1]); }
  s = half_add(s); q = half_add(q);
  mean = s * (1.f / 1024.f);
  rstd = __builtin_amdgcn_rsqf(fmaxf(q * (1.f / 1024.f) - mean * mean, 0.f) + 1e-5f);
}
DI void split3(float x, unsigned& a, unsigned& b, unsigned& c) {
  a = cvtpk(x, x) & 0xffffu; x -= bflo(a);
  b = cvtpk(x, x) & 0xffffu; x -= bflo(b);
  c = cvtpk(x, x) & 0xffffu;
}
DI bf16x8 cs_frag(float cs, int hi) {
  unsigned a, b, c; split3(cs, a, b, c);
  u32x4 p = {a | (b << 16), a | (c << 16), b | (a << 16), 0u};
  if (hi) p = (u32x4){0u, 0u, 0u, 0u};
  return __builtin_bit_cast(bf16x8, p);
}
DI bf16x8 mean_frag(float mean) {
  unsigned a, b, c; split3(-mean, a, b, c);
  u32x4 p = {a | (a << 16), b | (a << 16), b | (c << 16), 0u};
  return __builtin_bit_cast(bf16x8, p);
}
constexpr int ramp(int s, int I0, int PD) { int n = I0 + 2 * (s + 1); if (n > s + 1 + PD) n = s + 1 + PD; return n > 64 ? 64 : n; }
template <int V> struct IC { static constexpr int value = V; };
template <int N, int I = 0, class F> DI void static_for(F&& f) {
  if constexpr (I < N) { f(IC<I>{}); static_for<N, I + 1>(f); }
}
DI void dual_softmax(const f32x16& p, f32x16& ep, f32x16& em) {
  float mx = p[0], mn = p[0];
  UNROLL for (int i = 1; i < 16; i++) { mx = fmaxf(mx, p[i]); mn = fminf(mn, p[i]); }
  mx = half_max(mx); mn = half_min(mn);
  const float a = -mx * LOG2E, b = mn * LOG2E;
  float sp = 0.f, sm = 0.f;
  UNROLL for (int i = 0; i < 16; i++) {
    ep[i] = __builtin_amdgcn_exp2f(__builtin_fmaf(p[i], LOG2E, a));
    em[i] = __builtin_amdgcn_exp2f(__builtin_fmaf(p[i], -LOG2E, b));
    sp += ep[i]; sm += em[i];
  }
  sp = __builtin_amdgcn_rcpf(half_add(sp)); sm = __builtin_amdgcn_rcpf(half_add(sm));
  UNROLL for (int i = 0; i < 16; i++) { ep[i] *= sp; em[i] *= sm; }
}
DI bf16x8 sel_frag(int lane) {
  short v = ((lane & 3) == 0 && ((lane >> 2) & 1) == ((lane >> 4) & 1)) ? (short)0x3f80 : (short)0;
  return (bf16x8){v, v, v, v, v, v, v, v};
}

__global__ __launch_bounds__(512) void k_prep(const char* __restrict__ x0, const char* __restrict__ wqkv, const u16* __restrict__ wphi,
                                              const char* __restrict__ woA, const char* __restrict__ wq, const u16* __restrict__ wk,
                                              const u16* __restrict__ wv, const char* __restrict__ woB, char* __restrict__ ws) {
  extern __shared__ __attribute__((aligned(16))) char lds_raw[];
  const lds_ptr lds = (lds_ptr)lds_raw;
  const int tid = threadIdx.x, lane = tid & 63, wid = __builtin_amdgcn_readfirstlane(tid >> 6), bid = blockIdx.x;
  const int r = lane & 31, hi = lane >> 5;
  if (bid < 192) {
    int kb, cb, ld, which;
    const char* W;
    if (bid < 96) { const int t = bid >> 3, g = bid & 7; cb = (t % 3) * 4 + (g >> 1); kb = (t / 3) * 2 + (g & 1); ld = 6144; W = wqkv; which = -1; }
    else { const int t = bid - 96; which = t >> 5; kb = (t >> 2) & 7; cb = t & 3; ld = 2048; W = which == 0 ? woA : (which == 1 ? wq : woB); }
    const unsigned l0 = (unsigned)(uintptr_t)lds + wid * 8192;
    UNROLL for (int i = 0; i < 8; i++) glds16(W + (size_t)(kb * 128 + wid * 16 + i * 2) * ld + cb * 512, hi * ld + r * 16, l0 + i * 1024);
    drain_vm();
    const int step = kb * 8 + wid;
    const lds_ptr p = lds + wid * 8192 + (8 * hi + ((lane & 15) >> 2)) * 512 + ((lane >> 4) & 1) * 32 + (lane & 3) * 8;
    float cs[8];
    UNROLL for (int j = 0; j < 8; j++) {
      const u32x4 fr = __builtin_bit_cast(u32x4, lds_trfrag<2048>(p + j * 64));
      unsigned dst;
      if (which < 0) dst = OFF_WTA + (((cb & 3) * 8 + j) * 192 + step * 3 + (cb >> 2)) * 1024;
      else dst = OFF_WT64 + which * 2 * MBy + ((cb * 4 + (j >> 1)) * 128 + step * 2 + (j & 1)) * 1024;
      *(GAS u32x4*)(ws + dst + lane * 16) = fr;
      float c = 0.f;
      UNROLL for (int e = 0; e < 4; e++) c = dot2(fr[e], 0x3f803f80u, c);
      cs[j] = half_add(c);
    }
    if (which < 0 || which == 1) {
      LAS float* red = (LAS float*)(lds + 65536);
      if (hi == 0) UNROLL for (int j = 0; j < 8; j++) red[wid * 256 + j * 32 + r] = cs[j];
      __syncthreads();
      if (tid < 256) {
        float t = 0.f;
        UNROLL for (int w = 0; w < 8; w++) t += red[w * 256 + tid];
        ((float*)(ws + OFF_CS))[kb * 4096 + (which < 0 ? 0 : 3072) + cb * 256 + tid] = t;
      }
    }
  } else if (bid < 256) {
    const int t = bid - 192, rt = (t & 1) * 32 + (t >> 1);
    u32x4 v[2][4];
    UNROLL for (int c = 0; c < 2; c++) UNROLL for (int i = 0; i < 4; i++)
      v[c][i] = *(const GAS u32x4*)(x0 + (size_t)(rt * 32 + r) * 2048 + (wid * 2 + c) * 128 + i * 32 + hi * 16);
    UNROLL for (int c = 0; c < 2; c++) {
      float s = 0, q = 0;
      UNROLL for (int i = 0; i < 4; i++) {
        *(GAS u32x4*)(ws + OFF_ACT + (size_t)rt * 65536 + ((wid * 2 + c) * 4 + i) * 1024 + lane * 16) = v[c][i];
        UNROLL for (int j = 0; j < 4; j++) { s = dot2(v[c][i][j], 0x3f803f80u, s); q = dot2(v[c][i][j], v[c][i][j], q); }
      }
      s = half_add(s); q = half_add(q);
      if (hi == 0) *(GAS f32x2*)(ws + OFF_ST + rt * 4096 + (wid * 2 + c) * 256 + r * 8) = (f32x2){s, q};
    }
  } else if (bid < 264) {
    const int t = (bid - 256) * 512 + tid, h = t >> 7, s = (t >> 6) & 1;
    bf16x8 o;
    UNROLL for (int j = 0; j < 8; j++) o[j] = (short)wphi[h * 1024 + (16 * s + 8 * (j >> 2) + 4 * hi + (j & 3)) * 32 + r];
    *(GAS bf16x8*)(ws + OFF_WPHIF + (size_t)t * 16) = o;
  } else if (bid < 280) {
    const int t = (bid - 264) * 512 + tid, type = t >> 12, kk = (t >> 7) & 31, u = (t >> 6) & 1;
    const u16* W = type ? wv : wk;
    bf16x8 o;
    UNROLL for (int j = 0; j < 8; j++) o[j] = (short)W[(32 * kk + 8 * (lane >> 4) + j) * 32 + 16 * u + (lane & 15)];
    *(GAS bf16x8*)(ws + OFF_WKVF + (size_t)t * 16) = o;
  } else if (bid < 282) {
    const char* W = (const char*)(bid == 280 ? wk : wv);
    float a[8] = {0, 0, 0, 0, 0, 0, 0, 0};
    u32x4 v[8];
    UNROLL for (int i = 0; i < 8; i++) v[i] = *(const GAS u32x4*)(W + (wid * 8 + i) * 1024 + lane * 16);
    UNROLL for (int i = 0; i < 8; i++) UNROLL for (int j = 0; j < 4; j++) { a[2 * j] += bflo(v[i][j]); a[2 * j + 1] += bfhi(v[i][j]); }
    UNROLL for (int j = 0; j < 8; j++) { a[j] += __shfl_xor(a[j], 4); a[j] += __shfl_xor(a[j], 8); a[j] += __shfl_xor(a[j], 16); a[j] += __shfl_xor(a[j], 32); }
    LAS float* red = (LAS float*)lds;
    if (lane < 4) UNROLL for (int j = 0; j < 8; j++) red[wid * 32 + lane * 8 + j] = a[j];
    __syncthreads();
    if (tid < 32) {
      float t = 0.f;
      UNROLL for (int w = 0; w < 8; w++) t += red[w * 32 + tid];
      ((float*)(ws + OFF_CSKV))[(bid - 280) * 32 + tid] = t;
    }
  } else {
    const int t = (bid - 282) * 512 + tid;
    *(GAS u32x4*)(ws + OFF_FL + (t >> 4) * 4096 + (t & 15) * 128) = (u32x4){0u, 0u, 0u, 0u};
  }
}
constexpr int PREP_LDS = 65536 + 8192;

struct Ctx {
  char* ws; rsrc_t rs; lds_ptr lds; unsigned l0;
  int lane, wid, grp, idx, hi;
};
DI Ctx fresh(const Ctx& x) {
  Ctx y = x;
  asm volatile("" : "+s"(y.wid), "+s"(y.grp), "+s"(y.idx), "+v"(y.lane));
  y.lane &= 63;
  y.hi = y.lane >> 5;
  return y;
}
struct LdsLane {
  lds_ptr b0, b1, b2;
  DI LdsLane(lds_ptr lds, int lane) : b0(lds + lane * 16), b1(b0 + 65536), b2(b0 + 131072) { asm volatile("" : "+v"(b0), "+v"(b1), "+v"(b2)); }
  DI bf16x8 rd(int off) const { return lds_b128((off < 65536 ? b0 : (off < 131072 ? b1 : b2)) + (off & 65535)); }
};
constexpr int LDS_ALL = 163840;
constexpr int A_SLOT = 24576, A_ROW = 200, A_IMG = 106496, A_KVL = A_IMG + 256 * A_ROW, A_ZL = A_KVL + 4096, A_CNT = A_ZL + 128;
static_assert(A_CNT + 16 <= LDS_ALL, "");
DI unsigned act_off(int i) { return OFF_ACT + i * 4 * MBy; }

DI int a_head(const Ctx& x) { return (x.grp >> 1) * 8 + (x.idx >> 2); }
DI int a_tile(const Ctx& x) { return (x.grp & 1) * 4 + (x.idx & 3); }
DI int a_rot(const Ctx& x) { return (x.idx >> 3) * 2 + (x.idx & 3) * 3 + x.grp; }
DI unsigned touch(const char* p) { return *(const GAS unsigned*)p; }
DI void keep(unsigned v) { asm volatile("" ::"v"(v)); }
DI void dma_wqkv(const Ctx& x, int c, int pg) {
  const int cc = (c + a_rot(x)) & 7;
  gldsN<3>(x.ws + OFF_WTA + a_head(x) * 196608 + cc * A_SLOT + pg * 3072, x.lane * 16, x.l0 + (c % 6) * A_SLOT + pg * 3072);
}
DI int rot64(const Ctx& x) { return (x.idx >> 1) + x.grp; }
constexpr int w64_at(int which, int c) {
  constexpr int t[3][8] = {{48, 64, 80, 0, 16, 32, 96, 112}, {128, 144, 48, 64, 80, 0, 16, 32}, {32, 48, 64, 80, 96, 112, 128, 144}};
  return t[which][c] * 1024;
}
template <int WHICH, int N> DI void dma_w64(const Ctx& x, int c, int p) {
  const int cc = (c + rot64(x)) & 7;
  gldsN<N>(x.ws + OFF_WT64 + WHICH * 2 * MBy + (x.idx >> 1) * 131072 + cc * 16384 + p * 1024, x.lane * 16, x.l0 + w64_at(WHICH, c) + p * 1024);
}
DI float colsum(const Ctx& x, int col) {
  const GAS float* p = (const GAS float*)(x.ws + OFF_CS) + col;
  return ((p[0] + p[4096]) + (p[2 * 4096] + p[3 * 4096])) + ((p[4 * 4096] + p[5 * 4096]) + (p[6 * 4096] + p[7 * 4096]));
}

DI void phase_a(const Ctx& x, int L) {
  char* ws = x.ws;
  const int lane = x.lane, wid = x.wid, hi = x.hi, h = a_head(x), tt = a_tile(x), r = lane & 31, rt = tt * 8 + wid;
  const lds_ptr lds = x.lds;
  constexpr int SPC = 8, NSTEP = 64, PD = 16, I0 = 6;
  const int rot = a_rot(x);
  STAMP(4 * L, 0);
  float csv[3];
  UNROLL for (int i = 0; i < 3; i++) csv[i] = colsum(x, i * 1024 + h * 32 + r);
  bf16x8 wph[2];
  UNROLL for (int s = 0; s < 2; s++) wph[s] = *(const GAS bf16x8*)(ws + OFF_WPHIF + (h * 2 + s) * 1024 + lane * 16);
  if (L) {
    if (wid == 0) wait_flags(ws, 4 * L, tt * 32 + (lane & 31));
    UNROLL for (int c = 4; c < 6; c++) dma_wqkv(x, c, wid);
    wait_vm_bar<6>();
  }
  if (wid == 7) *(LAS unsigned*)(lds + A_CNT) = 0u;
  STAMP(4 * L, 10);
  const unsigned abase = act_off(4 * L) + rt * 65536;
  auto aload = [&](int s) AI { return __builtin_bit_cast(bf16x8, ld16(x.rs, lane * 16 + (s & 3) * 1024, abase + (((s / SPC) + rot) & 7) * 8192 + (s & 4) * 1024)); };
  bf16x8 a[PD];
  UNROLL for (int s = 0; s < I0; s++) a[s] = aload(s);
  f32x16 acc[3] = {{}, {}, {}};
  const int q4 = (lane & 15) >> 2, cbp = ((lane >> 4) & 1) * 32 + (lane & 3) * 8;
  bf16x8 bf[2][3];
  const LdsLane ll(lds, lane);
  auto bread = [&](int s) AI { UNROLL for (int i = s < 48 ? 0 : 1; i < 3; i++) bf[s & 1][i] = ll.rd(((s % 48) * 3 + i) * 1024); };
  u32x2 stv[8];
  unsigned tch[2];
  if (L == 0) {
    UNROLL for (int c = 2; c < 6; c++) dma_wqkv(x, c, wid);
    wait_vm_bar<I0 + 12>();
  }
  STAMP(4 * L, 1); STAMPC(4 * L, 14);
  bread(0);
  static_for<NSTEP>([&](auto sc) AI {
    constexpr int s = decltype(sc)::value;
    if (s == 15) { wait_vm_bar<ramp(14, I0, PD) - I0>(); dma_wqkv(x, 6, wid); }
    if (s == 25) { bar_only(); dma_wqkv(x, 7, wid); dma_w64<0, 2>(x, 0, wid * 2); }
    if (s == 47) {
      asm volatile("s_waitcnt vmcnt(22) lgkmcnt(0)\n\ts_barrier" ::: "memory");
      dma_w64<0, 2>(x, 1, wid * 2); dma_w64<0, 2>(x, 2, wid * 2);
    }
    if (s == 48) {
      stats_load(x.rs, OFF_ST + 2 * L * 262144 + rt * 4096, lane, stv);
      tch[0] = touch(ws + OFF_WT64 + (x.idx * 8 + wid) * 8192 + lane * 128);
      tch[1] = touch(ws + OFF_WT64 + 2 * MBy + (x.idx * 8 + wid) * 8192 + lane * 128);
    }
    if (s + 1 < NSTEP) bread(s + 1);
    SB();
    const bf16x8 av = a[s % PD];
    UNROLL for (int i = s < 48 ? 0 : 1; i < 3; i++) acc[i] = MFMA32(bf[s & 1][i], av, acc[i]);
    if (s == 48) asm volatile("" : "+v"(acc[0]));
    if (s == 4) asm volatile("" : "+v"(csv[0]), "+v"(csv[1]), "+v"(csv[2]));
    SB();
    UNROLL for (int n = ramp(s - 1, I0, PD); n < ramp(s, I0, PD); n++) a[n % PD] = aload(n);
    SB();
  });
  STAMP(4 * L, 2); STAMPC(4 * L, 15);
  float mean, rstd;
  stats_finish(stv, mean, rstd);
  const bf16x8 mf = mean_frag(mean);
  acc[1] = MFMA32(cs_frag(csv[1], hi), mf, acc[1]);
  acc[2] = MFMA32(cs_frag(csv[2], hi), mf, acc[2]);
  const f32x16 zero = {};
  {
    UNROLL for (int i = 0; i < 16; i++) { acc[1][i] *= rstd; acc[2][i] *= rstd; }
    f32x16 p = MFMA32(wph[0], pack8(acc[1], 0), zero);
    p = MFMA32(wph[1], pack8(acc[1], 1), p);
    const lds_ptr row = lds + A_IMG + (wid * 32 + r) * A_ROW + hi * 8;
    UNROLL for (int g = 0; g < 4; g++) *(LAS u32x2*)(row + 128 + g * 16) = (u32x2){cvtpk(acc[2][4 * g], acc[2][4 * g + 1]), cvtpk(acc[2][4 * g + 2], acc[2][4 * g + 3])};
    f32x16 ep, em;
    dual_softmax(p, ep, em);
    UNROLL for (int g = 0; g < 4; g++) {
      *(LAS u32x2*)(row + g * 16) = (u32x2){cvtpk(ep[4 * g], ep[4 * g + 1]), cvtpk(ep[4 * g + 2], ep[4 * g + 3])};
      *(LAS u32x2*)(row + 64 + g * 16) = (u32x2){cvtpk(em[4 * g], em[4 * g + 1]), cvtpk(em[4 * g + 2], em[4 * g + 3])};
    }
  }
  STAMP(4 * L, 3);
  lgkm_bar();
  STAMP(4 * L, 4);
  const unsigned slab = OFF_SLAB + (L * 256 + h * 8) * SLAB_B;
  if (wid < 4) {
    const int fh = wid & 1;
    const lds_ptr ip = lds + A_IMG + (8 * hi + q4) * A_ROW + cbp;
    if (wid < 2) {
      f32x16 kv = zero;
      UNROLL for (int u = 0; u < 16; u++) {
        const bf16x8 pf = lds_trfrag<4 * A_ROW>(ip + u * 16 * A_ROW + fh * 64), vf = lds_trfrag<4 * A_ROW>(ip + u * 16 * A_ROW + 128);
        kv = MFMA32(pf, vf, kv);
      }
      UNROLL for (int g = 0; g < 4; g++)
        st16(x.rs, (u32x4){__float_as_uint(kv[4 * g]), __float_as_uint(kv[4 * g + 1]), __float_as_uint(kv[4 * g + 2]), __float_as_uint(kv[4 * g + 3])},
             g * 1024 + lane * 16, slab + tt * SLAB_B + fh * 4096);
    } else {
      const bf16x8 sel = sel_frag(lane);
      f32x4 z = {};
      UNROLL for (int u = 0; u < 16; u++) z = MFMA16(sel, lds_trfrag<4 * A_ROW>(ip + u * 16 * A_ROW + fh * 64), z);
      const int zi = __float_as_int(z[0]);
      const u32x4 z4 = {(unsigned)zi, (unsigned)__builtin_amdgcn_mov_dpp(zi, 0x55, 0xf, 0xf, true), (unsigned)__builtin_amdgcn_mov_dpp(zi, 0xAA, 0xf, 0xf, true),
                        (unsigned)__builtin_amdgcn_mov_dpp(zi, 0xFF, 0xf, 0xf, true)};
      if ((lane & 35) == 0) st16(x.rs, z4, lane * 4, slab + tt * SLAB_B + 8192 + fh * 128);
    }
    if (last_arriver(lds + A_CNT, 4, lane)) set_flag(ws, 8 + L, h * 8 + tt, lane);
  }
  STAMP(4 * L, 5);
  bf16x8 phq[4];
  {
    acc[0] = MFMA32(cs_frag(csv[0], hi), mf, acc[0]);
    UNROLL for (int s = 48; s < 64; s++) acc[0] = MFMA32(ll.rd((s - 48) * 3072), a[s % PD], acc[0]);
    UNROLL for (int i = 0; i < 16; i++) acc[0][i] *= rstd;
    f32x16 p = MFMA32(wph[0], pack8(acc[0], 0), zero);
    p = MFMA32(wph[1], pack8(acc[0], 1), p);
    f32x16 ep, em;
    dual_softmax(p, ep, em);
    phq[0] = pack8(ep, 0); phq[1] = pack8(ep, 1); phq[2] = pack8(em, 0); phq[3] = pack8(em, 1);
  }
  keep(tch[0]); keep(tch[1]);
  STAMP(4 * L, 6);
  if (wid == 7) wait_flags(ws, 8 + L, h * 8 + (lane & 7));
  bar_only();
  STAMP(4 * L, 7);
  {
    f32x4 v[8];
    UNROLL for (int t = 0; t < 8; t++) v[t] = __builtin_bit_cast(f32x4, ld16(x.rs, lane * 16, slab + t * SLAB_B + wid * 1024));
    float zs = 0.f;
    if (wid == 0) UNROLL for (int t = 0; t < 8; t++) zs += __uint_as_float(ld4(x.rs, lane * 4, slab + t * SLAB_B + 8192));
    const f32x4 s = ((v[0] + v[1]) + (v[2] + v[3])) + ((v[4] + v[5]) + (v[6] + v[7]));
    *(LAS u32x2*)(lds + A_KVL + (wid >> 1) * 1024 + lane * 16 + (wid & 1) * 8) = (u32x2){cvtpk(s[0], s[1]), cvtpk(s[2], s[3])};
    if (wid == 0) *(LAS u16*)(lds + A_ZL + lane * 2) = (u16)cvtpk(zs, zs);
  }
  lgkm_bar();
  STAMP(4 * L, 8);
  {
    f32x16 num = zero, den = zero;
    UNROLL for (int fs = 0; fs < 4; fs++) {
      const bf16x8 kf = lds_b128(lds + A_KVL + fs * 1024 + lane * 16);
      const s16x4 z0 = *(LAS s16x4*)(lds + A_ZL + fs * 32 + hi * 8), z1 = *(LAS s16x4*)(lds + A_ZL + fs * 32 + hi * 8 + 16);
      num = MFMA32(kf, phq[fs], num);
      den = MFMA32(__builtin_shufflevector(z0, z1, 0, 1, 2, 3, 4, 5, 6, 7), phq[fs], den);
    }
    const float rd = 1.f / (den[0] + 1e-6f);
    UNROLL for (int gp = 0; gp < 2; gp++) {
      float v[8];
      gather8(num, gp, v);
      st16(x.rs, (u32x4){cvtpk(v[0] * rd, v[1] * rd), cvtpk(v[2] * rd, v[3] * rd), cvtpk(v[4] * rd, v[5] * rd), cvtpk(v[6] * rd, v[7] * rd)},
           gp * 1024 + lane * 16, act_off(4 * L + 1) + rt * 65536 + h * 2048);
    }
  }
  wait_vm_bar<0>();
  if (wid == 7) set_flag(ws, 4 * L + 1, tt * 32 + h, lane);
  STAMP(4 * L, 9);
}

constexpr int G_PD = 16, G_I0 = 8;
template <int WHICH, class Hook>
DI void gemm64(const Ctx& x, unsigned abase, f32x16 (&acc)[2], Hook hook) {
  const int lane = x.lane, rot = rot64(x);
  auto aload = [&](int s) AI { return __builtin_bit_cast(bf16x8, ld16(x.rs, lane * 16 + (s & 3) * 1024, abase + (((s >> 3) + rot) & 7) * 8192 + (s & 4) * 1024)); };
  bf16x8 a[G_PD];
  UNROLL for (int s = 0; s < G_I0; s++) a[s] = aload(s);
  bf16x8 bf[3][2];
  const LdsLane ll(x.lds, lane);
  auto bread = [&](int s) AI { UNROLL for (int n = 0; n < 2; n++) bf[s % 3][n] = ll.rd(w64_at(WHICH, s >> 3) + (s & 7) * 2048 + n * 1024); };
  hook(-1);
  bread(0); bread(1);
  static_for<64>([&](auto sc) AI {
    constexpr int s = decltype(sc)::value;
    hook(s);
    if (s + 2 < 64) bread(s + 2);
    SB();
    const bf16x8 av = a[s % G_PD];
    acc[0] = MFMA32(bf[s % 3][0], av, acc[0]);
    acc[1] = MFMA32(bf[s % 3][1], av, acc[1]);
    SB();
    UNROLL for (int n = ramp(s - 1, G_I0, G_PD); n < ramp(s, G_I0, G_PD); n++) a[n % G_PD] = aload(n);
    SB();
  });
}

template <bool AFTER_B>
DI void out_compute(const Ctx& x, int L, u32x4 (&res)[4], char* out) {
  char* ws = x.ws;
  const int lane = x.lane, hi = x.hi, ph = 4 * L + (AFTER_B ? 3 : 1);
  const bool last = AFTER_B && L == 1;
  const int pn = x.idx >> 1, rt = (x.grp * 2 + (x.idx & 1)) * 4 + x.wid, r = lane & 31;
  STAMP(ph, 0);
  bar_only();
  STAMP(ph, 3);
  f32x16 acc[2] = {{}, {}};
  gemm64<AFTER_B ? 2 : 0>(x, act_off(ph) + rt * 65536, acc, [&](int s) AI { if (s == 46) bar_only(); });
  STAMP(ph, 1);
  float ss = 0.f, sq = 0.f;
  UNROLL for (int i = 0; i < 4; i++) {
    float v[8];
    gather8(acc[i >> 1], i & 1, v);
    UNROLL for (int j = 0; j < 4; j++) res[i][j] = cvtpk(v[2 * j] + bflo(res[i][j]), v[2 * j + 1] + bfhi(res[i][j]));
    if (last) *(GAS u32x4*)(out + (size_t)(rt * 32 + r) * 2048 + pn * 128 + i * 32 + hi * 16) = res[i];
    else st16(x.rs, res[i], i * 1024 + lane * 16, act_off(ph + 1) + rt * 65536 + pn * 4096);
    UNROLL for (int j = 0; j < 4; j++) { ss = dot2(res[i][j], 0x3f803f80u, ss); sq = dot2(res[i][j], res[i][j], sq); }
  }
  if (!last) {
    ss = half_add(ss); sq = half_add(sq);
    const u32x4 sp = stats_pair(ss, sq);
    if ((lane & 33) == 0) st16(x.rs, sp, r * 8, OFF_ST + ((ph + 1) >> 1) * 262144 + rt * 4096 + pn * 256);
  }
  bar_only();
  wait_vm_bar<0>();
  STAMP(ph, 2);
}
DI void phase_out_b(const Ctx& x, int L, u32x4 (&res)[4], char* out) {
  if (x.wid < 4) {
    out_compute<true>(x, L, res, out);
    if (x.wid == 0 && L == 0) set_flag(x.ws, 4, x.grp * 32 + (x.idx & 1) * 16 + (x.idx >> 1), x.lane);
    return;
  }
  const int w4 = x.wid - 4, id0 = x.grp * 32 + (x.idx & 1) * 16;
  if (w4 == 3) wait_flags(x.ws, 4 * L + 3, id0 + (x.lane & 15));
  else {
    UNROLL for (int c = 6; c < 8; c++) { dma_w64<2, 3>(x, c, w4 * 5); dma_w64<2, 2>(x, c, w4 * 5 + 3); if (w4 == c - 6) dma_w64<2, 1>(x, c, 15); }
    if (L == 0) UNROLL for (int i = 0; i < 3; i++) { if (w4 * 3 + i < 8) dma_wqkv(x, 0, w4 * 3 + i); }
  }
  bar_only();
  drain_vm();
  bar_only();
  if (L == 0) UNROLL for (int c = 1; c < 4; c++) UNROLL for (int i = 0; i < 2; i++) dma_wqkv(x, c, w4 * 2 + i);
  bar_only();
  bar_only();
}

constexpr int B_QX = 65536, B_PART = 98304;
constexpr int kv_at(int tile) { return (((tile >> 3) + 3) % 5) * 32768 + (tile & 7) * 4096; }
constexpr float QSCALE = 0.17677669529663687f * LOG2E;
DI void phase_out_a_b(const Ctx& x, int L, u32x4 (&res)[4]) {
  char* ws = x.ws;
  const lds_ptr lds = x.lds;
  const int lane = x.lane, wid = x.wid, hi = x.hi, g4 = lane >> 4, c16 = lane & 15, ph = 4 * L + 2;
  const int pm = x.grp * 2 + (x.idx & 1), pj = x.idx >> 1;
  const int rt = pm * 4 + (wid & 3), head = pj * 2 + (wid >> 2);
  const unsigned stb = OFF_ST + (2 * L + 1) * 262144, kvf = OFF_KVF + L * 262144;
  const int id0 = x.grp * 32 + (x.idx & 1) * 16;
  auto kv_dma = [&](int ch, int first) AI {
    gldsN_sc1<4>(ws + kvf + ((ch + x.grp) & 7) * 32768 + first * 1024, lane * 16, x.l0 + kv_at(ch * 8) + first * 1024);
  };
  auto kv_dma2 = [&](int ch, int first) AI {
    gldsN_sc1<2>(ws + kvf + ((ch + x.grp) & 7) * 32768 + first * 1024, lane * 16, x.l0 + kv_at(ch * 8) + first * 1024);
  };
  auto kv_all = [&]() AI {
    const u32x4 v = ld16(x.rs, lane * 4096, OFF_FL + (10 + L) * 128);
    return __all((v[0] & v[1] & v[2] & v[3]) == 1u);
  };
  bool kv_ok = true;
  bf16x8 qf[2];
  if (wid < 4) {
    out_compute<false>(x, L, res, nullptr);
    STAMP(ph, 0);
    float csv[2];
    UNROLL for (int n = 0; n < 2; n++) csv[n] = colsum(x, 3072 + pj * 64 + n * 32 + (lane & 31));
    bar_only();
    STAMP(ph, 1);
    f32x16 acc[2] = {{}, {}};
    u32x2 stv[8];
    gemm64<1>(x, act_off(ph) + rt * 65536, acc, [&](int s) AI {
      if (s == 4) asm volatile("" : "+v"(csv[0]), "+v"(csv[1]));
      if (s == 48) stats_load(x.rs, stb + rt * 4096, lane, stv);
    });
    STAMP(ph, 2);
    float mean, rstd;
    stats_finish(stv, mean, rstd);
    const bf16x8 mf = mean_frag(mean);
    UNROLL for (int n = 0; n < 2; n++) acc[n] = MFMA32(cs_frag(csv[n], hi), mf, acc[n]);
    const float sc = rstd * QSCALE;
    UNROLL for (int i = 0; i < 16; i++) { acc[0][i] *= sc; acc[1][i] *= sc; }
    qf[0] = pack8(acc[0], 0); qf[1] = pack8(acc[0], 1);
    bar_only();
    UNROLL for (int s = 0; s < 2; s++) *(LAS bf16x8*)(lds + B_QX + (wid * 2 + s) * 1024 + lane * 16) = pack8(acc[1], s);
    lgkm_bar();
  } else {
    const int w4 = wid - 4, krt = pm * 4 + (pj >> 2), kth = (pj >> 1) & 1, ktype = pj & 1, u = wid & 1, kh = wid < 6;
    if (wid == 7) wait_flags(ws, ph - 1, x.grp * 32 + (lane & 31));
    else {
      UNROLL for (int c = 3; c < 8; c++) { dma_w64<0, 3>(x, c, w4 * 5); dma_w64<0, 2>(x, c, w4 * 5 + 3); if (w4 == c % 3) dma_w64<0, 1>(x, c, 15); }
      UNROLL for (int c = 0; c < 2; c++) { dma_w64<1, 3>(x, c, w4 * 5); dma_w64<1, 2>(x, c, w4 * 5 + 3); if (w4 == c) dma_w64<1, 1>(x, c, 15); }
    }
    wait_vm_bar<10>();
    bf16x8 kx[16], kw[16];
    UNROLL for (int i = 0; i < 16; i++) kw[i] = *(const GAS bf16x8*)(ws + OFF_WKVF + ((ktype * 32 + kh * 16 + i) * 2 + u) * 1024 + lane * 16);
    f32x4 cs;
    {
      const GAS float* CS = (const GAS float*)(ws + OFF_CSKV);
      if (ktype == 0) cs = *(const GAS f32x4*)(CS + 16 * u + 4 * g4);
      else { const float c = CS[32 + 16 * u + c16]; cs = (f32x4){c, c, c, c}; }
    }
    bar_only();
    UNROLL for (int c = 2; c < 7; c++) dma_w64<1, 4>(x, c, w4 * 4);
    bar_only();
    dma_w64<1, 4>(x, 7, w4 * 4);
    volatile LAS unsigned* pflag = (volatile LAS unsigned*)(lds + B_PART + 2048 + u * 64);
    if (!kh) { pflag[0] = 0u; pflag[32] = 0u; }
    bar_only();
    STAMP(ph, 0);
    if (wid == 7) { set_flag(ws, ph, id0 + pj, lane); wait_flags(ws, ph, id0 + c16); }
    asm volatile("s_waitcnt vmcnt(0) lgkmcnt(0)\n\ts_barrier" ::: "memory");
    __builtin_amdgcn_s_setprio(2);
    UNROLL for (int i = 0; i < 16; i++)
      kx[i] = __builtin_bit_cast(bf16x8, ld16(x.rs, (g4 >> 1) * 1024 + (16 * kth + c16 + 32 * (g4 & 1)) * 16 + (i & 1) * 2048,
                                              act_off(ph) + krt * 65536 + kh * 32768 + (i >> 1) * 4096));
    u32x2 sv[4];
    if (!kh) UNROLL for (int i = 0; i < 4; i++) sv[i] = __builtin_amdgcn_raw_buffer_load_b64(x.rs, (g4 * 4 + i) * 256 + (16 * kth + c16) * 8, stb + krt * 4096, 16);
    f32x4 d[2] = {{}, {}};
    if (ktype == 0) { UNROLL for (int i = 0; i < 16; i++) d[i & 1] = MFMA16(kw[i], kx[i], d[i & 1]); }
    else { UNROLL for (int i = 0; i < 16; i++) d[i & 1] = MFMA16(kx[i], kw[i], d[i & 1]); }
    d[0] += d[1];
    if (kh) {
      *(LAS f32x4*)(lds + B_PART + u * 1024 + lane * 16) = d[0];
      asm volatile("s_waitcnt lgkmcnt(0)" ::: "memory");
      *pflag = 1u;
    } else {
      float s = (__uint_as_float(sv[0][0]) + __uint_as_float(sv[1][0])) + (__uint_as_float(sv[2][0]) + __uint_as_float(sv[3][0]));
      float q = (__uint_as_float(sv[0][1]) + __uint_as_float(sv[1][1])) + (__uint_as_float(sv[2][1]) + __uint_as_float(sv[3][1]));
      s += __shfl_xor(s, 16); q += __shfl_xor(q, 16);
      s = half_add(s); q = half_add(q);
      const float mean = s * (1.f / 1024.f);
      const float rstd = __builtin_amdgcn_rsqf(fmaxf(q * (1.f / 1024.f) - mean * mean, 0.f) + 1e-5f);
      f32x4 m4, r4;
      UNROLL for (int j = 0; j < 4; j++) {
        const float mj = __shfl(mean, 4 * g4 + j), rj = __shfl(rstd, 4 * g4 + j);
        m4[j] = ktype ? mj : mean; r4[j] = ktype ? rj : rstd;
      }
      for (unsigned spins = 0; spins < SPIN_CAP * 16 && *pflag != 1u; spins++) __builtin_amdgcn_s_sleep(1);
      asm volatile("" ::: "memory");
      const f32x4 e = (d[0] + *(LAS f32x4*)(lds + B_PART + u * 1024 + lane * 16) - m4 * cs) * r4;
      const int frag = ktype ? 2 + kth : u, lp = (ktype ? 16 * u : 16 * kth) + c16 + 32 * (g4 & 1);
      const unsigned e0 = cvtpk(e[0], e[1]), e1 = cvtpk(e[2], e[3]);
      const auto w0 = __builtin_amdgcn_permlane32_swap(e0, e0, false, false), w1 = __builtin_amdgcn_permlane32_swap(e1, e1, false, false);
      if (lane < 32) st16(x.rs, (u32x4){w0[0], w1[0], w0[1], w1[1]}, frag * 1024 + lp * 16, kvf + krt * 4096);
      if (last_arriver(lds + B_PART + 2048 + 128, 2, lane)) set_flag(ws, 10 + L, x.grp * 32 + x.idx, lane);
      __builtin_amdgcn_s_setprio(0);
      STAMP(ph, 1);
      if (wid == 7) {
        wait_flags(ws, 10 + L, x.grp * 32 + (lane & 31));
        STAMP(ph, 2);
        UNROLL for (int i = 0; i < 8; i++) kv_dma(0, i * 4);
        kv_ok = kv_all();
        drain_vm();
        STAMP(ph, 7);
      }
    }
    __builtin_amdgcn_s_setprio(0);
    bar_only();
    if (wid == 7) *(LAS unsigned*)(lds + B_QX + 8192) = kv_ok;
    lgkm_bar();
    UNROLL for (int s = 0; s < 2; s++) qf[s] = lds_b128(lds + B_QX + ((wid & 3) * 2 + s) * 1024 + lane * 16);
  }
  __builtin_amdgcn_s_waitcnt(0x0F70);
  const bool kv_early = __builtin_amdgcn_readfirstlane(*(LAS unsigned*)(lds + B_QX + 8192)) != 0;
  STAMP(ph, 3); STAMPC(ph, 14);
  if (kv_early) kv_dma(1, wid * 4);
  const bf16x8 sel = sel_frag(lane);
  const f32x16 zero = {};
  f32x16 O = zero, negm, SA, SB_;
  f32x4 LS = {};
  bf16x8 kf[2], vf[2];
  const LdsLane ll(lds, lane);
  kf[0] = ll.rd(kv_at(0)); kf[1] = ll.rd(kv_at(0) + 1024);
  SA = MFMA32(kf[0], qf[0], zero); SA = MFMA32(kf[1], qf[1], SA);
  kf[0] = ll.rd(kv_at(1)); kf[1] = ll.rd(kv_at(1) + 1024);
  vf[0] = ll.rd(kv_at(0) + 2048); vf[1] = ll.rd(kv_at(0) + 3072);
  {
    float m = SA[0];
    UNROLL for (int i = 1; i < 16; i++) m = fmaxf(m, SA[i]);
    m = half_max(m);
    UNROLL for (int i = 0; i < 16; i++) { negm[i] = -m; SA[i] -= m; }
  }
#define EX(i) Sc[i] = __builtin_amdgcn_exp2f(Sc[i])
#define PIN(x) asm volatile("" : "+v"(x))
  u32x4 p0, p1 = {0u, 0u, 0u, 0u};
  unsigned tch = 0;
  auto step = [&](f32x16& Sc, f32x16& Sn, int kp, int vc, int vp) AI {
    SB();
    Sn = MFMA32(kf[0], qf[0], negm);
    SB();
    kf[0] = ll.rd(kp);
    EX(0); EX(1); EX(2); PIN(Sc);
    SB();
    O = MFMA32(vf[1], __builtin_bit_cast(bf16x8, p1), O);
    SB();
    vf[1] = ll.rd(vc + 3072);
    EX(3); EX(4); EX(5); PIN(Sc);
    SB();
    Sn = MFMA32(kf[1], qf[1], Sn);
    SB();
    kf[1] = ll.rd(kp + 1024);
    EX(6); EX(7);
    p0[0] = cvtpk(Sc[0], Sc[1]); p0[1] = cvtpk(Sc[2], Sc[3]); p0[2] = cvtpk(Sc[4], Sc[5]); p0[3] = cvtpk(Sc[6], Sc[7]); PIN(p0);
    SB();
    LS = MFMA16(sel, __builtin_bit_cast(bf16x8, p1), LS);
    SB();
    EX(8); EX(9); PIN(Sc);
    SB();
    O = MFMA32(vf[0], __builtin_bit_cast(bf16x8, p0), O);
    SB();
    vf[0] = ll.rd(vp + 2048);
    EX(10); EX(11); EX(12); PIN(Sc);
    SB();
    LS = MFMA16(sel, __builtin_bit_cast(bf16x8, p0), LS);
    SB();
    EX(13); EX(14); EX(15);
    p1[0] = cvtpk(Sc[8], Sc[9]); p1[1] = cvtpk(Sc[10], Sc[11]); p1[2] = cvtpk(Sc[12], Sc[13]); p1[3] = cvtpk(Sc[14], Sc[15]); PIN(p1);
    SB();
  };
  auto hook = [&](int t) AI {
    if (t < 2) { if (kv_early) kv_dma(2 + t, wid * 4); }
    if (t == 2 && !kv_ok) {
      _Pragma("nounroll") for (unsigned spins = 0; spins < SPIN_CAP && !kv_all(); spins++) asm volatile("s_sleep 4" ::: "memory");
      _Pragma("nounroll") for (int i = 0; i < 8; i++) { kv_dma(1, i * 4); kv_dma(2, i * 4); kv_dma(3, i * 4); }
    }
    if (t == 6) wait_vm_bar<0>();
    if (t == 8 || t == 10) kv_dma2(4, wid * 4 + t - 8);
    if (t == 12 && wid < 6)
      tch = touch(ws + OFF_WTA + (x.grp >> 1) * 8 * 196608 + (x.idx * 6 + wid) * 8192 + lane * 128);
    if (t == 25) { keep(tch); wait_vm_bar<0>(); }
    if (t >= 25 && t < 31) kv_dma2(5 + (t - 25) / 2, wid * 4 + ((t - 25) & 1) * 2);
    if (t == 38) wait_vm_bar<0>();
    if (t == 39 || t == 41) dma_w64<2, 2>(x, (t - 39) / 2, wid * 2);
    if (t == 49) bar_only();
    if (t >= 50 && t < 58 && !(t & 1)) dma_w64<2, 2>(x, 2 + (t - 50) / 2, wid * 2);
  };
  static_for<32>([&](auto tc) AI {
    constexpr int t = decltype(tc)::value * 2;
    hook(t);
    step(SA, SB_, kv_at((t + 2) & 63), kv_at(t), kv_at(t + 1));
    hook(t + 1);
    step(SB_, SA, kv_at((t + 3) & 63), kv_at(t + 1), kv_at((t + 2) & 63));
  });
  O = MFMA32(vf[1], __builtin_bit_cast(bf16x8, p1), O);
  LS = MFMA16(sel, __builtin_bit_cast(bf16x8, p1), LS);
  STAMP(ph, 4); STAMPC(ph, 15);
  float l = LS[0];
  {
    float chk = l;
    UNROLL for (int i = 0; i < 16; i++) chk = fmaxf(chk, fabsf(O[i]) * 0x1p-27f);
    bool bad = !(chk < 0x1p100f);
    UNROLL for (int i = 0; i < 16; i++) bad |= (O[i] != O[i]);
    if (__any(bad)) {
      O = zero; l = 0.f;
      float m = -__builtin_inff();
      for (int t = 0; t < 64; t++) {
        bf16x8 f[4];
        UNROLL for (int i = 0; i < 4; i++) f[i] = __builtin_bit_cast(bf16x8, ld16(x.rs, i * 1024 + lane * 16, kvf + t * 4096));
        f32x16 s = MFMA32(f[0], qf[0], zero);
        s = MFMA32(f[1], qf[1], s);
        float mt = s[0];
        UNROLL for (int i = 1; i < 16; i++) mt = fmaxf(mt, s[i]);
        const float mn = fmaxf(m, half_max(mt));
        const float al = __builtin_amdgcn_exp2f(m - mn);
        m = mn;
        UNROLL for (int i = 0; i < 16; i++) { s[i] = __builtin_amdgcn_exp2f(s[i] - mn); O[i] *= al; }
        const bf16x8 p0 = pack8(s, 0), p1 = pack8(s, 1);
        f32x4 lt = MFMA16(sel, p0, ((f32x4){0.f, 0.f, 0.f, 0.f}));
        lt = MFMA16(sel, p1, lt);
        l = l * al + lt[0];
        O = MFMA32(f[2], p0, O);
        O = MFMA32(f[3], p1, O);
      }
    }
  }
  const float rl = 1.f / l;
  UNROLL for (int gp = 0; gp < 2; gp++) {
    float v[8];
    gather8(O, gp, v);
    st16(x.rs, (u32x4){cvtpk(v[0] * rl, v[1] * rl), cvtpk(v[2] * rl, v[3] * rl), cvtpk(v[4] * rl, v[5] * rl), cvtpk(v[6] * rl, v[7] * rl)},
         gp * 1024 + lane * 16, act_off(ph + 1) + rt * 65536 + head * 2048);
  }
  asm volatile("s_waitcnt vmcnt(0) lgkmcnt(0)\n\ts_barrier" ::: "memory");
  if (wid == 7) set_flag(ws, ph + 1, id0 + pj, lane);
  STAMP(ph, 5);
}

__global__ __launch_bounds__(512, 2) void k_main(char* ws, char* out) {
  extern __shared__ __attribute__((aligned(16))) char lds_raw[];
  Ctx x;
  x.ws = ws;
  x.rs = __builtin_amdgcn_make_buffer_rsrc(ws, 0, WS_USED, 0x00020000);
  x.lds = (lds_ptr)lds_raw;
  x.l0 = (unsigned)(uintptr_t)x.lds;
  x.lane = threadIdx.x & 63;
  x.wid = __builtin_amdgcn_readfirstlane(threadIdx.x >> 6);
  x.grp = blockIdx.x & 7;
  x.idx = blockIdx.x >> 3;
  x.hi = x.lane >> 5;
  UNROLL for (int c = 0; c < 2; c++) dma_wqkv(x, c, x.wid);
  u32x4 res[4];
  UNROLL for (int i = 0; i < 4; i++)
    res[i] = ld16(x.rs, i * 1024 + x.lane * 16, act_off(0) + ((x.grp * 2 + (x.idx & 1)) * 4 + (x.wid & 3)) * 65536 + (x.idx >> 1) * 4096);
  for (int L = 0; L < 2; L++) {
    phase_a(fresh(x), L);
    phase_out_a_b(fresh(x), L, res);
    phase_out_b(fresh(x), L, res, out);
  }
}

extern "C" void kernel_launch(void* const* d_in, const int* in_sizes, int n_in, void* d_out, int out_size, void* d_ws, size_t ws_size,
                              hipStream_t stream) {
  char* ws = (char*)d_ws;
  (void)hipFuncSetAttribute((const void*)k_main, hipFuncAttributeMaxDynamicSharedMemorySize, LDS_ALL);
  (void)hipFuncSetAttribute((const void*)k_prep, hipFuncAttributeMaxDynamicSharedMemorySize, PREP_LDS);
  k_prep<<<284, 512, PREP_LDS, stream>>>((const char*)d_in[0], (const char*)d_in[1], (const u16*)d_in[2], (const char*)d_in[3], (const char*)d_in[5],
                                         (const u16*)d_in[6], (const u16*)d_in[7], (const char*)d_in[8], ws);
  k_main<<<256, 512, LDS_ALL, stream>>>(ws, (char*)d_out);
}
```

```cpp
#include <hip/hip_runtime.h>
#include <hip/hip_bf16.h>
#include <stdint.h>

typedef unsigned short u16;
using bf16x8 = __attribute__((ext_vector_type(8))) short;
using s16x4 = __attribute__((ext_vector_type(4))) short;
using f32x16 = __attribute__((ext_vector_type(16))) float;
using f32x4 = __attribute__((ext_vector_type(4))) float;
using f32x2 = __attribute__((ext_vector_type(2))) float;
using u32x4 = __attribute__((ext_vector_type(4))) unsigned;
using u32x2 = __attribute__((ext_vector_type(2))) unsigned;
typedef __bf16 bf16x2_t __attribute__((ext_vector_type(2)));
#define LAS __attribute__((address_space(3)))
#define GAS __attribute__((address_space(1)))
typedef LAS char* lds_ptr;
typedef short v4i16_t __attribute__((ext_vector_type(4)));
#define DI __device__ __forceinline__
#define MFMA32(a, b, c) __builtin_amdgcn_mfma_f32_32x32x16_bf16((a), (b), (c), 0, 0, 0)
#define MFMA16(a, b, c) __builtin_amdgcn_mfma_f32_16x16x32_bf16((a), (b), (c), 0, 0, 0)
#define SB() __builtin_amdgcn_sched_barrier(0)
#define RLX_AGENT __ATOMIC_RELAXED, __HIP_MEMORY_SCOPE_AGENT
#define UNROLL _Pragma("unroll")
#define AI __attribute__((always_inline))

constexpr float LOG2E = 1.4426950408889634f;
constexpr unsigned SPIN_CAP = 1u << 20;

constexpr unsigned MBy = 1u << 20;
constexpr unsigned OFF_FL = 768 * 1024;
constexpr unsigned OFF_CSKV = 120 * 1024;
constexpr unsigned OFF_CS = 512 * 1024;
constexpr unsigned OFF_WKVF = 128 * 1024;
constexpr unsigned OFF_WPHIF = 256 * 1024;
constexpr unsigned OFF_ST = 1 * MBy;
constexpr unsigned OFF_KVF = 2 * MBy;
constexpr unsigned OFF_SLAB = 4 * MBy;
constexpr unsigned SLAB_B = 8448;
constexpr unsigned OFF_ACT = 16 * MBy;
constexpr unsigned OFF_WTA = 48 * MBy;
constexpr unsigned OFF_WT64 = 54 * MBy;
constexpr unsigned WS_USED = 60 * MBy;

#define STAMP(k, i)
#define STAMPC(k, i)
DI void glds16(const void* sbase, unsigned voff, unsigned lds_base) {
  unsigned sv;
  asm volatile("s_mov_b32 %0, m0\n\ts_mov_b32 m0, %3\n\ts_nop 0\n\tglobal_load_lds_dwordx4 %1, %2\n\ts_mov_b32 m0, %0"
               : "=&s"(sv) : "v"(voff), "s"(sbase), "s"(lds_base) : "memory");
}
DI void glds4(const void* sbase, unsigned voff, unsigned lds_base) {
  unsigned sv;
  asm volatile("s_mov_b32 %0, m0\n\ts_mov_b32 m0, %3\n\ts_nop 0\n\tglobal_load_lds_dword %1, %2\n\ts_mov_b32 m0, %0"
               : "=&s"(sv) : "v"(voff), "s"(sbase), "s"(lds_base) : "memory");
}
#define GL_(o, sc) "global_load_lds_dwordx4 %1, %2 offset:" #o sc "\n\t"
#define GLDS_N(NAME, SC)                                                                                                   \
  template <int N> DI void NAME(const void* sbase, unsigned voff, unsigned lds_base) {                                     \
    unsigned sv;                                                                                                           \
    if constexpr (N == 1)                                                                                                  \
      asm volatile("s_mov_b32 %0, m0\n\ts_mov_b32 m0, %3\n\ts_nop 0\n\t" GL_(0, SC) "s_mov_b32 m0, %0"                    \
                   : "=&s"(sv) : "v"(voff), "s"(sbase), "s"(lds_base) : "memory");                                         \
    else if constexpr (N == 2)                                                                                                  \
      asm volatile("s_mov_b32 %0, m0\n\ts_mov_b32 m0, %3\n\ts_nop 0\n\t" GL_(0, SC) GL_(1024, SC) "s_mov_b32 m0, %0"       \
                   : "=&s"(sv) : "v"(voff), "s"(sbase), "s"(lds_base) : "memory");                                         \
    else if constexpr (N == 3)                                                                                             \
      asm volatile("s_mov_b32 %0, m0\n\ts_mov_b32 m0, %3\n\ts_nop 0\n\t" GL_(0, SC) GL_(1024, SC) GL_(2048, SC) "s_mov_b32 m0, %0" \
                   : "=&s"(sv) : "v"(voff), "s"(sbase), "s"(lds_base) : "memory");                                         \
    else                                                                                                                   \
      asm volatile("s_mov_b32 %0, m0\n\ts_mov_b32 m0, %3\n\ts_nop 0\n\t" GL_(0, SC) GL_(1024, SC) GL_(2048, SC) GL_(3072, SC) "s_mov_b32 m0, %0" \
                   : "=&s"(sv) : "v"(voff), "s"(sbase), "s"(lds_base) : "memory");                                         \
  }
GLDS_N(gldsN, "")
GLDS_N(gldsN_sc1, " sc1")
DI void glds16_sc1(const void* sbase, unsigned voff, unsigned lds_base) {
  unsigned sv;
  asm volatile("s_mov_b32 %0, m0\n\ts_mov_b32 m0, %3\n\ts_nop 0\n\tglobal_load_lds_dwordx4 %1, %2 sc1\n\ts_mov_b32 m0, %0"
               : "=&s"(sv) : "v"(voff), "s"(sbase), "s"(lds_base) : "memory");
}
template <int N> DI void wait_vm_bar() { asm volatile("s_waitcnt vmcnt(%0)\n\ts_barrier" ::"n"(N) : "memory"); }
DI void bar_only() { asm volatile("s_barrier" ::: "memory"); }
DI void lgkm_bar() { asm volatile("s_waitcnt lgkmcnt(0)\n\ts_barrier" ::: "memory"); }
DI void drain_vm() { asm volatile("s_waitcnt vmcnt(0)" ::: "memory"); }
DI s16x4 lds_tr(lds_ptr p) { return __builtin_bit_cast(s16x4, __builtin_amdgcn_ds_read_tr16_b64_v4i16((LAS v4i16_t*)p)); }
template <int E> DI bf16x8 lds_trfrag(lds_ptr p) { s16x4 lo = lds_tr(p), hi = lds_tr(p + E); return __builtin_shufflevector(lo, hi, 0, 1, 2, 3, 4, 5, 6, 7); }
DI bf16x8 lds_b128(lds_ptr p) { return *(LAS bf16x8*)p; }
DI unsigned cvtpk(float lo, float hi) {
  bf16x2_t v = __builtin_convertvector((f32x2){lo, hi}, bf16x2_t);
  return __builtin_bit_cast(unsigned, v);
}
DI float bflo(unsigned u) { return __uint_as_float(u << 16); }
DI float bfhi(unsigned u) { return __uint_as_float(u & 0xffff0000u); }
DI float dot2(unsigned a, unsigned b, float c) {
  return __builtin_amdgcn_fdot2_f32_bf16(__builtin_bit_cast(bf16x2_t, a), __builtin_bit_cast(bf16x2_t, b), c, false);
}
DI float half_add(float x) { auto r = __builtin_amdgcn_permlane32_swap(__float_as_uint(x), __float_as_uint(x), false, false); return __uint_as_float(r[0]) + __uint_as_float(r[1]); }
DI float half_max(float x) { auto r = __builtin_amdgcn_permlane32_swap(__float_as_uint(x), __float_as_uint(x), false, false); return fmaxf(__uint_as_float(r[0]), __uint_as_float(r[1])); }
DI float half_min(float x) { auto r = __builtin_amdgcn_permlane32_swap(__float_as_uint(x), __float_as_uint(x), false, false); return fminf(__uint_as_float(r[0]), __uint_as_float(r[1])); }
DI bf16x8 pack8(const f32x16& x, int s) {
  u32x4 p = {cvtpk(x[8 * s], x[8 * s + 1]), cvtpk(x[8 * s + 2], x[8 * s + 3]), cvtpk(x[8 * s + 4], x[8 * s + 5]), cvtpk(x[8 * s + 6], x[8 * s + 7])};
  return __builtin_bit_cast(bf16x8, p);
}
DI void gather8(const f32x16& a, int gp, float (&v)[8]) {
  UNROLL for (int j = 0; j < 4; j++) {
    auto s = __builtin_amdgcn_permlane32_swap(__float_as_uint(a[8 * gp + j]), __float_as_uint(a[8 * gp + 4 + j]), false, false);
    v[j] = __uint_as_float(s[0]); v[4 + j] = __uint_as_float(s[1]);
  }
}
typedef __amdgpu_buffer_rsrc_t rsrc_t;
DI u32x4 ld16(rsrc_t rs, unsigned voff, unsigned soff) { return __builtin_amdgcn_raw_buffer_load_b128(rs, voff, soff, 16); }
DI unsigned ld4(rsrc_t rs, unsigned voff, unsigned soff) { return __builtin_amdgcn_raw_buffer_load_b32(rs, voff, soff, 16); }
DI void st16(rsrc_t rs, u32x4 v, unsigned voff, unsigned soff) { __builtin_amdgcn_raw_buffer_store_b128(v, rs, voff + soff, 0, 16); }
DI void st8(rsrc_t rs, u32x2 v, unsigned voff, unsigned soff) { __builtin_amdgcn_raw_buffer_store_b64(v, rs, voff, soff, 16); }
DI void st4(rsrc_t rs, unsigned v, unsigned voff, unsigned soff) { __builtin_amdgcn_raw_buffer_store_b32(v, rs, voff, soff, 16); }
DI unsigned flag_at(int slot, int id) { return OFF_FL + (id >> 2) * 4096 + slot * 128 + (id & 3) * 4; }
DI void set_flag(char* ws, int slot, int id, int lane) {
  if (lane == 0) __hip_atomic_store((GAS unsigned*)(ws + flag_at(slot, id)), 1u, RLX_AGENT);
}
DI void wait_flags(char* ws, int slot, int id) {
  const unsigned off = flag_at(slot, id);
  for (unsigned spins = 0; spins < SPIN_CAP; spins++) {
    const unsigned v = __hip_atomic_load((GAS unsigned*)(ws + off), RLX_AGENT);
    if (__all(v == 1u)) break;
    __builtin_amdgcn_s_sleep(2);
  }
  asm volatile("" ::: "memory");
}
DI bool last_arriver(lds_ptr cnt, unsigned n, int lane) {
  drain_vm();
  unsigned old = 0;
  if (lane == 0) old = __hip_atomic_fetch_add((LAS unsigned*)cnt, 1u, __ATOMIC_RELAXED, __HIP_MEMORY_SCOPE_WORKGROUP);
  return (unsigned)__builtin_amdgcn_readfirstlane(old) == n - 1;
}
DI void stats_load(rsrc_t rs, unsigned base, int lane, u32x2 (&v)[8]) {
  UNROLL for (int i = 0; i < 8; i++) v[i] = __builtin_amdgcn_raw_buffer_load_b64(rs, ((lane >> 5) * 8 + i) * 256 + (lane & 31) * 8, base, 16);
}
DI u32x4 stats_pair(float s, float q) {
  return (u32x4){__float_as_uint(s), __float_as_uint(q), (unsigned)__builtin_amdgcn_mov_dpp(__float_as_int(s), 0xB1, 0xf, 0xf, true),
                 (unsigned)__builtin_amdgcn_mov_dpp(__float_as_int(q), 0xB1, 0xf, 0xf, true)};
}
DI void stats_finish(const u32x2 (&v)[8], float& mean, float& rstd) {
  float s = 0.f, q = 0.f;
  UNROLL for (int i = 0; i < 8; i++) { s += __uint_as_float(v[i][0]); q += __uint_as_float(v[i][1]); }
  s = half_add(s); q = half_add(q);
  mean = s * (1.f / 1024.f);
  rstd = __builtin_amdgcn_rsqf(fmaxf(q * (1.f / 1024.f) - mean * mean, 0.f) + 1e-5f);
}
DI void split3(float x, unsigned& a, unsigned& b, unsigned& c) {
  a = cvtpk(x, x) & 0xffffu; x -= bflo(a);
  b = cvtpk(x, x) & 0xffffu; x -= bflo(b);
  c = cvtpk(x, x) & 0xffffu;
}
DI bf16x8 cs_frag(float cs, int hi) {
  unsigned a, b, c; split3(cs, a, b, c);
  u32x4 p = {a | (b << 16), a | (c << 16), b | (a << 16), 0u};
  if (hi) p = (u32x4){0u, 0u, 0u, 0u};
  return __builtin_bit_cast(bf16x8, p);
}
DI bf16x8 mean_frag(float mean) {
  unsigned a, b, c; split3(-mean, a, b, c);
  u32x4 p = {a | (a << 16), b | (a << 16), b | (c << 16), 0u};
  return __builtin_bit_cast(bf16x8, p);
}
constexpr int ramp(int s, int I0, int PD) { int n = I0 + 2 * (s + 1); if (n > s + 1 + PD) n = s + 1 + PD; return n > 64 ? 64 : n; }
template <int V> struct IC { static constexpr int value = V; };
template <int N, int I = 0, class F> DI void static_for(F&& f) {
  if constexpr (I < N) { f(IC<I>{}); static_for<N, I + 1>(f); }
}
DI void dual_softmax(const f32x16& p, f32x16& ep, f32x16& em) {
  float mx = p[0], mn = p[0];
  UNROLL for (int i = 1; i < 16; i++) { mx = fmaxf(mx, p[i]); mn = fminf(mn, p[i]); }
  mx = half_max(mx); mn = half_min(mn);
  const float a = -mx * LOG2E, b = mn * LOG2E;
  float sp = 0.f, sm = 0.f;
  UNROLL for (int i = 0; i < 16; i++) {
    ep[i] = __builtin_amdgcn_exp2f(__builtin_fmaf(p[i], LOG2E, a));
    em[i] = __builtin_amdgcn_exp2f(__builtin_fmaf(p[i], -LOG2E, b));
    sp += ep[i]; sm += em[i];
  }
  sp = __builtin_amdgcn_rcpf(half_add(sp)); sm = __builtin_amdgcn_rcpf(half_add(sm));
  UNROLL for (int i = 0; i < 16; i++) { ep[i] *= sp; em[i] *= sm; }
}
DI bf16x8 sel_frag(int lane) {
  short v = ((lane & 3) == 0 && ((lane >> 2) & 1) == ((lane >> 4) & 1)) ? (short)0x3f80 : (short)0;
  return (bf16x8){v, v, v, v, v, v, v, v};
}

constexpr int MAIN_CODE_BYTES = 52 * 1024;
__global__ __launch_bounds__(512) void k_prep(const char* __restrict__ x0, const char* __restrict__ wqkv, const u16* __restrict__ wphi,
                                              const char* __restrict__ woA, const char* __restrict__ wq, const u16* __restrict__ wk,
                                              const u16* __restrict__ wv, const char* __restrict__ woB, char* __restrict__ ws) {
  extern __shared__ __attribute__((aligned(16))) char lds_raw[];
  const lds_ptr lds = (lds_ptr)lds_raw;
  const int tid = threadIdx.x, lane = tid & 63, wid = __builtin_amdgcn_readfirstlane(tid >> 6), bid = blockIdx.x;
  const int r = lane & 31, hi = lane >> 5;
  if (bid < 192) {
    int kb, cb, ld, which;
    const char* W;
    if (bid < 96) { const int t = bid >> 3, g = bid & 7; cb = (t % 3) * 4 + (g >> 1); kb = (t / 3) * 2 + (g & 1); ld = 6144; W = wqkv; which = -1; }
    else { const int t = bid - 96; which = t >> 5; kb = (t >> 2) & 7; cb = t & 3; ld = 2048; W = which == 0 ? woA : (which == 1 ? wq : woB); }
    const unsigned l0 = (unsigned)(uintptr_t)lds + wid * 8192;
    UNROLL for (int i = 0; i < 8; i++) glds16(W + (size_t)(kb * 128 + wid * 16 + i * 2) * ld + cb * 512, hi * ld + r * 16, l0 + i * 1024);
    drain_vm();
    const int step = kb * 8 + wid;
    const lds_ptr p = lds + wid * 8192 + (8 * hi + ((lane & 15) >> 2)) * 512 + ((lane >> 4) & 1) * 32 + (lane & 3) * 8;
    float cs[8];
    UNROLL for (int j = 0; j < 8; j++) {
      const u32x4 fr = __builtin_bit_cast(u32x4, lds_trfrag<2048>(p + j * 64));
      unsigned dst;
      if (which < 0) dst = OFF_WTA + (((cb & 3) * 8 + j) * 192 + step * 3 + (cb >> 2)) * 1024;
      else dst = OFF_WT64 + which * 2 * MBy + ((cb * 4 + (j >> 1)) * 128 + step * 2 + (j & 1)) * 1024;
      *(GAS u32x4*)(ws + dst + lane * 16) = fr;
      float c = 0.f;
      UNROLL for (int e = 0; e < 4; e++) c = dot2(fr[e], 0x3f803f80u, c);
      cs[j] = half_add(c);
    }
    if (which < 0 || which == 1) {
      LAS float* red = (LAS float*)(lds + 65536);
      if (hi == 0) UNROLL for (int j = 0; j < 8; j++) red[wid * 256 + j * 32 + r] = cs[j];
      __syncthreads();
      if (tid < 256) {
        float t = 0.f;
        UNROLL for (int w = 0; w < 8; w++) t += red[w * 256 + tid];
        ((float*)(ws + OFF_CS))[kb * 4096 + (which < 0 ? 0 : 3072) + cb * 256 + tid] = t;
      }
    }
  } else if (bid < 256) {
    const int t = bid - 192, rt = (t & 1) * 32 + (t >> 1);
    u32x4 v[2][4];
    UNROLL for (int c = 0; c < 2; c++) UNROLL for (int i = 0; i < 4; i++)
      v[c][i] = *(const GAS u32x4*)(x0 + (size_t)(rt * 32 + r) * 2048 + (wid * 2 + c) * 128 + i * 32 + hi * 16);
    UNROLL for (int c = 0; c < 2; c++) {
      float s = 0, q = 0;
      UNROLL for (int i = 0; i < 4; i++) {
        *(GAS u32x4*)(ws + OFF_ACT + (size_t)rt * 65536 + ((wid * 2 + c) * 4 + i) * 1024 + lane * 16) = v[c][i];
        UNROLL for (int j = 0; j < 4; j++) { s = dot2(v[c][i][j], 0x3f803f80u, s); q = dot2(v[c][i][j], v[c][i][j], q); }
      }
      s = half_add(s); q = half_add(q);
      if (hi == 0) *(GAS f32x2*)(ws + OFF_ST + rt * 4096 + (wid * 2 + c) * 256 + r * 8) = (f32x2){s, q};
    }
  } else if (bid < 264) {
    const int t = (bid - 256) * 512 + tid, h = t >> 7, s = (t >> 6) & 1;
    bf16x8 o;
    UNROLL for (int j = 0; j < 8; j++) o[j] = (short)wphi[h * 1024 + (16 * s + 8 * (j >> 2) + 4 * hi + (j & 3)) * 32 + r];
    *(GAS bf16x8*)(ws + OFF_WPHIF + (size_t)t * 16) = o;
  } else if (bid < 280) {
    const int t = (bid - 264) * 512 + tid, type = t >> 12, kk = (t >> 7) & 31, u = (t >> 6) & 1;
    const u16* W = type ? wv : wk;
    bf16x8 o;
    UNROLL for (int j = 0; j < 8; j++) o[j] = (short)W[(32 * kk + 8 * (lane >> 4) + j) * 32 + 16 * u + (lane & 15)];
    *(GAS bf16x8*)(ws + OFF_WKVF + (size_t)t * 16) = o;
  } else if (bid < 282) {
    const char* W = (const char*)(bid == 280 ? wk : wv);
    float a[8] = {0, 0, 0, 0, 0, 0, 0, 0};
    u32x4 v[8];
    UNROLL for (int i = 0; i < 8; i++) v[i] = *(const GAS u32x4*)(W + (wid * 8 + i) * 1024 + lane * 16);
    UNROLL for (int i = 0; i < 8; i++) UNROLL for (int j = 0; j < 4; j++) { a[2 * j] += bflo(v[i][j]); a[2 * j + 1] += bfhi(v[i][j]); }
    UNROLL for (int j = 0; j < 8; j++) { a[j] += __shfl_xor(a[j], 4); a[j] += __shfl_xor(a[j], 8); a[j] += __shfl_xor(a[j], 16); a[j] += __shfl_xor(a[j], 32); }
    LAS float* red = (LAS float*)lds;
    if (lane < 4) UNROLL for (int j = 0; j < 8; j++) red[wid * 32 + lane * 8 + j] = a[j];
    __syncthreads();
    if (tid < 32) {
      float t = 0.f;
      UNROLL for (int w = 0; w < 8; w++) t += red[w * 32 + tid];
      ((float*)(ws + OFF_CSKV))[(bid - 280) * 32 + tid] = t;
    }
  } else if (bid >= 288) {
    unsigned long long pc;
    asm volatile("s_getpc_b64 s[40:41]\n\ts_add_u32 s40, s40, _Z6k_mainPcS_@rel32@lo+4\n\ts_addc_u32 s41, s41, _Z6k_mainPcS_@rel32@hi+12\n\ts_mov_b64 %0, s[40:41]"
                 : "=s"(pc) : : "s40", "s41", "scc");
    if (tid * 128 < MAIN_CODE_BYTES) { const unsigned v = *(const GAS unsigned*)(pc + tid * 128); asm volatile("" ::"v"(v)); }
  } else if (bid < 284) {
    const int t = (bid - 282) * 512 + tid;
    *(GAS u32x4*)(ws + OFF_FL + (t >> 4) * 4096 + (t & 15) * 128) = (u32x4){0u, 0u, 0u, 0u};
  }
}
constexpr int PREP_LDS = 65536 + 8192;

struct Ctx {
  char* ws; rsrc_t rs; lds_ptr lds; unsigned l0;
  int lane, wid, grp, idx, hi;
};
DI Ctx fresh(const Ctx& x) {
  Ctx y = x;
  asm volatile("" : "+s"(y.wid), "+s"(y.grp), "+s"(y.idx), "+v"(y.lane));
  y.lane &= 63;
  y.hi = y.lane >> 5;
  return y;
}
struct LdsLane {
  lds_ptr b0, b1, b2;
  DI LdsLane(lds_ptr lds, int lane) : b0(lds + lane * 16), b1(b0 + 65536), b2(b0 + 131072) { asm volatile("" : "+v"(b0), "+v"(b1), "+v"(b2)); }
  DI bf16x8 rd(int off) const { return lds_b128((off < 65536 ? b0 : (off < 131072 ? b1 : b2)) + (off & 65535)); }
};
constexpr int LDS_ALL = 163840;
constexpr int A_SLOT = 24576, A_ROW = 200, A_IMG = 106496, A_KVL = A_IMG + 256 * A_ROW, A_ZL = A_KVL + 4096, A_CNT = A_ZL + 128;
static_assert(A_CNT + 16 <= LDS_ALL, "");
DI unsigned act_off(int i) { return OFF_ACT + i * 4 * MBy; }

DI int a_head(const Ctx& x) { return (x.grp >> 1) * 8 + (x.idx >> 2); }
DI int a_tile(const Ctx& x) { return (x.grp & 1) * 4 + (x.idx & 3); }
DI int a_rot(const Ctx& x) { return (x.idx >> 3) * 2 + (x.idx & 3) * 3 + x.grp; }
DI unsigned touch(const char* p) { return *(const GAS unsigned*)p; }
DI void keep(unsigned v) { asm volatile("" ::"v"(v)); }
DI void dma_wqkv(const Ctx& x, int c, int pg) {
  const int cc = (c + a_rot(x)) & 7;
  gldsN<3>(x.ws + OFF_WTA + a_head(x) * 196608 + cc * A_SLOT + pg * 3072, x.lane * 16, x.l0 + (c % 6) * A_SLOT + pg * 3072);
}
DI int rot64(const Ctx& x) { return (x.idx >> 1) + x.grp; }
constexpr int w64_at(int which, int c) {
  constexpr int t[3][8] = {{48, 64, 80, 0, 16, 32, 96, 112}, {128, 144, 48, 64, 80, 0, 16, 32}, {32, 48, 64, 80, 96, 112, 128, 144}};
  return t[which][c] * 1024;
}
template <int WHICH, int N> DI void dma_w64(const Ctx& x, int c, int p) {
  const int cc = (c + rot64(x)) & 7;
  gldsN<N>(x.ws + OFF_WT64 + WHICH * 2 * MBy + (x.idx >> 1) * 131072 + cc * 16384 + p * 1024, x.lane * 16, x.l0 + w64_at(WHICH, c) + p * 1024);
}
DI float colsum(const Ctx& x, int col) {
  const GAS float* p = (const GAS float*)(x.ws + OFF_CS) + col;
  return ((p[0] + p[4096]) + (p[2 * 4096] + p[3 * 4096])) + ((p[4 * 4096] + p[5 * 4096]) + (p[6 * 4096] + p[7 * 4096]));
}

DI void phase_a(const Ctx& x, int L) {
  char* ws = x.ws;
  const int lane = x.lane, wid = x.wid, hi = x.hi, h = a_head(x), tt = a_tile(x), r = lane & 31, rt = tt * 8 + wid;
  const lds_ptr lds = x.lds;
  constexpr int SPC = 8, NSTEP = 64, PD = 16, I0 = 6;
  const int rot = a_rot(x);
  STAMP(4 * L, 0);
  float csv[3];
  UNROLL for (int i = 0; i < 3; i++) csv[i] = colsum(x, i * 1024 + h * 32 + r);
  bf16x8 wph[2];
  UNROLL for (int s = 0; s < 2; s++) wph[s] = *(const GAS bf16x8*)(ws + OFF_WPHIF + (h * 2 + s) * 1024 + lane * 16);
  if (L) {
    if (wid == 0) wait_flags(ws, 4 * L, tt * 32 + (lane & 31));
    UNROLL for (int c = 4; c < 6; c++) dma_wqkv(x, c, wid);
    wait_vm_bar<6>();
  }
  if (wid == 7) *(LAS unsigned*)(lds + A_CNT) = 0u;
  STAMP(4 * L, 10);
  const unsigned abase = act_off(4 * L) + rt * 65536;
  auto aload = [&](int s) AI { return __builtin_bit_cast(bf16x8, ld16(x.rs, lane * 16 + (s & 3) * 1024, abase + (((s / SPC) + rot) & 7) * 8192 + (s & 4) * 1024)); };
  bf16x8 a[PD];
  UNROLL for (int s = 0; s < I0; s++) a[s] = aload(s);
  f32x16 acc[3] = {{}, {}, {}};
  const int q4 = (lane & 15) >> 2, cbp = ((lane >> 4) & 1) * 32 + (lane & 3) * 8;
  bf16x8 bf[2][3];
  const LdsLane ll(lds, lane);
  auto bread = [&](int s) AI { UNROLL for (int i = s < 48 ? 0 : 1; i < 3; i++) bf[s & 1][i] = ll.rd(((s % 48) * 3 + i) * 1024); };
  u32x2 stv[8];
  unsigned tch[2];
  if (L == 0) {
    UNROLL for (int c = 2; c < 6; c++) dma_wqkv(x, c, wid);
    wait_vm_bar<I0 + 12>();
  }
  STAMP(4 * L, 1); STAMPC(4 * L, 14);
  bread(0);
  static_for<NSTEP>([&](auto sc) AI {
    constexpr int s = decltype(sc)::value;
    if (s == 15) { wait_vm_bar<ramp(14, I0, PD) - I0>(); dma_wqkv(x, 6, wid); }
    if (s == 25) { bar_only(); dma_wqkv(x, 7, wid); dma_w64<0, 2>(x, 0, wid * 2); }
    if (s == 47) {
      asm volatile("s_waitcnt vmcnt(22) lgkmcnt(0)\n\ts_barrier" ::: "memory");
      dma_w64<0, 2>(x, 1, wid * 2); dma_w64<0, 2>(x, 2, wid * 2);
    }
    if (s == 48) {
      stats_load(x.rs, OFF_ST + 2 * L * 262144 + rt * 4096, lane, stv);
      tch[0] = touch(ws + OFF_WT64 + (x.idx * 8 + wid) * 8192 + lane * 128);
      tch[1] = touch(ws + OFF_WT64 + 2 * MBy + (x.idx * 8 + wid) * 8192 + lane * 128);
    }
    if (s + 1 < NSTEP) bread(s + 1);
    SB();
    const bf16x8 av = a[s % PD];
    UNROLL for (int i = s < 48 ? 0 : 1; i < 3; i++) acc[i] = MFMA32(bf[s & 1][i], av, acc[i]);
    if (s == 48) asm volatile("" : "+v"(acc[0]));
    if (s == 4) asm volatile("" : "+v"(csv[0]), "+v"(csv[1]), "+v"(csv[2]));
    SB();
    UNROLL for (int n = ramp(s - 1, I0, PD); n < ramp(s, I0, PD); n++) a[n % PD] = aload(n);
    SB();
  });
  STAMP(4 * L, 2); STAMPC(4 * L, 15);
  float mean, rstd;
  stats_finish(stv, mean, rstd);
  const bf16x8 mf = mean_frag(mean);
  acc[1] = MFMA32(cs_frag(csv[1], hi), mf, acc[1]);
  acc[2] = MFMA32(cs_frag(csv[2], hi), mf, acc[2]);
  const f32x16 zero = {};
  {
    UNROLL for (int i = 0; i < 16; i++) { acc[1][i] *= rstd; acc[2][i] *= rstd; }
    f32x16 p = MFMA32(wph[0], pack8(acc[1], 0), zero);
    p = MFMA32(wph[1], pack8(acc[1], 1), p);
    const lds_ptr row = lds + A_IMG + (wid * 32 + r) * A_ROW + hi * 8;
    UNROLL for (int g = 0; g < 4; g++) *(LAS u32x2*)(row + 128 + g * 16) = (u32x2){cvtpk(acc[2][4 * g], acc[2][4 * g + 1]), cvtpk(acc[2][4 * g + 2], acc[2][4 * g + 3])};
    f32x16 ep, em;
    dual_softmax(p, ep, em);
    UNROLL for (int g = 0; g < 4; g++) {
      *(LAS u32x2*)(row + g * 16) = (u32x2){cvtpk(ep[4 * g], ep[4 * g + 1]), cvtpk(ep[4 * g + 2], ep[4 * g + 3])};
      *(LAS u32x2*)(row + 64 + g * 16) = (u32x2){cvtpk(em[4 * g], em[4 * g + 1]), cvtpk(em[4 * g + 2], em[4 * g + 3])};
    }
  }
  STAMP(4 * L, 3);
  lgkm_bar();
  STAMP(4 * L, 4);
  const unsigned slab = OFF_SLAB + (L * 256 + h * 8) * SLAB_B;
  if (wid < 4) {
    const int fh = wid & 1;
    const lds_ptr ip = lds + A_IMG + (8 * hi + q4) * A_ROW + cbp;
    if (wid < 2) {
      f32x16 kv = zero;
      UNROLL for (int u = 0; u < 16; u++) {
        const bf16x8 pf = lds_trfrag<4 * A_ROW>(ip + u * 16 * A_ROW + fh * 64), vf = lds_trfrag<4 * A_ROW>(ip + u * 16 * A_ROW + 128);
        kv = MFMA32(pf, vf, kv);
      }
      UNROLL for (int g = 0; g < 4; g++)
        st16(x.rs, (u32x4){__float_as_uint(kv[4 * g]), __float_as_uint(kv[4 * g + 1]), __float_as_uint(kv[4 * g + 2]), __float_as_uint(kv[4 * g + 3])},
             g * 1024 + lane * 16, slab + tt * SLAB_B + fh * 4096);
    } else {
      const bf16x8 sel = sel_frag(lane);
      f32x4 z = {};
      UNROLL for (int u = 0; u < 16; u++) z = MFMA16(sel, lds_trfrag<4 * A_ROW>(ip + u * 16 * A_ROW + fh * 64), z);
      const int zi = __float_as_int(z[0]);
      const u32x4 z4 = {(unsigned)zi, (unsigned)__builtin_amdgcn_mov_dpp(zi, 0x55, 0xf, 0xf, true), (unsigned)__builtin_amdgcn_mov_dpp(zi, 0xAA, 0xf, 0xf, true),
                        (unsigned)__builtin_amdgcn_mov_dpp(zi, 0xFF, 0xf, 0xf, true)};
      if ((lane & 35) == 0) st16(x.rs, z4, lane * 4, slab + tt * SLAB_B + 8192 + fh * 128);
    }
    if (last_arriver(lds + A_CNT, 4, lane)) set_flag(ws, 8 + L, h * 8 + tt, lane);
  }
  STAMP(4 * L, 5);
  bf16x8 phq[4];
  {
    acc[0] = MFMA32(cs_frag(csv[0], hi), mf, acc[0]);
    UNROLL for (int s = 48; s < 64; s++) acc[0] = MFMA32(ll.rd((s - 48) * 3072), a[s % PD], acc[0]);
    UNROLL for (int i = 0; i < 16; i++) acc[0][i] *= rstd;
    f32x16 p = MFMA32(wph[0], pack8(acc[0], 0), zero);
    p = MFMA32(wph[1], pack8(acc[0], 1), p);
    f32x16 ep, em;
    dual_softmax(p, ep, em);
    phq[0] = pack8(ep, 0); phq[1] = pack8(ep, 1); phq[2] = pack8(em, 0); phq[3] = pack8(em, 1);
  }
  keep(tch[0]); keep(tch[1]);
  STAMP(4 * L, 6);
  if (wid == 7) wait_flags(ws, 8 + L, h * 8 + (lane & 7));
  bar_only();
  STAMP(4 * L, 7);
  {
    f32x4 v[8];
    UNROLL for (int t = 0; t < 8; t++) v[t] = __builtin_bit_cast(f32x4, ld16(x.rs, lane * 16, slab + t * SLAB_B + wid * 1024));
    float zs = 0.f;
    if (wid == 0) UNROLL for (int t = 0; t < 8; t++) zs += __uint_as_float(ld4(x.rs, lane * 4, slab + t * SLAB_B + 8192));
    const f32x4 s = ((v[0] + v[1]) + (v[2] + v[3])) + ((v[4] + v[5]) + (v[6] + v[7]));
    *(LAS u32x2*)(lds + A_KVL + (wid >> 1) * 1024 + lane * 16 + (wid & 1) * 8) = (u32x2){cvtpk(s[0], s[1]), cvtpk(s[2], s[3])};
    if (wid == 0) *(LAS u16*)(lds + A_ZL + lane * 2) = (u16)cvtpk(zs, zs);
  }
  lgkm_bar();
  STAMP(4 * L, 8);
  {
    f32x16 num = zero, den = zero;
    UNROLL for (int fs = 0; fs < 4; fs++) {
      const bf16x8 kf = lds_b128(lds + A_KVL + fs * 1024 + lane * 16);
      const s16x4 z0 = *(LAS s16x4*)(lds + A_ZL + fs * 32 + hi * 8), z1 = *(LAS s16x4*)(lds + A_ZL + fs * 32 + hi * 8 + 16);
      num = MFMA32(kf, phq[fs], num);
      den = MFMA32(__builtin_shufflevector(z0, z1, 0, 1, 2, 3, 4, 5, 6, 7), phq[fs], den);
    }
    const float rd = 1.f / (den[0] + 1e-6f);
    UNROLL for (int gp = 0; gp < 2; gp++) {
      float v[8];
      gather8(num, gp, v);
      st16(x.rs, (u32x4){cvtpk(v[0] * rd, v[1] * rd), cvtpk(v[2] * rd, v[3] * rd), cvtpk(v[4] * rd, v[5] * rd), cvtpk(v[6] * rd, v[7] * rd)},
           gp * 1024 + lane * 16, act_off(4 * L + 1) + rt * 65536 + h * 2048);
    }
  }
  wait_vm_bar<0>();
  if (wid == 7) set_flag(ws, 4 * L + 1, tt * 32 + h, lane);
  STAMP(4 * L, 9);
}

constexpr int G_PD = 16, G_I0 = 8;
template <int WHICH, class Hook>
DI void gemm64(const Ctx& x, unsigned abase, f32x16 (&acc)[2], Hook hook) {
  const int lane = x.lane, rot = rot64(x);
  auto aload = [&](int s) AI { return __builtin_bit_cast(bf16x8, ld16(x.rs, lane * 16 + (s & 3) * 1024, abase + (((s >> 3) + rot) & 7) * 8192 + (s & 4) * 1024)); };
  bf16x8 a[G_PD];
  UNROLL for (int s = 0; s < G_I0; s++) a[s] = aload(s);
  bf16x8 bf[3][2];
  const LdsLane ll(x.lds, lane);
  auto bread = [&](int s) AI { UNROLL for (int n = 0; n < 2; n++) bf[s % 3][n] = ll.rd(w64_at(WHICH, s >> 3) + (s & 7) * 2048 + n * 1024); };
  hook(-1);
  bread(0); bread(1);
  static_for<64>([&](auto sc) AI {
    constexpr int s = decltype(sc)::value;
    hook(s);
    if (s + 2 < 64) bread(s + 2);
    SB();
    const bf16x8 av = a[s % G_PD];
    acc[0] = MFMA32(bf[s % 3][0], av, acc[0]);
    acc[1] = MFMA32(bf[s % 3][1], av, acc[1]);
    SB();
    UNROLL for (int n = ramp(s - 1, G_I0, G_PD); n < ramp(s, G_I0, G_PD); n++) a[n % G_PD] = aload(n);
    SB();
  });
}

template <bool AFTER_B>
DI void out_compute(const Ctx& x, int L, u32x4 (&res)[4], char* out) {
  char* ws = x.ws;
  const int lane = x.lane, hi = x.hi, ph = 4 * L + (AFTER_B ? 3 : 1);
  const bool last = AFTER_B && L == 1;
  const int pn = x.idx >> 1, rt = (x.grp * 2 + (x.idx & 1)) * 4 + x.wid, r = lane & 31;
  STAMP(ph, 0);
  bar_only();
  STAMP(ph, 3);
  f32x16 acc[2] = {{}, {}};
  gemm64<AFTER_B ? 2 : 0>(x, act_off(ph) + rt * 65536, acc, [&](int s) AI { if (s == 46) bar_only(); });
  STAMP(ph, 1);
  float ss = 0.f, sq = 0.f;
  UNROLL for (int i = 0; i < 4; i++) {
    float v[8];
    gather8(acc[i >> 1], i & 1, v);
    UNROLL for (int j = 0; j < 4; j++) res[i][j] = cvtpk(v[2 * j] + bflo(res[i][j]), v[2 * j + 1] + bfhi(res[i][j]));
    if (last) *(GAS u32x4*)(out + (size_t)(rt * 32 + r) * 2048 + pn * 128 + i * 32 + hi * 16) = res[i];
    else st16(x.rs, res[i], i * 1024 + lane * 16, act_off(ph + 1) + rt * 65536 + pn * 4096);
    UNROLL for (int j = 0; j < 4; j++) { ss = dot2(res[i][j], 0x3f803f80u, ss); sq = dot2(res[i][j], res[i][j], sq); }
  }
  if (!last) {
    ss = half_add(ss); sq = half_add(sq);
    const u32x4 sp = stats_pair(ss, sq);
    if ((lane & 33) == 0) st16(x.rs, sp, r * 8, OFF_ST + ((ph + 1) >> 1) * 262144 + rt * 4096 + pn * 256);
  }
  bar_only();
  wait_vm_bar<0>();
  STAMP(ph, 2);
}
DI void phase_out_b(const Ctx& x, int L, u32x4 (&res)[4], char* out) {
  if (x.wid < 4) {
    out_compute<true>(x, L, res, out);
    if (x.wid == 0 && L == 0) set_flag(x.ws, 4, x.grp * 32 + (x.idx & 1) * 16 + (x.idx >> 1), x.lane);
    return;
  }
  const int w4 = x.wid - 4, id0 = x.grp * 32 + (x.idx & 1) * 16;
  if (w4 == 3) wait_flags(x.ws, 4 * L + 3, id0 + (x.lane & 15));
  else {
    UNROLL for (int c = 6; c < 8; c++) { dma_w64<2, 3>(x, c, w4 * 5); dma_w64<2, 2>(x, c, w4 * 5 + 3); if (w4 == c - 6) dma_w64<2, 1>(x, c, 15); }
    if (L == 0) UNROLL for (int i = 0; i < 3; i++) { if (w4 * 3 + i < 8) dma_wqkv(x, 0, w4 * 3 + i); }
  }
  bar_only();
  drain_vm();
  bar_only();
  if (L == 0) UNROLL for (int c = 1; c < 4; c++) UNROLL for (int i = 0; i < 2; i++) dma_wqkv(x, c, w4 * 2 + i);
  bar_only();
  bar_only();
}

constexpr int B_QX = 65536, B_PART = 98304;
constexpr int kv_at(int tile) { return (((tile >> 3) + 3) % 5) * 32768 + (tile & 7) * 4096; }
constexpr float QSCALE = 0.17677669529663687f * LOG2E;
DI void phase_out_a_b(const Ctx& x, int L, u32x4 (&res)[4]) {
  char* ws = x.ws;
  const lds_ptr lds = x.lds;
  const int lane = x.lane, wid = x.wid, hi = x.hi, g4 = lane >> 4, c16 = lane & 15, ph = 4 * L + 2;
  const int pm = x.grp * 2 + (x.idx & 1), pj = x.idx >> 1;
  const int rt = pm * 4 + (wid & 3), head = pj * 2 + (wid >> 2);
  const unsigned stb = OFF_ST + (2 * L + 1) * 262144, kvf = OFF_KVF + L * 262144;
  const int id0 = x.grp * 32 + (x.idx & 1) * 16;
  auto kv_dma = [&](int ch, int first) AI {
    gldsN_sc1<4>(ws + kvf + ((ch + x.grp) & 7) * 32768 + first * 1024, lane * 16, x.l0 + kv_at(ch * 8) + first * 1024);
  };
  auto kv_dma2 = [&](int ch, int first) AI {
    gldsN_sc1<2>(ws + kvf + ((ch + x.grp) & 7) * 32768 + first * 1024, lane * 16, x.l0 + kv_at(ch * 8) + first * 1024);
  };
  auto kv_all = [&]() AI {
    const u32x4 v = ld16(x.rs, lane * 4096, OFF_FL + (10 + L) * 128);
    return __all((v[0] & v[1] & v[2] & v[3]) == 1u);
  };
  bool kv_ok = true;
  bf16x8 qf[2];
  if (wid < 4) {
    out_compute<false>(x, L, res, nullptr);
    STAMP(ph, 0);
    float csv[2];
    UNROLL for (int n = 0; n < 2; n++) csv[n] = colsum(x, 3072 + pj * 64 + n * 32 + (lane & 31));
    bar_only();
    STAMP(ph, 1);
    f32x16 acc[2] = {{}, {}};
    u32x2 stv[8];
    gemm64<1>(x, act_off(ph) + rt * 65536, acc, [&](int s) AI {
      if (s == 4) asm volatile("" : "+v"(csv[0]), "+v"(csv[1]));
      if (s == 48) stats_load(x.rs, stb + rt * 4096, lane, stv);
    });
    STAMP(ph, 2);
    float mean, rstd;
    stats_finish(stv, mean, rstd);
    const bf16x8 mf = mean_frag(mean);
    UNROLL for (int n = 0; n < 2; n++) acc[n] = MFMA32(cs_frag(csv[n], hi), mf, acc[n]);
    const float sc = rstd * QSCALE;
    UNROLL for (int i = 0; i < 16; i++) { acc[0][i] *= sc; acc[1][i] *= sc; }
    qf[0] = pack8(acc[0], 0); qf[1] = pack8(acc[0], 1);
    bar_only();
    UNROLL for (int s = 0; s < 2; s++) *(LAS bf16x8*)(lds + B_QX + (wid * 2 + s) * 1024 + lane * 16) = pack8(acc[1], s);
    lgkm_bar();
  } else {
    const int w4 = wid - 4, krt = pm * 4 + (pj >> 2), kth = (pj >> 1) & 1, ktype = pj & 1, u = wid & 1, kh = wid < 6;
    if (wid == 7) wait_flags(ws, ph - 1, x.grp * 32 + (lane & 31));
    else {
      UNROLL for (int c = 3; c < 8; c++) { dma_w64<0, 3>(x, c, w4 * 5); dma_w64<0, 2>(x, c, w4 * 5 + 3); if (w4 == c % 3) dma_w64<0, 1>(x, c, 15); }
      UNROLL for (int c = 0; c < 2; c++) { dma_w64<1, 3>(x, c, w4 * 5); dma_w64<1, 2>(x, c, w4 * 5 + 3); if (w4 == c) dma_w64<1, 1>(x, c, 15); }
    }
    wait_vm_bar<10>();
    bf16x8 kx[16], kw[16];
    UNROLL for (int i = 0; i < 16; i++) kw[i] = *(const GAS bf16x8*)(ws + OFF_WKVF + ((ktype * 32 + kh * 16 + i) * 2 + u) * 1024 + lane * 16);
    f32x4 cs;
    {
      const GAS float* CS = (const GAS float*)(ws + OFF_CSKV);
      if (ktype == 0) cs = *(const GAS f32x4*)(CS + 16 * u + 4 * g4);
      else { const float c = CS[32 + 16 * u + c16]; cs = (f32x4){c, c, c, c}; }
    }
    bar_only();
    UNROLL for (int c = 2; c < 7; c++) dma_w64<1, 4>(x, c, w4 * 4);
    bar_only();
    dma_w64<1, 4>(x, 7, w4 * 4);
    volatile LAS unsigned* pflag = (volatile LAS unsigned*)(lds + B_PART + 2048 + u * 64);
    if (!kh) { pflag[0] = 0u; pflag[32] = 0u; }
    bar_only();
    STAMP(ph, 0);
    if (wid == 7) { set_flag(ws, ph, id0 + pj, lane); wait_flags(ws, ph, id0 + c16); }
    asm volatile("s_waitcnt vmcnt(0) lgkmcnt(0)\n\ts_barrier" ::: "memory");
    __builtin_amdgcn_s_setprio(2);
    UNROLL for (int i = 0; i < 16; i++)
      kx[i] = __builtin_bit_cast(bf16x8, ld16(x.rs, (g4 >> 1) * 1024 + (16 * kth + c16 + 32 * (g4 & 1)) * 16 + (i & 1) * 2048,
                                              act_off(ph) + krt * 65536 + kh * 32768 + (i >> 1) * 4096));
    u32x2 sv[4];
    if (!kh) UNROLL for (int i = 0; i < 4; i++) sv[i] = __builtin_amdgcn_raw_buffer_load_b64(x.rs, (g4 * 4 + i) * 256 + (16 * kth + c16) * 8, stb + krt * 4096, 16);
    f32x4 d[2] = {{}, {}};
    if (ktype == 0) { UNROLL for (int i = 0; i < 16; i++) d[i & 1] = MFMA16(kw[i], kx[i], d[i & 1]); }
    else { UNROLL for (int i = 0; i < 16; i++) d[i & 1] = MFMA16(kx[i], kw[i], d[i & 1]); }
    d[0] += d[1];
    if (kh) {
      *(LAS f32x4*)(lds + B_PART + u * 1024 + lane * 16) = d[0];
      asm volatile("s_waitcnt lgkmcnt(0)" ::: "memory");
      *pflag = 1u;
    } else {
      float s = (__uint_as_float(sv[0][0]) + __uint_as_float(sv[1][0])) + (__uint_as_float(sv[2][0]) + __uint_as_float(sv[3][0]));
      float q = (__uint_as_float(sv[0][1]) + __uint_as_float(sv[1][1])) + (__uint_as_float(sv[2][1]) + __uint_as_float(sv[3][1]));
      s += __shfl_xor(s, 16); q += __shfl_xor(q, 16);
      s = half_add(s); q = half_add(q);
      const float mean = s * (1.f / 1024.f);
      const float rstd = __builtin_amdgcn_rsqf(fmaxf(q * (1.f / 1024.f) - mean * mean, 0.f) + 1e-5f);
      f32x4 m4, r4;
      UNROLL for (int j = 0; j < 4; j++) {
        const float mj = __shfl(mean, 4 * g4 + j), rj = __shfl(rstd, 4 * g4 + j);
        m4[j] = ktype ? mj : mean; r4[j] = ktype ? rj * 0x1p64f : rstd;
      }
      for (unsigned spins = 0; spins < SPIN_CAP * 16 && *pflag != 1u; spins++) __builtin_amdgcn_s_sleep(1);
      asm volatile("" ::: "memory");
      const f32x4 e = (d[0] + *(LAS f32x4*)(lds + B_PART + u * 1024 + lane * 16) - m4 * cs) * r4;
      const int frag = ktype ? 2 + kth : u, lp = (ktype ? 16 * u : 16 * kth) + c16 + 32 * (g4 & 1);
      const unsigned e0 = cvtpk(e[0], e[1]), e1 = cvtpk(e[2], e[3]);
      const auto w0 = __builtin_amdgcn_permlane32_swap(e0, e0, false, false), w1 = __builtin_amdgcn_permlane32_swap(e1, e1, false, false);
      if (lane < 32) st16(x.rs, (u32x4){w0[0], w1[0], w0[1], w1[1]}, frag * 1024 + lp * 16, kvf + krt * 4096);
      if (last_arriver(lds + B_PART + 2048 + 128, 2, lane)) set_flag(ws, 10 + L, x.grp * 32 + x.idx, lane);
      __builtin_amdgcn_s_setprio(0);
      STAMP(ph, 1);
      if (wid == 7) {
        wait_flags(ws, 10 + L, x.grp * 32 + (lane & 31));
        STAMP(ph, 2);
        UNROLL for (int i = 0; i < 8; i++) kv_dma(0, i * 4);
        kv_ok = kv_all();
        drain_vm();
        STAMP(ph, 7);
      }
    }
    __builtin_amdgcn_s_setprio(0);
    bar_only();
    if (wid == 7) *(LAS unsigned*)(lds + B_QX + 8192) = kv_ok;
    lgkm_bar();
    UNROLL for (int s = 0; s < 2; s++) qf[s] = lds_b128(lds + B_QX + ((wid & 3) * 2 + s) * 1024 + lane * 16);
  }
  __builtin_amdgcn_s_waitcnt(0x0F70);
  const bool kv_early = __builtin_amdgcn_readfirstlane(*(LAS unsigned*)(lds + B_QX + 8192)) != 0;
  STAMP(ph, 3); STAMPC(ph, 14);
  if (kv_early) kv_dma(1, wid * 4);
  const bf16x8 sel = sel_frag(lane);
  const f32x16 zero = {};
  f32x16 O = zero, negm, SA, SB_;
  f32x4 LS = {};
  bf16x8 kf[2], vf[2];
  const LdsLane ll(lds, lane);
  kf[0] = ll.rd(kv_at(0)); kf[1] = ll.rd(kv_at(0) + 1024);
  SA = MFMA32(kf[0], qf[0], zero); SA = MFMA32(kf[1], qf[1], SA);
  kf[0] = ll.rd(kv_at(1)); kf[1] = ll.rd(kv_at(1) + 1024);
  vf[0] = ll.rd(kv_at(0) + 2048); vf[1] = ll.rd(kv_at(0) + 3072);
  {
    float m = SA[0];
    UNROLL for (int i = 1; i < 16; i++) m = fmaxf(m, SA[i]);
    m = half_max(m) + 100.f;
    UNROLL for (int i = 0; i < 16; i++) { negm[i] = -m; SA[i] -= m; }
  }
#define EX(i) Sc[i] = __builtin_amdgcn_exp2f(Sc[i])
#define PIN(x) asm volatile("" : "+v"(x))
  u32x4 p0, p1 = {0u, 0u, 0u, 0u};
  unsigned tch = 0;
  auto step = [&](f32x16& Sc, f32x16& Sn, int kp, int vc, int vp) AI {
    SB();
    Sn = MFMA32(kf[0], qf[0], negm);
    SB();
    kf[0] = ll.rd(kp);
    EX(0); EX(1); EX(2); PIN(Sc);
    SB();
    O = MFMA32(vf[1], __builtin_bit_cast(bf16x8, p1), O);
    SB();
    vf[1] = ll.rd(vc + 3072);
    EX(3); EX(4); EX(5); PIN(Sc);
    SB();
    Sn = MFMA32(kf[1], qf[1], Sn);
    SB();
    kf[1] = ll.rd(kp + 1024);
    EX(6); EX(7);
    p0[0] = cvtpk(Sc[0], Sc[1]); p0[1] = cvtpk(Sc[2], Sc[3]); p0[2] = cvtpk(Sc[4], Sc[5]); p0[3] = cvtpk(Sc[6], Sc[7]); PIN(p0);
    SB();
    LS = MFMA16(sel, __builtin_bit_cast(bf16x8, p1), LS);
    SB();
    EX(8); EX(9); PIN(Sc);
    SB();
    O = MFMA32(vf[0], __builtin_bit_cast(bf16x8, p0), O);
    SB();
    vf[0] = ll.rd(vp + 2048);
    EX(10); EX(11); EX(12); PIN(Sc);
    SB();
    LS = MFMA16(sel, __builtin_bit_cast(bf16x8, p0), LS);
    SB();
    EX(13); EX(14); EX(15);
    p1[0] = cvtpk(Sc[8], Sc[9]); p1[1] = cvtpk(Sc[10], Sc[11]); p1[2] = cvtpk(Sc[12], Sc[13]); p1[3] = cvtpk(Sc[14], Sc[15]); PIN(p1);
    SB();
  };
  auto hook = [&](int t) AI {
    if (t < 2) { if (kv_early) kv_dma(2 + t, wid * 4); }
    if (t == 2 && !kv_ok) {
      _Pragma("nounroll") for (unsigned spins = 0; spins < SPIN_CAP && !kv_all(); spins++) asm volatile("s_sleep 4" ::: "memory");
      _Pragma("nounroll") for (int i = 0; i < 8; i++) { kv_dma(1, i * 4); kv_dma(2, i * 4); kv_dma(3, i * 4); }
    }
    if (t == 6) wait_vm_bar<0>();
    if (t == 8 || t == 10) kv_dma2(4, wid * 4 + t - 8);
    if (t == 12 && wid < 6)
      tch = touch(ws + OFF_WTA + (x.grp >> 1) * 8 * 196608 + (x.idx * 6 + wid) * 8192 + lane * 128);
    if (t == 25) { keep(tch); wait_vm_bar<0>(); }
    if (t >= 25 && t < 31) kv_dma2(5 + (t - 25) / 2, wid * 4 + ((t - 25) & 1) * 2);
    if (t == 38) wait_vm_bar<0>();
    if (t == 39 || t == 41) dma_w64<2, 2>(x, (t - 39) / 2, wid * 2);
    if (t == 49) bar_only();
    if (t >= 50 && t < 58 && !(t & 1)) dma_w64<2, 2>(x, 2 + (t - 50) / 2, wid * 2);
  };
  static_for<32>([&](auto tc) AI {
    constexpr int t = decltype(tc)::value * 2;
    hook(t);
    step(SA, SB_, kv_at((t + 2) & 63), kv_at(t), kv_at(t + 1));
    hook(t + 1);
    step(SB_, SA, kv_at((t + 3) & 63), kv_at(t + 1), kv_at((t + 2) & 63));
  });
  O = MFMA32(vf[1], __builtin_bit_cast(bf16x8, p1), O);
  LS = MFMA16(sel, __builtin_bit_cast(bf16x8, p1), LS);
  STAMP(ph, 4); STAMPC(ph, 15);
  float l = LS[0];
  {
    float chk = l;
    UNROLL for (int i = 0; i < 16; i++) chk = fmaxf(chk, fabsf(O[i]) * 0x1p-27f);
    bool bad = !(chk < 0x1p100f);
    UNROLL for (int i = 0; i < 16; i++) bad |= (O[i] != O[i]);
    if (__any(bad)) {
      O = zero; l = 0.f;
      float m = -__builtin_inff();
      for (int t = 0; t < 64; t++) {
        bf16x8 f[4];
        UNROLL for (int i = 0; i < 4; i++) f[i] = __builtin_bit_cast(bf16x8, ld16(x.rs, i * 1024 + lane * 16, kvf + t * 4096));
        f32x16 s = MFMA32(f[0], qf[0], zero);
        s = MFMA32(f[1], qf[1], s);
        float mt = s[0];
        UNROLL for (int i = 1; i < 16; i++) mt = fmaxf(mt, s[i]);
        const float mn = fmaxf(m, half_max(mt));
        const float al = __builtin_amdgcn_exp2f(m - mn);
        m = mn;
        UNROLL for (int i = 0; i < 16; i++) { s[i] = __builtin_amdgcn_exp2f(s[i] - mn); O[i] *= al; }
        const bf16x8 p0 = pack8(s, 0), p1 = pack8(s, 1);
        f32x4 lt = MFMA16(sel, p0, ((f32x4){0.f, 0.f, 0.f, 0.f}));
        lt = MFMA16(sel, p1, lt);
        l = l * al + lt[0];
        O = MFMA32(f[2], p0, O);
        O = MFMA32(f[3], p1, O);
      }
    }
  }
  const float rl = (1.f / l) * 0x1p-64f;
  UNROLL for (int gp = 0; gp < 2; gp++) {
    float v[8];
    gather8(O, gp, v);
    st16(x.rs, (u32x4){cvtpk(v[0] * rl, v[1] * rl), cvtpk(v[2] * rl, v[3] * rl), cvtpk(v[4] * rl, v[5] * rl), cvtpk(v[6] * rl, v[7] * rl)},
         gp * 1024 + lane * 16, act_off(ph + 1) + rt * 65536 + head * 2048);
  }
  asm volatile("s_waitcnt vmcnt(0) lgkmcnt(0)\n\ts_barrier" ::: "memory");
  if (wid == 7) set_flag(ws, ph + 1, id0 + pj, lane);
  STAMP(ph, 5);
}

__global__ __launch_bounds__(512, 2) void k_main(char* ws, char* out) {
  extern __shared__ __attribute__((aligned(16))) char lds_raw[];
  Ctx x;
  x.ws = ws;
  x.rs = __builtin_amdgcn_make_buffer_rsrc(ws, 0, WS_USED, 0x00020000);
  x.lds = (lds_ptr)lds_raw;
  x.l0 = (unsigned)(uintptr_t)x.lds;
  x.lane = threadIdx.x & 63;
  x.wid = __builtin_amdgcn_readfirstlane(threadIdx.x >> 6);
  x.grp = blockIdx.x & 7;
  x.idx = blockIdx.x >> 3;
  x.hi = x.lane >> 5;
  UNROLL for (int c = 0; c < 2; c++) dma_wqkv(x, c, x.wid);
  u32x4 res[4];
  UNROLL for (int i = 0; i < 4; i++)
    res[i] = ld16(x.rs, i * 1024 + x.lane * 16, act_off(0) + ((x.grp * 2 + (x.idx & 1)) * 4 + (x.wid & 3)) * 65536 + (x.idx >> 1) * 4096);
  for (int L = 0; L < 2; L++) {
    phase_a(fresh(x), L);
    phase_out_a_b(fresh(x), L, res);
    phase_out_b(fresh(x), L, res, out);
  }
}

extern "C" void kernel_launch(void* const* d_in, const int* in_sizes, int n_in, void* d_out, int out_size, void* d_ws, size_t ws_size,
                              hipStream_t stream) {
  char* ws = (char*)d_ws;
  (void)hipFuncSetAttribute((const void*)k_main, hipFuncAttributeMaxDynamicSharedMemorySize, LDS_ALL);
  (void)hipFuncSetAttribute((const void*)k_prep, hipFuncAttributeMaxDynamicSharedMemorySize, PREP_LDS);
  k_prep<<<296, 512, PREP_LDS, stream>>>((const char*)d_in[0], (const char*)d_in[1], (const u16*)d_in[2], (const char*)d_in[3], (const char*)d_in[5],
                                         (const u16*)d_in[6], (const u16*)d_in[7], (const char*)d_in[8], ws);
  k_main<<<256, 512, LDS_ALL, stream>>>(ws, (char*)d_out);
}
```

```cpp
#include <hip/hip_runtime.h>
#include <hip/hip_bf16.h>
#include <stdint.h>

typedef unsigned short u16;
using bf16x8 = __attribute__((ext_vector_type(8))) short;
using s16x4 = __attribute__((ext_vector_type(4))) short;
using f32x16 = __attribute__((ext_vector_type(16))) float;
using f32x4 = __attribute__((ext_vector_type(4))) float;
using f32x2 = __attribute__((ext_vector_type(2))) float;
using u32x4 = __attribute__((ext_vector_type(4))) unsigned;
using u32x2 = __attribute__((ext_vector_type(2))) unsigned;
typedef __bf16 bf16x2_t __attribute__((ext_vector_type(2)));
#define LAS __attribute__((address_space(3)))
#define GAS __attribute__((address_space(1)))
typedef LAS char* lds_ptr;
typedef short v4i16_t __attribute__((ext_vector_type(4)));
#define DI __device__ __forceinline__
#define MFMA32(a, b, c) __builtin_amdgcn_mfma_f32_32x32x16_bf16((a), (b), (c), 0, 0, 0)
#define MFMA16(a, b, c) __builtin_amdgcn_mfma_f32_16x16x32_bf16((a), (b), (c), 0, 0, 0)
#define SB() __builtin_amdgcn_sched_barrier(0)
#define RLX_AGENT __ATOMIC_RELAXED, __HIP_MEMORY_SCOPE_AGENT
#define UNROLL _Pragma("unroll")
#define AI __attribute__((always_inline))

constexpr float LOG2E = 1.4426950408889634f;
constexpr unsigned SPIN_CAP = 1u << 20;

constexpr unsigned MBy = 1u << 20;
constexpr unsigned OFF_FL = 768 * 1024;
constexpr unsigned OFF_CSKV = 120 * 1024;
constexpr unsigned OFF_CS = 512 * 1024;
constexpr unsigned OFF_WKVF = 128 * 1024;
constexpr unsigned OFF_WPHIF = 256 * 1024;
constexpr unsigned OFF_ST = 1 * MBy;
constexpr unsigned OFF_KVF = 2 * MBy;
constexpr unsigned OFF_SLAB = 4 * MBy;
constexpr unsigned SLAB_B = 8448;
constexpr unsigned OFF_ACT = 16 * MBy;
constexpr unsigned OFF_WTA = 48 * MBy;
constexpr unsigned OFF_WT64 = 54 * MBy;
constexpr unsigned WS_USED = 60 * MBy;

#if defined(__HIP_DEVICE_COMPILE__)
#pragma clang attribute push(__attribute__((target("no-packed-fp32-ops"))), apply_to = function)
#endif
#define U2F(x) __builtin_bit_cast(float, (unsigned)(x))
#define F2U(x) __builtin_bit_cast(unsigned, (float)(x))
#define TID ((int)__builtin_amdgcn_workitem_id_x())
#define BID ((int)__builtin_amdgcn_workgroup_id_x())
DI bool all_lanes(bool p) { return __builtin_amdgcn_ballot_w64(!p) == 0; }
DI bool any_lane(bool p) { return __builtin_amdgcn_ballot_w64(p) != 0; }
DI float shfl(float v, int src) { return U2F(__builtin_amdgcn_ds_bpermute(src * 4, (int)F2U(v))); }
#define STAMP(k, i)
#define STAMPC(k, i)
DI void glds16(const void* sbase, unsigned voff, unsigned lds_base) {
  unsigned sv;
  asm volatile("s_mov_b32 %0, m0\n\ts_mov_b32 m0, %3\n\ts_nop 0\n\tglobal_load_lds_dwordx4 %1, %2\n\ts_mov_b32 m0, %0"
               : "=&s"(sv) : "v"(voff), "s"(sbase), "s"(lds_base) : "memory");
}
#define GL_(o, sc) "global_load_lds_dwordx4 %1, %2 offset:" #o sc "\n\t"
#define GLDS_N(NAME, SC)                                                                                                   \
  template <int N> DI void NAME(const void* sbase, unsigned voff, unsigned lds_base) {                                     \
    unsigned sv;                                                                                                           \
    if constexpr (N == 1)                                                                                                  \
      asm volatile("s_mov_b32 %0, m0\n\ts_mov_b32 m0, %3\n\ts_nop 0\n\t" GL_(0, SC) "s_mov_b32 m0, %0"                    \
                   : "=&s"(sv) : "v"(voff), "s"(sbase), "s"(lds_base) : "memory");                                         \
    else if constexpr (N == 2)                                                                                                  \
      asm volatile("s_mov_b32 %0, m0\n\ts_mov_b32 m0, %3\n\ts_nop 0\n\t" GL_(0, SC) GL_(1024, SC) "s_mov_b32 m0, %0"       \
                   : "=&s"(sv) : "v"(voff), "s"(sbase), "s"(lds_base) : "memory");                                         \
    else if constexpr (N == 3)                                                                                             \
      asm volatile("s_mov_b32 %0, m0\n\ts_mov_b32 m0, %3\n\ts_nop 0\n\t" GL_(0, SC) GL_(1024, SC) GL_(2048, SC) "s_mov_b32 m0, %0" \
                   : "=&s"(sv) : "v"(voff), "s"(sbase), "s"(lds_base) : "memory");                                         \
    else                                                                                                                   \
      asm volatile("s_mov_b32 %0, m0\n\ts_mov_b32 m0, %3\n\ts_nop 0\n\t" GL_(0, SC) GL_(1024, SC) GL_(2048, SC) GL_(3072, SC) "s_mov_b32 m0, %0" \
                   : "=&s"(sv) : "v"(voff), "s"(sbase), "s"(lds_base) : "memory");                                         \
  }
GLDS_N(gldsN, "")
GLDS_N(gldsN_sc1, " sc1")
DI void glds16_sc1(const void* sbase, unsigned voff, unsigned lds_base) {
  unsigned sv;
  asm volatile("s_mov_b32 %0, m0\n\ts_mov_b32 m0, %3\n\ts_nop 0\n\tglobal_load_lds_dwordx4 %1, %2 sc1\n\ts_mov_b32 m0, %0"
               : "=&s"(sv) : "v"(voff), "s"(sbase), "s"(lds_base) : "memory");
}
template <int N> DI void wait_vm_bar() { asm volatile("s_waitcnt vmcnt(%0)\n\ts_barrier" ::"n"(N) : "memory"); }
DI void bar_only() { asm volatile("s_barrier" ::: "memory"); }
DI void lgkm_bar() { asm volatile("s_waitcnt lgkmcnt(0)\n\ts_barrier" ::: "memory"); }
DI void drain_vm() { asm volatile("s_waitcnt vmcnt(0)" ::: "memory"); }
DI s16x4 lds_tr(lds_ptr p) { return __builtin_bit_cast(s16x4, __builtin_amdgcn_ds_read_tr16_b64_v4i16((LAS v4i16_t*)p)); }
template <int E> DI bf16x8 lds_trfrag(lds_ptr p) { s16x4 lo = lds_tr(p), hi = lds_tr(p + E); return __builtin_shufflevector(lo, hi, 0, 1, 2, 3, 4, 5, 6, 7); }
DI bf16x8 lds_b128(lds_ptr p) { return *(LAS bf16x8*)p; }
DI unsigned cvtpk(float lo, float hi) {
  bf16x2_t v = __builtin_convertvector((f32x2){lo, hi}, bf16x2_t);
  return __builtin_bit_cast(unsigned, v);
}
DI float bflo(unsigned u) { return U2F(u << 16); }
DI float bfhi(unsigned u) { return U2F(u & 0xffff0000u); }
DI float dot2(unsigned a, unsigned b, float c) {
  return __builtin_amdgcn_fdot2_f32_bf16(__builtin_bit_cast(bf16x2_t, a), __builtin_bit_cast(bf16x2_t, b), c, false);
}
DI float half_add(float x) { auto r = __builtin_amdgcn_permlane32_swap(F2U(x), F2U(x), false, false); return U2F(r[0]) + U2F(r[1]); }
DI float half_max(float x) { auto r = __builtin_amdgcn_permlane32_swap(F2U(x), F2U(x), false, false); return __builtin_fmaxf(U2F(r[0]), U2F(r[1])); }
DI float half_min(float x) { auto r = __builtin_amdgcn_permlane32_swap(F2U(x), F2U(x), false, false); return __builtin_fminf(U2F(r[0]), U2F(r[1])); }
DI bf16x8 pack8(const f32x16& x, int s) {
  u32x4 p = {cvtpk(x[8 * s], x[8 * s + 1]), cvtpk(x[8 * s + 2], x[8 * s + 3]), cvtpk(x[8 * s + 4], x[8 * s + 5]), cvtpk(x[8 * s + 6], x[8 * s + 7])};
  return __builtin_bit_cast(bf16x8, p);
}
DI void gather8(const f32x16& a, int gp, float (&v)[8]) {
  UNROLL for (int j = 0; j < 4; j++) {
    auto s = __builtin_amdgcn_permlane32_swap(F2U(a[8 * gp + j]), F2U(a[8 * gp + 4 + j]), false, false);
    v[j] = U2F(s[0]); v[4 + j] = U2F(s[1]);
  }
}
typedef __amdgpu_buffer_rsrc_t rsrc_t;
DI u32x4 ld16(rsrc_t rs, unsigned voff, unsigned soff) { return __builtin_amdgcn_raw_buffer_load_b128(rs, voff, soff, 16); }
DI unsigned ld4(rsrc_t rs, unsigned voff, unsigned soff) { return __builtin_amdgcn_raw_buffer_load_b32(rs, voff, soff, 16); }
DI void st16(rsrc_t rs, u32x4 v, unsigned voff, unsigned soff) { __builtin_amdgcn_raw_buffer_store_b128(v, rs, voff + soff, 0, 16); }
DI void st8(rsrc_t rs, u32x2 v, unsigned voff, unsigned soff) { __builtin_amdgcn_raw_buffer_store_b64(v, rs, voff, soff, 16); }
DI void st4(rsrc_t rs, unsigned v, unsigned voff, unsigned soff) { __builtin_amdgcn_raw_buffer_store_b32(v, rs, voff, soff, 16); }
constexpr unsigned EMPTY = 0xffffffffu;
DI u32x4 not_empty(f32x4 v) {
  u32x4 r;
  UNROLL for (int j = 0; j < 4; j++) r[j] = F2U(v[j]) < EMPTY ? F2U(v[j]) : EMPTY - 1;
  return r;
}
DI unsigned flag_at(int slot, int id) { return OFF_FL + (id >> 2) * 4096 + slot * 128 + (id & 3) * 4; }
DI void set_flag(char* ws, int slot, int id, int lane) {
  if (lane == 0) __hip_atomic_store((GAS unsigned*)(ws + flag_at(slot, id)), 1u, RLX_AGENT);
}
DI void wait_flags(char* ws, int slot, int id) {
  const unsigned off = flag_at(slot, id);
  _Pragma("nounroll") for (unsigned spins = 0; spins < SPIN_CAP; spins++) {
    const unsigned v = __hip_atomic_load((GAS unsigned*)(ws + off), RLX_AGENT);
    if (all_lanes(v == 1u)) break;
    __builtin_amdgcn_s_sleep(2);
  }
  asm volatile("" ::: "memory");
}
DI bool last_arriver(lds_ptr cnt, unsigned n, int lane) {
  drain_vm();
  unsigned old = 0;
  if (lane == 0) old = __hip_atomic_fetch_add((LAS unsigned*)cnt, 1u, __ATOMIC_RELAXED, __HIP_MEMORY_SCOPE_WORKGROUP);
  return (unsigned)__builtin_amdgcn_readfirstlane(old) == n - 1;
}
DI void stats_load(rsrc_t rs, unsigned base, int lane, u32x2 (&v)[8]) {
  UNROLL for (int i = 0; i < 8; i++) v[i] = __builtin_amdgcn_raw_buffer_load_b64(rs, ((lane >> 5) * 8 + i) * 256 + (lane & 31) * 8, base, 16);
}
DI u32x4 stats_pair(float s, float q) {
  return (u32x4){F2U(s), F2U(q), (unsigned)__builtin_amdgcn_mov_dpp((int)F2U(s), 0xB1, 0xf, 0xf, true),
                 (unsigned)__builtin_amdgcn_mov_dpp((int)F2U(q), 0xB1, 0xf, 0xf, true)};
}
DI void stats_finish(const u32x2 (&v)[8], float& mean, float& rstd) {
  float s = 0.f, q = 0.f;
  UNROLL for (int i = 0; i < 8; i++) { s += U2F(v[i][0]); q += U2F(v[i][1]); }
  s = half_add(s); q = half_add(q);
  mean = s * (1.f / 1024.f);
  rstd = __builtin_amdgcn_rsqf(__builtin_fmaxf(q * (1.f / 1024.f) - mean * mean, 0.f) + 1e-5f);
}
DI void split3(float x, unsigned& a, unsigned& b, unsigned& c) {
  a = cvtpk(x, x) & 0xffffu; x -= bflo(a);
  b = cvtpk(x, x) & 0xffffu; x -= bflo(b);
  c = cvtpk(x, x) & 0xffffu;
}
DI bf16x8 cs_frag(float cs, int hi) {
  unsigned a, b, c; split3(cs, a, b, c);
  u32x4 p = {a | (b << 16), a | (c << 16), b | (a << 16), 0u};
  if (hi) p = (u32x4){0u, 0u, 0u, 0u};
  return __builtin_bit_cast(bf16x8, p);
}
DI bf16x8 mean_frag(float mean) {
  unsigned a, b, c; split3(-mean, a, b, c);
  u32x4 p = {a | (a << 16), b | (a << 16), b | (c << 16), 0u};
  return __builtin_bit_cast(bf16x8, p);
}
constexpr int ramp(int s, int I0, int PD) { int n = I0 + 2 * (s + 1); if (n > s + 1 + PD) n = s + 1 + PD; return n > 64 ? 64 : n; }
template <int V> struct IC { static constexpr int value = V; };
template <int N, int I = 0, class F> DI void static_for(F&& f) {
  if constexpr (I < N) { f(IC<I>{}); static_for<N, I + 1>(f); }
}
DI void dual_softmax(const f32x16& p, f32x16& ep, f32x16& em) {
  float mx = p[0], mn = p[0];
  UNROLL for (int i = 1; i < 16; i++) { mx = __builtin_fmaxf(mx, p[i]); mn = __builtin_fminf(mn, p[i]); }
  mx = half_max(mx); mn = half_min(mn);
  const float a = -mx * LOG2E, b = mn * LOG2E;
  float sp = 0.f, sm = 0.f;
  UNROLL for (int i = 0; i < 16; i++) {
    ep[i] = __builtin_amdgcn_exp2f(__builtin_fmaf(p[i], LOG2E, a));
    em[i] = __builtin_amdgcn_exp2f(__builtin_fmaf(p[i], -LOG2E, b));
    sp += ep[i]; sm += em[i];
  }
  sp = __builtin_amdgcn_rcpf(half_add(sp)); sm = __builtin_amdgcn_rcpf(half_add(sm));
  UNROLL for (int i = 0; i < 16; i++) { ep[i] *= sp; em[i] *= sm; }
}
DI bf16x8 sel_frag(int lane) {
  short v = ((lane & 3) == 0 && ((lane >> 2) & 1) == ((lane >> 4) & 1)) ? (short)0x3f80 : (short)0;
  return (bf16x8){v, v, v, v, v, v, v, v};
}

constexpr int MAIN_CODE_BYTES = 32 * 1024;
__global__ __launch_bounds__(512) void k_prep(const char* __restrict__ x0, const char* __restrict__ wqkv, const u16* __restrict__ wphi,
                                              const char* __restrict__ woA, const char* __restrict__ wq, const u16* __restrict__ wk,
                                              const u16* __restrict__ wv, const char* __restrict__ woB, char* __restrict__ ws) {
  extern __shared__ __attribute__((aligned(16))) char lds_raw[];
  const lds_ptr lds = (lds_ptr)lds_raw;
  const int tid = TID, lane = tid & 63, wid = __builtin_amdgcn_readfirstlane(tid >> 6), bid = BID;
  const int r = lane & 31, hi = lane >> 5;
  if (bid < 256) UNROLL for (int i = 0; i < 3; i++)
    if (i * 512 + tid < (int)(2 * SLAB_B / 16)) *(GAS u32x4*)(ws + OFF_SLAB + bid * 2 * SLAB_B + (i * 512 + tid) * 16) = (u32x4){EMPTY, EMPTY, EMPTY, EMPTY};
  if (bid < 192) {
    int kb, cb, ld, which;
    const char* W;
    if (bid < 96) { const int t = bid >> 3, g = bid & 7; cb = (t % 3) * 4 + (g >> 1); kb = (t / 3) * 2 + (g & 1); ld = 6144; W = wqkv; which = -1; }
    else { const int t = bid - 96; which = t >> 5; kb = (t >> 2) & 7; cb = t & 3; ld = 2048; W = which == 0 ? woA : (which == 1 ? wq : woB); }
    const unsigned l0 = (unsigned)(uintptr_t)lds + wid * 8192;
    UNROLL for (int i = 0; i < 8; i++) glds16(W + (size_t)(kb * 128 + wid * 16 + i * 2) * ld + cb * 512, hi * ld + r * 16, l0 + i * 1024);
    drain_vm();
    const int step = kb * 8 + wid;
    const lds_ptr p = lds + wid * 8192 + (8 * hi + ((lane & 15) >> 2)) * 512 + ((lane >> 4) & 1) * 32 + (lane & 3) * 8;
    float cs[8];
    UNROLL for (int j = 0; j < 8; j++) {
      const u32x4 fr = __builtin_bit_cast(u32x4, lds_trfrag<2048>(p + j * 64));
      unsigned dst;
      if (which < 0) dst = OFF_WTA + (((cb & 3) * 8 + j) * 192 + step * 3 + (cb >> 2)) * 1024;
      else dst = OFF_WT64 + which * 2 * MBy + ((cb * 4 + (j >> 1)) * 128 + step * 2 + (j & 1)) * 1024;
      *(GAS u32x4*)(ws + dst + lane * 16) = fr;
      float c = 0.f;
      UNROLL for (int e = 0; e < 4; e++) c = dot2(fr[e], 0x3f803f80u, c);
      cs[j] = half_add(c);
    }
    if (which < 0 || which == 1) {
      LAS float* red = (LAS float*)(lds + 65536);
      if (hi == 0) UNROLL for (int j = 0; j < 8; j++) red[wid * 256 + j * 32 + r] = cs[j];
      lgkm_bar();
      if (tid < 256) {
        float t = 0.f;
        UNROLL for (int w = 0; w < 8; w++) t += red[w * 256 + tid];
        ((float*)(ws + OFF_CS))[kb * 4096 + (which < 0 ? 0 : 3072) + cb * 256 + tid] = t;
      }
    }
  } else if (bid < 256) {
    const int t = bid - 192, rt = (t & 1) * 32 + (t >> 1);
    u32x4 v[2][4];
    UNROLL for (int c = 0; c < 2; c++) UNROLL for (int i = 0; i < 4; i++)
      v[c][i] = *(const GAS u32x4*)(x0 + (size_t)(rt * 32 + r) * 2048 + (wid * 2 + c) * 128 + i * 32 + hi * 16);
    UNROLL for (int c = 0; c < 2; c++) {
      float s = 0, q = 0;
      UNROLL for (int i = 0; i < 4; i++) {
        *(GAS u32x4*)(ws + OFF_ACT + (size_t)rt * 65536 + ((wid * 2 + c) * 4 + i) * 1024 + lane * 16) = v[c][i];
        UNROLL for (int j = 0; j < 4; j++) { s = dot2(v[c][i][j], 0x3f803f80u, s); q = dot2(v[c][i][j], v[c][i][j], q); }
      }
      s = half_add(s); q = half_add(q);
      if (hi == 0) *(GAS f32x2*)(ws + OFF_ST + rt * 4096 + (wid * 2 + c) * 256 + r * 8) = (f32x2){s, q};
    }
  } else if (bid < 264) {
    const int t = (bid - 256) * 512 + tid, h = t >> 7, s = (t >> 6) & 1;
    bf16x8 o;
    UNROLL for (int j = 0; j < 8; j++) o[j] = (short)wphi[h * 1024 + (16 * s + 8 * (j >> 2) + 4 * hi + (j & 3)) * 32 + r];
    *(GAS bf16x8*)(ws + OFF_WPHIF + (size_t)t * 16) = o;
  } else if (bid < 280) {
    const int t = (bid - 264) * 512 + tid, type = t >> 12, kk = (t >> 7) & 31, u = (t >> 6) & 1;
    const u16* W = type ? wv : wk;
    bf16x8 o;
    UNROLL for (int j = 0; j < 8; j++) o[j] = (short)W[(32 * kk + 8 * (lane >> 4) + j) * 32 + 16 * u + (lane & 15)];
    *(GAS bf16x8*)(ws + OFF_WKVF + (size_t)t * 16) = o;
  } else if (bid < 282) {
    const char* W = (const char*)(bid == 280 ? wk : wv);
    float a[8] = {0, 0, 0, 0, 0, 0, 0, 0};
    u32x4 v[8];
    UNROLL for (int i = 0; i < 8; i++) v[i] = *(const GAS u32x4*)(W + (wid * 8 + i) * 1024 + lane * 16);
    UNROLL for (int i = 0; i < 8; i++) UNROLL for (int j = 0; j < 4; j++) { a[2 * j] += bflo(v[i][j]); a[2 * j + 1] += bfhi(v[i][j]); }
    UNROLL for (int j = 0; j < 8; j++) { a[j] += shfl(a[j], lane ^ 4); a[j] += shfl(a[j], lane ^ 8); a[j] += shfl(a[j], lane ^ 16); a[j] += shfl(a[j], lane ^ 32); }
    LAS float* red = (LAS float*)lds;
    if (lane < 4) UNROLL for (int j = 0; j < 8; j++) red[wid * 32 + lane * 8 + j] = a[j];
    lgkm_bar();
    if (tid < 32) {
      float t = 0.f;
      UNROLL for (int w = 0; w < 8; w++) t += red[w * 32 + tid];
      ((float*)(ws + OFF_CSKV))[(bid - 280) * 32 + tid] = t;
    }
  } else if (bid >= 288) {
    unsigned long long pc;
    asm volatile("s_getpc_b64 s[40:41]\n\ts_add_u32 s40, s40, _Z6k_mainPcS_@rel32@lo+4\n\ts_addc_u32 s41, s41, _Z6k_mainPcS_@rel32@hi+12\n\ts_mov_b64 %0, s[40:41]"
                 : "=s"(pc) : : "s40", "s41", "scc");
    if (tid * 128 < MAIN_CODE_BYTES) { const unsigned v = *(const GAS unsigned*)(pc + tid * 128); asm volatile("" ::"v"(v)); }
  } else if (bid < 284) {
    const int t = (bid - 282) * 512 + tid;
    *(GAS u32x4*)(ws + OFF_FL + (t >> 4) * 4096 + (t & 15) * 128) = (u32x4){0u, 0u, 0u, 0u};
  }
}
constexpr int PREP_LDS = 65536 + 8192;

struct Ctx {
  char* ws; rsrc_t rs; lds_ptr lds; unsigned l0;
  int lane, wid, grp, idx, hi;
};
DI Ctx fresh(const Ctx& x, int swap) {
  Ctx y = x;
  y.wid ^= swap;
  asm volatile("" : "+s"(y.wid), "+s"(y.grp), "+s"(y.idx), "+v"(y.lane));
  y.lane &= 63;
  y.hi = y.lane >> 5;
  return y;
}
struct LdsLane {
  lds_ptr b0, b1, b2;
  DI LdsLane(lds_ptr lds, int lane) : b0(lds + lane * 16), b1(b0 + 65536), b2(b0 + 131072) { asm volatile("" : "+v"(b0), "+v"(b1), "+v"(b2)); }
  DI bf16x8 rd(int off) const { return lds_b128((off < 65536 ? b0 : (off < 131072 ? b1 : b2)) + (off & 65535)); }
};
constexpr int LDS_ALL = 163840;
constexpr int A_SLOT = 24576, A_ROW = 200, A_IMG = 106496, A_KVL = A_IMG + 256 * A_ROW, A_ZL = A_KVL + 4096, A_CNT = A_ZL + 128;
static_assert(A_CNT + 16 <= LDS_ALL, "");
DI unsigned act_off(int i) { return OFF_ACT + i * 4 * MBy; }

DI int a_head(const Ctx& x) { return (x.grp >> 1) * 8 + (x.idx >> 2); }
DI int a_tile(const Ctx& x) { return (x.grp & 1) * 4 + (x.idx & 3); }
DI void dma_wqkv(const Ctx& x, int c, int pg) {
  gldsN<3>(x.ws + OFF_WTA + a_head(x) * 196608 + c * A_SLOT + pg * 3072, x.lane * 16, x.l0 + (c % 6) * A_SLOT + pg * 3072);
}
constexpr int w64_at(int which, int c) {
  constexpr int t[3][8] = {{48, 64, 80, 0, 16, 32, 96, 112}, {128, 144, 48, 64, 80, 0, 16, 32}, {32, 48, 64, 80, 96, 112, 128, 144}};
  return t[which][c] * 1024;
}
template <int WHICH, int N> DI void dma_w64(const Ctx& x, int c, int p) {
  gldsN<N>(x.ws + OFF_WT64 + WHICH * 2 * MBy + (x.idx >> 1) * 131072 + c * 16384 + p * 1024, x.lane * 16, x.l0 + w64_at(WHICH, c) + p * 1024);
}
DI float colsum_half(const Ctx& x, int col) {
  const GAS float* p = (const GAS float*)(x.ws + OFF_CS) + x.hi * 4 * 4096 + col;
  return (p[0] + p[4096]) + (p[2 * 4096] + p[3 * 4096]);
}

DI void phase_a(const Ctx& x, int L, u32x4 (&res)[4]) {
  char* ws = x.ws;
  const int lane = x.lane, wid = x.wid, hi = x.hi, h = a_head(x), tt = a_tile(x), r = lane & 31, rt = tt * 8 + wid;
  const lds_ptr lds = x.lds;
  constexpr int NSTEP = 64, PD = 16, I0 = 6;
  STAMP(4 * L, 0);
  float csv[3];
  bf16x8 wph[2], csf[3], mf;
  float rstd;
  if (L) {
    if (wid == 0) wait_flags(ws, 4 * L, tt * 32 + (lane & 31));
    UNROLL for (int c = 4; c < 6; c++) dma_wqkv(x, c, wid);
    wait_vm_bar<6>();
  }

  STAMP(4 * L, 10);
  const unsigned abase = act_off(4 * L) + rt * 65536;
  auto aload = [&](int s) AI { return __builtin_bit_cast(bf16x8, ld16(x.rs, lane * 16 + (s & 3) * 1024, abase + (s >> 2) * 4096)); };
  bf16x8 a[PD];
  UNROLL for (int s = 0; s < I0; s++) a[s] = aload(s);
  f32x16 acc[3] = {{}, {}, {}};
  const int q4 = (lane & 15) >> 2;
  bf16x8 bf[2][3];
  const LdsLane ll(lds, lane);
  auto bread = [&](int s) AI { UNROLL for (int i = s < 48 ? 0 : 1; i < 3; i++) bf[s & 1][i] = ll.rd(((s % 48) * 3 + i) * 1024); };
  u32x2 stv[8];
  if (L == 0) {
    UNROLL for (int c = 2; c < 6; c++) dma_wqkv(x, c, wid);
    wait_vm_bar<I0 + 12>();
  }
  STAMP(4 * L, 1); STAMPC(4 * L, 14);
  bread(0);
  static_for<NSTEP>([&](auto sc) AI {
    constexpr int s = decltype(sc)::value;
    if (s == 15) { wait_vm_bar<ramp(14, I0, PD) - I0>(); dma_wqkv(x, 6, wid); }
    if (s == 25) { bar_only(); dma_wqkv(x, 7, wid); dma_w64<0, 2>(x, 0, wid * 2); }
    if (s == 47) {
      asm volatile("s_waitcnt vmcnt(22) lgkmcnt(0)\n\ts_barrier" ::: "memory");
      dma_w64<0, 2>(x, 1, wid * 2); dma_w64<0, 2>(x, 2, wid * 2);
    }
    if (s == 28) {
      UNROLL for (int i = 0; i < 3; i++) csv[i] = colsum_half(x, i * 1024 + h * 32 + r);
      stats_load(x.rs, OFF_ST + 2 * L * 262144 + rt * 4096, lane, stv);
    }
    if (s == 44) {
      float mean;
      stats_finish(stv, mean, rstd);
      mf = mean_frag(mean);
      UNROLL for (int i = 0; i < 3; i++) csf[i] = cs_frag(half_add(csv[i]), hi);
    }
    if (s == 45) UNROLL for (int i = 0; i < 3; i++) acc[i] = MFMA32(csf[i], mf, acc[i]);
    if (s == 48) {
      UNROLL for (int i = 0; i < 2; i++) wph[i] = *(const GAS bf16x8*)(ws + OFF_WPHIF + (h * 2 + i) * 1024 + lane * 16);
      if (L == 0) UNROLL for (int i = 0; i < 4; i++)
        res[i] = ld16(x.rs, i * 1024 + lane * 16, act_off(0) + ((x.grp * 2 + (x.idx & 1)) * 4 + (wid & 3)) * 65536 + (x.idx >> 1) * 4096);
    }
    if (s + 1 < NSTEP) bread(s + 1);
    SB();
    const bf16x8 av = a[s % PD];
    UNROLL for (int i = s < 48 ? 0 : 1; i < 3; i++) acc[i] = MFMA32(bf[s & 1][i], av, acc[i]);
    if (s == 48) asm volatile("" : "+v"(acc[0]));
    if (s == 44) asm volatile("" : "+v"(rstd));
    SB();
    UNROLL for (int n = ramp(s - 1, I0, PD); n < ramp(s, I0, PD); n++) a[n % PD] = aload(n);
    SB();
  });
  STAMP(4 * L, 2); STAMPC(4 * L, 15);
  const f32x16 zero = {};
  {
    UNROLL for (int i = 0; i < 16; i++) { acc[1][i] *= rstd; acc[2][i] *= rstd; }
    f32x16 p = MFMA32(wph[0], pack8(acc[1], 0), zero);
    p = MFMA32(wph[1], pack8(acc[1], 1), p);
    const lds_ptr row = lds + A_IMG + (wid * 32 + r) * A_ROW + hi * 8;
    UNROLL for (int g = 0; g < 4; g++) *(LAS u32x2*)(row + 128 + g * 16) = (u32x2){cvtpk(acc[2][4 * g], acc[2][4 * g + 1]), cvtpk(acc[2][4 * g + 2], acc[2][4 * g + 3])};
    f32x16 ep, em;
    dual_softmax(p, ep, em);
    UNROLL for (int g = 0; g < 4; g++) {
      *(LAS u32x2*)(row + g * 16) = (u32x2){cvtpk(ep[4 * g], ep[4 * g + 1]), cvtpk(ep[4 * g + 2], ep[4 * g + 3])};
      *(LAS u32x2*)(row + 64 + g * 16) = (u32x2){cvtpk(em[4 * g], em[4 * g + 1]), cvtpk(em[4 * g + 2], em[4 * g + 3])};
    }
  }
  STAMP(4 * L, 3);
  lgkm_bar();
  STAMP(4 * L, 4);
  const unsigned slab = OFF_SLAB + (L * 256 + h * 8) * SLAB_B;
  {
    __builtin_amdgcn_s_setprio(3);
    const int ft = wid >> 1, dt = wid & 1;
    const lds_ptr ip = lds + A_IMG + (8 * (lane >> 4) + q4) * A_ROW + (lane & 3) * 8;
    bf16x8 pf[8], vf[8];
    UNROLL for (int u = 0; u < 8; u++) { pf[u] = lds_trfrag<4 * A_ROW>(ip + u * 32 * A_ROW + ft * 32); vf[u] = lds_trfrag<4 * A_ROW>(ip + u * 32 * A_ROW + 128 + dt * 32); }
    SB();
    f32x4 kv = {}, z = {};
    UNROLL for (int u = 0; u < 8; u++) kv = MFMA16(pf[u], vf[u], kv);
    st16(x.rs, not_empty(kv), lane * 16, slab + tt * SLAB_B + wid * 1024);
    if (dt == 0) {
      const short one = (lane & 15) == 0 ? (short)0x3f80 : (short)0;
      const bf16x8 ones = {one, one, one, one, one, one, one, one};
      UNROLL for (int u = 0; u < 8; u++) z = MFMA16(pf[u], ones, z);
      if ((lane & 15) == 0) st16(x.rs, not_empty(z), lane, slab + tt * SLAB_B + 8192 + ft * 64);
    }
  }
  __builtin_amdgcn_s_setprio(0);
  STAMP(4 * L, 5);
  u32x4 gv[8];
  unsigned gz;
  auto gather = [&]() AI {
    UNROLL for (int t = 0; t < 8; t++)
      asm volatile("global_load_dwordx4 %0, %1, %2 sc1" : "=v"(gv[t]) : "v"(lane * 16), "s"(ws + slab + t * SLAB_B + wid * 1024) : "memory");
    asm volatile("global_load_dword %0, %1, %2 sc1" : "=v"(gz) : "v"((lane >> 3) * SLAB_B + (lane & 7) * 4), "s"(ws + slab + 8192 + wid * 32) : "memory");
  };
#define GATHER_WAIT(N)                                                                                                                   \
  asm volatile("s_waitcnt vmcnt(" #N ")"                                                                                                 \
               : "+v"(gv[0]), "+v"(gv[1]), "+v"(gv[2]), "+v"(gv[3]), "+v"(gv[4]), "+v"(gv[5]), "+v"(gv[6]), "+v"(gv[7]), "+v"(gz)::"memory")
  bf16x8 phq[4];
  {
    bf16x8 qw[16];
    UNROLL for (int s = 0; s < 16; s++) qw[s] = ll.rd(s * 3072);
    SB();
    UNROLL for (int s = 0; s < 16; s++) acc[0] = MFMA32(qw[s], a[s], acc[0]);
    asm volatile("" : "+v"(acc[0]));
    gather();
    SB();
    UNROLL for (int i = 0; i < 16; i++) acc[0][i] *= rstd;
    f32x16 p = MFMA32(wph[0], pack8(acc[0], 0), zero);
    p = MFMA32(wph[1], pack8(acc[0], 1), p);
    f32x16 ep, em;
    dual_softmax(p, ep, em);
    phq[0] = pack8(ep, 0); phq[1] = pack8(ep, 1); phq[2] = pack8(em, 0); phq[3] = pack8(em, 1);
    asm volatile("" : "+v"(phq[0]), "+v"(phq[1]), "+v"(phq[2]), "+v"(phq[3]));
  }
  GATHER_WAIT(0);
  STAMP(4 * L, 6);
  _Pragma("nounroll") for (unsigned spins = 0; spins < SPIN_CAP; spins++) {
    unsigned m = gz;
    UNROLL for (int t = 0; t < 8; t++) UNROLL for (int j = 0; j < 4; j++) m = m > gv[t][j] ? m : gv[t][j];
    if (all_lanes(m != EMPTY)) break;
    asm volatile("s_sleep 2" ::: "memory");
    gather();
    GATHER_WAIT(0);
  }
  STAMP(4 * L, 7);
  {
    f32x4 s;
    UNROLL for (int j = 0; j < 4; j++)
      s[j] = ((U2F(gv[0][j]) + U2F(gv[1][j])) + (U2F(gv[2][j]) + U2F(gv[3][j]))) + ((U2F(gv[4][j]) + U2F(gv[5][j])) + (U2F(gv[6][j]) + U2F(gv[7][j])));
    *(LAS u32x2*)(lds + A_KVL + (wid >> 1) * 1024 + ((wid & 1) * 16 + (lane & 15) + ((lane >> 4) & 1) * 32) * 16 + hi * 8) = (u32x2){cvtpk(s[0], s[1]), cvtpk(s[2], s[3])};
    float zs = U2F(gz);
    zs += shfl(zs, lane ^ 8); zs += shfl(zs, lane ^ 16); zs = half_add(zs);
    if (lane < 8) *(LAS u16*)(lds + A_ZL + (wid * 8 + lane) * 2) = (u16)cvtpk(zs, zs);
  }
  lgkm_bar();
  STAMP(4 * L, 8);
  {
    f32x16 num = zero, den = zero;
    UNROLL for (int fs = 0; fs < 4; fs++) {
      const bf16x8 kf = lds_b128(lds + A_KVL + fs * 1024 + lane * 16);
      const s16x4 z0 = *(LAS s16x4*)(lds + A_ZL + fs * 32 + hi * 8), z1 = *(LAS s16x4*)(lds + A_ZL + fs * 32 + hi * 8 + 16);
      num = MFMA32(kf, phq[fs], num);
      den = MFMA32(__builtin_shufflevector(z0, z1, 0, 1, 2, 3, 4, 5, 6, 7), phq[fs], den);
    }
    const float rd = 1.f / (den[0] + 1e-6f);
    UNROLL for (int gp = 0; gp < 2; gp++) {
      float v[8];
      gather8(num, gp, v);
      st16(x.rs, (u32x4){cvtpk(v[0] * rd, v[1] * rd), cvtpk(v[2] * rd, v[3] * rd), cvtpk(v[4] * rd, v[5] * rd), cvtpk(v[6] * rd, v[7] * rd)},
           gp * 1024 + lane * 16, act_off(4 * L + 1) + rt * 65536 + h * 2048);
    }
  }
  wait_vm_bar<0>();
  if (wid == 7) set_flag(ws, 4 * L + 1, tt * 32 + h, lane);
  STAMP(4 * L, 9);
}

constexpr unsigned w64_tbl(int which) { unsigned t = 0; for (int c = 0; c < 8; c++) t |= (unsigned)(w64_at(which, c) >> 14) << (4 * c); return t; }
template <int WHICH, class Hook>
DI void gemm64(const Ctx& x, unsigned abase, f32x16 (&acc)[2], Hook hook) {
  const int lane = x.lane;
  auto aload = [&](unsigned base, int j) AI { return __builtin_bit_cast(bf16x8, ld16(x.rs, lane * 16 + (j & 3) * 1024, base + (j >> 2) * 4096)); };
  bf16x8 a[16], bf[4][2];
  UNROLL for (int j = 0; j < 16; j++) a[j] = aload(abase, j);
  const lds_ptr lb = x.lds + lane * 16;
  lds_ptr b0 = lb + w64_at(WHICH, 0);
  UNROLL for (int j = 0; j < 2; j++) UNROLL for (int n = 0; n < 2; n++) bf[j][n] = lds_b128(b0 + j * 2048 + n * 1024);
  auto trip = [&](int it, auto lastc) AI {
    constexpr bool LAST = decltype(lastc)::value;
    const unsigned t = w64_tbl(WHICH) >> (8 * it);
    const lds_ptr b1 = lb + ((t >> 4) & 15) * 16384, b2 = lb + ((t >> 8) & 15) * 16384;
    abase += 16384;
    static_for<16>([&](auto jc) AI {
      constexpr int j = decltype(jc)::value, r = j + 2;
      hook(it, j);
      if (!LAST || r < 16) UNROLL for (int n = 0; n < 2; n++) bf[r & 3][n] = lds_b128((r < 8 ? b0 : (r < 16 ? b1 : b2)) + (r & 7) * 2048 + n * 1024);
      SB();
      acc[0] = MFMA32(bf[j & 3][0], a[j], acc[0]);
      acc[1] = MFMA32(bf[j & 3][1], a[j], acc[1]);
      SB();
      if (!LAST) a[j] = aload(abase, j);
      SB();
    });
    b0 = b2;
  };
  _Pragma("nounroll") for (int it = 0; it < 3; it++) trip(it, IC<0>{});
  trip(3, IC<1>{});
}

template <bool AFTER_B>
DI void out_compute(const Ctx& x, int L, u32x4 (&res)[4], char* out) {
  char* ws = x.ws;
  const int lane = x.lane, hi = x.hi, ph = 4 * L + (AFTER_B ? 3 : 1);
  const bool last = AFTER_B && L == 1;
  const int pn = x.idx >> 1, rt = (x.grp * 2 + (x.idx & 1)) * 4 + x.wid, r = lane & 31;
  STAMP(ph, 0);
  bar_only();
  STAMP(ph, 3);
  f32x16 acc[2] = {{}, {}};
  gemm64<AFTER_B ? 2 : 0>(x, act_off(ph) + rt * 65536, acc, [&](int it, int j) AI {
    if ((j == 14 && it == 2) || (!AFTER_B && j == 6 && it == 1)) bar_only();
  });
  STAMP(ph, 1);
  float ss = 0.f, sq = 0.f;
  UNROLL for (int i = 0; i < 4; i++) {
    float v[8];
    gather8(acc[i >> 1], i & 1, v);
    UNROLL for (int j = 0; j < 4; j++) res[i][j] = cvtpk(v[2 * j] + bflo(res[i][j]), v[2 * j + 1] + bfhi(res[i][j]));
    if (last) *(GAS u32x4*)(out + (size_t)(rt * 32 + r) * 2048 + pn * 128 + i * 32 + hi * 16) = res[i];
    else st16(x.rs, res[i], i * 1024 + lane * 16, act_off(ph + 1) + rt * 65536 + pn * 4096);
    UNROLL for (int j = 0; j < 4; j++) { ss = dot2(res[i][j], 0x3f803f80u, ss); sq = dot2(res[i][j], res[i][j], sq); }
  }
  if (!last) {
    ss = half_add(ss); sq = half_add(sq);
    const u32x4 sp = stats_pair(ss, sq);
    if ((lane & 33) == 0) st16(x.rs, sp, r * 8, OFF_ST + ((ph + 1) >> 1) * 262144 + rt * 4096 + pn * 256);
  }
  bar_only();
  wait_vm_bar<0>();
  STAMP(ph, 2);
}
DI void phase_out_b(const Ctx& x, int L, u32x4 (&res)[4], char* out) {
  if (x.wid < 4) {
    out_compute<true>(x, L, res, out);
    if (x.wid == 0 && L == 0) set_flag(x.ws, 4, x.grp * 32 + (x.idx & 1) * 16 + (x.idx >> 1), x.lane);
    return;
  }
  const int w4 = x.wid - 4, id0 = x.grp * 32 + (x.idx & 1) * 16;
  if (w4 == 3) wait_flags(x.ws, 4 * L + 3, id0 + (x.lane & 15));
  else {
    UNROLL for (int c = 6; c < 8; c++) { dma_w64<2, 3>(x, c, w4 * 5); dma_w64<2, 2>(x, c, w4 * 5 + 3); if (w4 == c - 6) dma_w64<2, 1>(x, c, 15); }
    if (L == 0) UNROLL for (int i = 0; i < 3; i++) { if (w4 * 3 + i < 8) dma_wqkv(x, 0, w4 * 3 + i); }
  }
  bar_only();
  drain_vm();
  bar_only();
  if (L == 0) UNROLL for (int c = 1; c < 4; c++) UNROLL for (int i = 0; i < 2; i++) dma_wqkv(x, c, w4 * 2 + i);
  bar_only();
  bar_only();
}

constexpr int B_QX = 65536, B_PART = 98304;
constexpr int kv_at(int tile) { return (((tile >> 3) + 3) % 5) * 32768 + (tile & 7) * 4096; }
constexpr float QSCALE = 0.17677669529663687f * LOG2E;
DI void phase_out_a_b(const Ctx& x, int L, u32x4 (&res)[4]) {
  char* ws = x.ws;
  const lds_ptr lds = x.lds;
  const int lane = x.lane, wid = x.wid, hi = x.hi, g4 = lane >> 4, c16 = lane & 15, ph = 4 * L + 2;
  const int pm = x.grp * 2 + (x.idx & 1), pj = x.idx >> 1;
  const int rt = pm * 4 + (wid & 3), head = pj * 2 + (wid >> 2);
  const unsigned stb = OFF_ST + (2 * L + 1) * 262144, kvf = OFF_KVF + L * 262144;
  const int id0 = x.grp * 32 + (x.idx & 1) * 16;
  auto kv_dma = [&](int ch, int first) AI {
    gldsN_sc1<4>(ws + kvf + ((ch + x.grp) & 7) * 32768 + first * 1024, lane * 16, x.l0 + kv_at(ch * 8) + first * 1024);
  };
  auto kv_dma2 = [&](int ch, int first) AI {
    gldsN_sc1<2>(ws + kvf + ((ch + x.grp) & 7) * 32768 + first * 1024, lane * 16, x.l0 + kv_at(ch * 8) + first * 1024);
  };
  auto kv_all = [&]() AI {
    const u32x4 v = ld16(x.rs, lane * 4096, OFF_FL + (10 + L) * 128);
    return all_lanes((v[0] & v[1] & v[2] & v[3]) == 1u);
  };
  bool kv_ok = true;
  bf16x8 qf[2];
  if (wid < 4) {
    out_compute<false>(x, L, res, nullptr);
    STAMP(ph, 0);
    float csv[2];
    UNROLL for (int n = 0; n < 2; n++) csv[n] = colsum_half(x, 3072 + pj * 64 + n * 32 + (lane & 31));
    bar_only();
    STAMP(ph, 1);
    f32x16 acc[2] = {{}, {}};
    u32x2 stv[8];
    float rstd;
    gemm64<1>(x, act_off(ph) + rt * 65536, acc, [&](int it, int j) AI {
      if (j == 0 && it == 3) stats_load(x.rs, stb + rt * 4096, lane, stv);
    });
    {
      float mean;
      stats_finish(stv, mean, rstd);
      const bf16x8 mf = mean_frag(mean);
      UNROLL for (int n = 0; n < 2; n++) acc[n] = MFMA32(cs_frag(half_add(csv[n]), hi), mf, acc[n]);
    }
    STAMP(ph, 2);
    const float sc = rstd * QSCALE;
    UNROLL for (int i = 0; i < 16; i++) { acc[0][i] *= sc; acc[1][i] *= sc; }
    qf[0] = pack8(acc[0], 0); qf[1] = pack8(acc[0], 1);
    bar_only();
    UNROLL for (int s = 0; s < 2; s++) *(LAS bf16x8*)(lds + B_QX + (wid * 2 + s) * 1024 + lane * 16) = pack8(acc[1], s);
    lgkm_bar();
  } else {
    const int w4 = wid - 4, krt = pm * 4 + (pj >> 2), kth = (pj >> 1) & 1, ktype = pj & 1, u = wid & 1;
    bf16x8 kx[8], kw[8][2];
    auto kw_load = [&]() AI {
      UNROLL for (int i = 0; i < 8; i++) UNROLL for (int n = 0; n < 2; n++)
        kw[i][n] = *(const GAS bf16x8*)(ws + OFF_WKVF + ((ktype * 32 + w4 * 8 + i) * 2 + n) * 1024 + lane * 16);
    };
    if (wid == 7) wait_flags(ws, ph - 1, x.grp * 32 + (lane & 31));
    else UNROLL for (int c = 3; c < 8; c++) { dma_w64<0, 3>(x, c, w4 * 5); dma_w64<0, 2>(x, c, w4 * 5 + 3); if (w4 == c % 3) dma_w64<0, 1>(x, c, 15); }
    bar_only();
    wait_vm_bar<0>();
    UNROLL for (int c = 0; c < 2; c++) dma_w64<1, 4>(x, c, w4 * 4);
    kw_load();
    f32x4 cs;
    {
      const GAS float* CS = (const GAS float*)(ws + OFF_CSKV);
      if (ktype == 0) cs = *(const GAS f32x4*)(CS + 16 * u + 4 * g4);
      else { const float c = CS[32 + 16 * u + c16]; cs = (f32x4){c, c, c, c}; }
    }
    bar_only();
    UNROLL for (int c = 2; c < 7; c++) dma_w64<1, 4>(x, c, w4 * 4);
    bar_only();
    dma_w64<1, 4>(x, 7, w4 * 4);
    volatile LAS unsigned* pcnt = (volatile LAS unsigned*)(lds + B_PART);
    if (wid == 6) { pcnt[0] = 0u; pcnt[16] = 0u; }
    bar_only();
    STAMP(ph, 0);
    if (wid == 7) { set_flag(ws, ph, id0 + pj, lane); wait_flags(ws, ph, id0 + c16); }
    asm volatile("s_waitcnt vmcnt(0) lgkmcnt(0)\n\ts_barrier" ::: "memory");
    __builtin_amdgcn_s_setprio(2);
    UNROLL for (int i = 0; i < 8; i++)
      kx[i] = __builtin_bit_cast(bf16x8, ld16(x.rs, (g4 >> 1) * 1024 + (16 * kth + c16 + 32 * (g4 & 1)) * 16 + (i & 1) * 2048,
                                              act_off(ph) + krt * 65536 + w4 * 16384 + (i >> 1) * 4096));
    u32x2 sv[4];
    if (wid >= 6) UNROLL for (int i = 0; i < 4; i++) sv[i] = __builtin_amdgcn_raw_buffer_load_b64(x.rs, (g4 * 4 + i) * 256 + (16 * kth + c16) * 8, stb + krt * 4096, 16);
    f32x4 d[2] = {{}, {}};
    if (ktype == 0) { UNROLL for (int i = 0; i < 8; i++) UNROLL for (int n = 0; n < 2; n++) d[n] = MFMA16(kw[i][n], kx[i], d[n]); }
    else { UNROLL for (int i = 0; i < 8; i++) UNROLL for (int n = 0; n < 2; n++) d[n] = MFMA16(kx[i], kw[i][n], d[n]); }
    UNROLL for (int n = 0; n < 2; n++) *(LAS f32x4*)(lds + B_PART + 1024 + (w4 * 2 + n) * 1024 + lane * 16) = d[n];
    asm volatile("s_waitcnt lgkmcnt(0)" ::: "memory");
    if (lane == 0) __hip_atomic_fetch_add((LAS unsigned*)(lds + B_PART), 1u, __ATOMIC_RELAXED, __HIP_MEMORY_SCOPE_WORKGROUP);
    if (wid >= 6) {
      float s = (U2F(sv[0][0]) + U2F(sv[1][0])) + (U2F(sv[2][0]) + U2F(sv[3][0]));
      float q = (U2F(sv[0][1]) + U2F(sv[1][1])) + (U2F(sv[2][1]) + U2F(sv[3][1]));
      s += shfl(s, lane ^ 16); q += shfl(q, lane ^ 16);
      s = half_add(s); q = half_add(q);
      const float mean = s * (1.f / 1024.f);
      const float rstd = __builtin_amdgcn_rsqf(__builtin_fmaxf(q * (1.f / 1024.f) - mean * mean, 0.f) + 1e-5f);
      f32x4 m4, r4;
      UNROLL for (int j = 0; j < 4; j++) {
        const float mj = shfl(mean, 4 * g4 + j), rj = shfl(rstd, 4 * g4 + j);
        m4[j] = ktype ? mj : mean; r4[j] = ktype ? rj * 0x1p64f : rstd;
      }
      _Pragma("nounroll") for (unsigned spins = 0; spins < SPIN_CAP * 16 && pcnt[0] != 4u; spins++) __builtin_amdgcn_s_sleep(1);
      asm volatile("" ::: "memory");
      f32x4 e = -m4 * cs;
      UNROLL for (int w = 0; w < 4; w++) e += *(LAS f32x4*)(lds + B_PART + 1024 + (w * 2 + u) * 1024 + lane * 16);
      e *= r4;
      const int frag = ktype ? 2 + kth : u, lp = (ktype ? 16 * u : 16 * kth) + c16 + 32 * (g4 & 1);
      const unsigned e0 = cvtpk(e[0], e[1]), e1 = cvtpk(e[2], e[3]);
      const auto w0 = __builtin_amdgcn_permlane32_swap(e0, e0, false, false), w1 = __builtin_amdgcn_permlane32_swap(e1, e1, false, false);
      if (lane < 32) st16(x.rs, (u32x4){w0[0], w1[0], w0[1], w1[1]}, frag * 1024 + lp * 16, kvf + krt * 4096);
      if (last_arriver(lds + B_PART + 64, 2, lane)) set_flag(ws, 10 + L, x.grp * 32 + x.idx, lane);
    }
    __builtin_amdgcn_s_setprio(0);
    {
      STAMP(ph, 1);
      if (wid == 7) {
        wait_flags(ws, 10 + L, x.grp * 32 + (lane & 31));
        STAMP(ph, 2);
        UNROLL for (int i = 0; i < 8; i++) kv_dma(0, i * 4);
        kv_ok = kv_all();
        drain_vm();
        STAMP(ph, 7);
      }
    }
    __builtin_amdgcn_s_setprio(0);
    bar_only();
    if (wid == 7) *(LAS unsigned*)(lds + B_QX + 8192) = kv_ok;
    lgkm_bar();
    UNROLL for (int s = 0; s < 2; s++) qf[s] = lds_b128(lds + B_QX + ((wid & 3) * 2 + s) * 1024 + lane * 16);
  }
  __builtin_amdgcn_s_waitcnt(0x0F70);
  const bool kv_early = __builtin_amdgcn_readfirstlane(*(LAS unsigned*)(lds + B_QX + 8192)) != 0;
  STAMP(ph, 3); STAMPC(ph, 14);
  if (kv_early) kv_dma(1, wid * 4);
  const bf16x8 sel = sel_frag(lane);
  const f32x16 zero = {};
  f32x16 O = zero, negm, SA, SB_;
  f32x4 LS = {};
  bf16x8 kf[2], vf[2];
  const LdsLane ll(lds, lane);
  kf[0] = ll.rd(kv_at(0)); kf[1] = ll.rd(kv_at(0) + 1024);
  SA = MFMA32(kf[0], qf[0], zero); SA = MFMA32(kf[1], qf[1], SA);
  kf[0] = ll.rd(kv_at(1)); kf[1] = ll.rd(kv_at(1) + 1024);
  vf[0] = ll.rd(kv_at(0) + 2048); vf[1] = ll.rd(kv_at(0) + 3072);
  {
    float m = SA[0];
    UNROLL for (int i = 1; i < 16; i++) m = __builtin_fmaxf(m, SA[i]);
    m = half_max(m) + 100.f;
    UNROLL for (int i = 0; i < 16; i++) { negm[i] = -m; SA[i] -= m; }
  }
#define EX(i) Sc[i] = __builtin_amdgcn_exp2f(Sc[i])
#define PIN(x) asm volatile("" : "+v"(x))
  u32x4 p0, p1 = {0u, 0u, 0u, 0u};
  auto step = [&](f32x16& Sc, f32x16& Sn, int kp, int vc, int vp) AI {
    SB();
    Sn = MFMA32(kf[0], qf[0], negm);
    SB();
    kf[0] = ll.rd(kp);
    EX(0); EX(1); EX(2); PIN(Sc);
    SB();
    O = MFMA32(vf[1], __builtin_bit_cast(bf16x8, p1), O);
    SB();
    vf[1] = ll.rd(vc + 3072);
    EX(3); EX(4); EX(5); PIN(Sc);
    SB();
    Sn = MFMA32(kf[1], qf[1], Sn);
    SB();
    kf[1] = ll.rd(kp + 1024);
    EX(6); EX(7);
    p0[0] = cvtpk(Sc[0], Sc[1]); p0[1] = cvtpk(Sc[2], Sc[3]); p0[2] = cvtpk(Sc[4], Sc[5]); p0[3] = cvtpk(Sc[6], Sc[7]); PIN(p0);
    SB();
    LS = MFMA16(sel, __builtin_bit_cast(bf16x8, p1), LS);
    SB();
    EX(8); EX(9); PIN(Sc);
    SB();
    O = MFMA32(vf[0], __builtin_bit_cast(bf16x8, p0), O);
    SB();
    vf[0] = ll.rd(vp + 2048);
    EX(10); EX(11); EX(12); PIN(Sc);
    SB();
    LS = MFMA16(sel, __builtin_bit_cast(bf16x8, p0), LS);
    SB();
    EX(13); EX(14); EX(15);
    p1[0] = cvtpk(Sc[8], Sc[9]); p1[1] = cvtpk(Sc[10], Sc[11]); p1[2] = cvtpk(Sc[12], Sc[13]); p1[3] = cvtpk(Sc[14], Sc[15]); PIN(p1);
    SB();
  };
  auto hook = [&](int t) AI {
    if (t < 2) { if (kv_early) kv_dma(2 + t, wid * 4); }
    if (t == 2 && !kv_ok) {
      _Pragma("nounroll") for (unsigned spins = 0; spins < SPIN_CAP && !kv_all(); spins++) asm volatile("s_sleep 4" ::: "memory");
      _Pragma("nounroll") for (int i = 0; i < 8; i++) { kv_dma(1, i * 4); kv_dma(2, i * 4); kv_dma(3, i * 4); }
    }
    if (t == 6) wait_vm_bar<0>();
    if (t == 8 || t == 10) kv_dma2(4, wid * 4 + t - 8);
    if (t == 25) wait_vm_bar<0>();
    if (t >= 25 && t < 31) kv_dma2(5 + (t - 25) / 2, wid * 4 + ((t - 25) & 1) * 2);
    if (t == 38) wait_vm_bar<0>();
    if (t == 39 || t == 41) dma_w64<2, 2>(x, (t - 39) / 2, wid * 2);
    if (t == 49) bar_only();
    if (t >= 50 && t < 58 && !(t & 1)) dma_w64<2, 2>(x, 2 + (t - 50) / 2, wid * 2);
  };
  static_for<32>([&](auto tc) AI {
    constexpr int t = decltype(tc)::value * 2;
    hook(t);
    step(SA, SB_, kv_at((t + 2) & 63), kv_at(t), kv_at(t + 1));
    hook(t + 1);
    step(SB_, SA, kv_at((t + 3) & 63), kv_at(t + 1), kv_at((t + 2) & 63));
  });
  O = MFMA32(vf[1], __builtin_bit_cast(bf16x8, p1), O);
  LS = MFMA16(sel, __builtin_bit_cast(bf16x8, p1), LS);
  STAMP(ph, 4); STAMPC(ph, 15);
  float l = LS[0];
  {
    float chk = l;
    UNROLL for (int i = 0; i < 16; i++) chk = __builtin_fmaxf(chk, __builtin_fabsf(O[i]) * 0x1p-27f);
    bool bad = !(chk < 0x1p100f);
    UNROLL for (int i = 0; i < 16; i++) bad |= (O[i] != O[i]);
    if (any_lane(bad)) {
      O = zero; l = 0.f;
      float m = -__builtin_inff();
      for (int t = 0; t < 64; t++) {
        bf16x8 f[4];
        UNROLL for (int i = 0; i < 4; i++) f[i] = __builtin_bit_cast(bf16x8, ld16(x.rs, i * 1024 + lane * 16, kvf + t * 4096));
        f32x16 s = MFMA32(f[0], qf[0], zero);
        s = MFMA32(f[1], qf[1], s);
        float mt = s[0];
        UNROLL for (int i = 1; i < 16; i++) mt = __builtin_fmaxf(mt, s[i]);
        const float mn = __builtin_fmaxf(m, half_max(mt));
        const float al = __builtin_amdgcn_exp2f(m - mn);
        m = mn;
        UNROLL for (int i = 0; i < 16; i++) { s[i] = __builtin_amdgcn_exp2f(s[i] - mn); O[i] *= al; }
        const bf16x8 p0 = pack8(s, 0), p1 = pack8(s, 1);
        f32x4 lt = MFMA16(sel, p0, ((f32x4){0.f, 0.f, 0.f, 0.f}));
        lt = MFMA16(sel, p1, lt);
        l = l * al + lt[0];
        O = MFMA32(f[2], p0, O);
        O = MFMA32(f[3], p1, O);
      }
    }
  }
  const float rl = (1.f / l) * 0x1p-64f;
  UNROLL for (int gp = 0; gp < 2; gp++) {
    float v[8];
    gather8(O, gp, v);
    st16(x.rs, (u32x4){cvtpk(v[0] * rl, v[1] * rl), cvtpk(v[2] * rl, v[3] * rl), cvtpk(v[4] * rl, v[5] * rl), cvtpk(v[6] * rl, v[7] * rl)},
         gp * 1024 + lane * 16, act_off(ph + 1) + rt * 65536 + head * 2048);
  }
  asm volatile("s_waitcnt vmcnt(0) lgkmcnt(0)\n\ts_barrier" ::: "memory");
  if (wid == 7) set_flag(ws, ph + 1, id0 + pj, lane);
  STAMP(ph, 5);
}

__global__ __launch_bounds__(512, 2) void k_main(char* ws, char* out) {
  extern __shared__ __attribute__((aligned(16))) char lds_raw[];
  Ctx x;
  x.ws = ws;
  x.rs = __builtin_amdgcn_make_buffer_rsrc(ws, 0, WS_USED, 0x00020000);
  x.lds = (lds_ptr)lds_raw;
  x.l0 = (unsigned)(uintptr_t)x.lds;
  x.lane = TID & 63;
  x.wid = __builtin_amdgcn_readfirstlane(TID >> 6);
  x.grp = BID & 7;
  x.idx = BID >> 3;
  x.hi = x.lane >> 5;
  UNROLL for (int c = 0; c < 2; c++) dma_wqkv(x, c, x.wid);
  u32x4 res[4] = {};
  for (int L = 0; L < 2; L++) {
    phase_a(fresh(x, 0), L, res);
    phase_out_a_b(fresh(x, 0), L, res);
    phase_out_b(fresh(x, 0), L, res, out);
  }
}

#if defined(__HIP_DEVICE_COMPILE__)
#pragma clang attribute pop
#endif

extern "C" void kernel_launch(void* const* d_in, const int* in_sizes, int n_in, void* d_out, int out_size, void* d_ws, size_t ws_size,
                              hipStream_t stream) {
  char* ws = (char*)d_ws;
  (void)hipFuncSetAttribute((const void*)k_main, hipFuncAttributeMaxDynamicSharedMemorySize, LDS_ALL);
  (void)hipFuncSetAttribute((const void*)k_prep, hipFuncAttributeMaxDynamicSharedMemorySize, PREP_LDS);
  k_prep<<<296, 512, PREP_LDS, stream>>>((const char*)d_in[0], (const char*)d_in[1], (const u16*)d_in[2], (const char*)d_in[3], (const char*)d_in[5],
                                         (const u16*)d_in[6], (const u16*)d_in[7], (const char*)d_in[8], ws);
  k_main<<<256, 512, LDS_ALL, stream>>>(ws, (char*)d_out);
}
```

```cpp
#include <hip/hip_runtime.h>
#include <hip/hip_bf16.h>
#include <stdint.h>

typedef unsigned short u16;
using bf16x8 = __attribute__((ext_vector_type(8))) short;
using s16x4 = __attribute__((ext_vector_type(4))) short;
using f32x16 = __attribute__((ext_vector_type(16))) float;
using f32x4 = __attribute__((ext_vector_type(4))) float;
using f32x2 = __attribute__((ext_vector_type(2))) float;
using u32x4 = __attribute__((ext_vector_type(4))) unsigned;
using u32x2 = __attribute__((ext_vector_type(2))) unsigned;
typedef __bf16 bf16x2_t __attribute__((ext_vector_type(2)));
#define LAS __attribute__((address_space(3)))
#define GAS __attribute__((address_space(1)))
typedef LAS char* lds_ptr;
typedef short v4i16_t __attribute__((ext_vector_type(4)));
#define DI __device__ __forceinline__
#define MFMA32(a, b, c) __builtin_amdgcn_mfma_f32_32x32x16_bf16((a), (b), (c), 0, 0, 0)
#define MFMA16(a, b, c) __builtin_amdgcn_mfma_f32_16x16x32_bf16((a), (b), (c), 0, 0, 0)
#define SB() __builtin_amdgcn_sched_barrier(0)
#define RLX_AGENT __ATOMIC_RELAXED, __HIP_MEMORY_SCOPE_AGENT
#define UNROLL _Pragma("unroll")
#define AI __attribute__((always_inline))

constexpr float LOG2E = 1.4426950408889634f;
constexpr unsigned SPIN_CAP = 1u << 20;

constexpr unsigned MBy = 1u << 20;
constexpr unsigned OFF_FL = 768 * 1024;
constexpr unsigned OFF_CSKV = 120 * 1024;
constexpr unsigned OFF_CS = 512 * 1024;
constexpr unsigned OFF_WKVF = 128 * 1024;
constexpr unsigned OFF_AUX = 256 * 1024;
constexpr unsigned OFF_ST = 1 * MBy;
constexpr unsigned OFF_KVF = 2 * MBy;
constexpr unsigned OFF_SLAB = 4 * MBy;
constexpr unsigned SLAB_B = 8448;
constexpr unsigned OFF_ACT = 16 * MBy;
constexpr unsigned OFF_WTA = 48 * MBy;
constexpr unsigned OFF_WT64 = 54 * MBy;
constexpr unsigned WS_USED = 60 * MBy;

#if defined(__HIP_DEVICE_COMPILE__)
#pragma clang attribute push(__attribute__((target("no-packed-fp32-ops"))), apply_to = function)
#endif
#define U2F(x) __builtin_bit_cast(float, (unsigned)(x))
#define F2U(x) __builtin_bit_cast(unsigned, (float)(x))
#define TID ((int)__builtin_amdgcn_workitem_id_x())
#define BID ((int)__builtin_amdgcn_workgroup_id_x())
DI bool all_lanes(bool p) { return __builtin_amdgcn_ballot_w64(!p) == 0; }
DI bool any_lane(bool p) { return __builtin_amdgcn_ballot_w64(p) != 0; }
DI float shfl(float v, int src) { return U2F(__builtin_amdgcn_ds_bpermute(src * 4, (int)F2U(v))); }
#define STAMP(k, i)
#define STAMPC(k, i)
DI void glds16(const void* sbase, unsigned voff, unsigned lds_base) {
  unsigned sv;
  asm volatile("s_mov_b32 %0, m0\n\ts_mov_b32 m0, %3\n\ts_nop 0\n\tglobal_load_lds_dwordx4 %1, %2\n\ts_mov_b32 m0, %0"
               : "=&s"(sv) : "v"(voff), "s"(sbase), "s"(lds_base) : "memory");
}
#define GL_(o, sc) "global_load_lds_dwordx4 %1, %2 offset:" #o sc "\n\t"
#define GLDS_N(NAME, SC)                                                                                                   \
  template <int N> DI void NAME(const void* sbase, unsigned voff, unsigned lds_base) {                                     \
    unsigned sv;                                                                                                           \
    if constexpr (N == 1)                                                                                                  \
      asm volatile("s_mov_b32 %0, m0\n\ts_mov_b32 m0, %3\n\ts_nop 0\n\t" GL_(0, SC) "s_mov_b32 m0, %0"                    \
                   : "=&s"(sv) : "v"(voff), "s"(sbase), "s"(lds_base) : "memory");                                         \
    else if constexpr (N == 2)                                                                                                  \
      asm volatile("s_mov_b32 %0, m0\n\ts_mov_b32 m0, %3\n\ts_nop 0\n\t" GL_(0, SC) GL_(1024, SC) "s_mov_b32 m0, %0"       \
                   : "=&s"(sv) : "v"(voff), "s"(sbase), "s"(lds_base) : "memory");                                         \
    else if constexpr (N == 3)                                                                                             \
      asm volatile("s_mov_b32 %0, m0\n\ts_mov_b32 m0, %3\n\ts_nop 0\n\t" GL_(0, SC) GL_(1024, SC) GL_(2048, SC) "s_mov_b32 m0, %0" \
                   : "=&s"(sv) : "v"(voff), "s"(sbase), "s"(lds_base) : "memory");                                         \
    else                                                                                                                   \
      asm volatile("s_mov_b32 %0, m0\n\ts_mov_b32 m0, %3\n\ts_nop 0\n\t" GL_(0, SC) GL_(1024, SC) GL_(2048, SC) GL_(3072, SC) "s_mov_b32 m0, %0" \
                   : "=&s"(sv) : "v"(voff), "s"(sbase), "s"(lds_base) : "memory");                                         \
  }
GLDS_N(gldsN, "")
GLDS_N(gldsN_sc1, " sc1")
DI void glds16_sc1(const void* sbase, unsigned voff, unsigned lds_base) {
  unsigned sv;
  asm volatile("s_mov_b32 %0, m0\n\ts_mov_b32 m0, %3\n\ts_nop 0\n\tglobal_load_lds_dwordx4 %1, %2 sc1\n\ts_mov_b32 m0, %0"
               : "=&s"(sv) : "v"(voff), "s"(sbase), "s"(lds_base) : "memory");
}
template <int N> DI void wait_vm_bar() { asm volatile("s_waitcnt vmcnt(%0)\n\ts_barrier" ::"n"(N) : "memory"); }
DI void bar_only() { asm volatile("s_barrier" ::: "memory"); }
DI void lgkm_bar() { asm volatile("s_waitcnt lgkmcnt(0)\n\ts_barrier" ::: "memory"); }
DI void drain_vm() { asm volatile("s_waitcnt vmcnt(0)" ::: "memory"); }
DI s16x4 lds_tr(lds_ptr p) { return __builtin_bit_cast(s16x4, __builtin_amdgcn_ds_read_tr16_b64_v4i16((LAS v4i16_t*)p)); }
template <int E> DI bf16x8 lds_trfrag(lds_ptr p) { s16x4 lo = lds_tr(p), hi = lds_tr(p + E); return __builtin_shufflevector(lo, hi, 0, 1, 2, 3, 4, 5, 6, 7); }
DI bf16x8 lds_b128(lds_ptr p) { return *(LAS bf16x8*)p; }
DI unsigned cvtpk(float lo, float hi) {
  bf16x2_t v = __builtin_convertvector((f32x2){lo, hi}, bf16x2_t);
  return __builtin_bit_cast(unsigned, v);
}
DI float bflo(unsigned u) { return U2F(u << 16); }
DI float bfhi(unsigned u) { return U2F(u & 0xffff0000u); }
DI float dot2(unsigned a, unsigned b, float c) {
  return __builtin_amdgcn_fdot2_f32_bf16(__builtin_bit_cast(bf16x2_t, a), __builtin_bit_cast(bf16x2_t, b), c, false);
}
DI float half_add(float x) { auto r = __builtin_amdgcn_permlane32_swap(F2U(x), F2U(x), false, false); return U2F(r[0]) + U2F(r[1]); }
DI float half_max(float x) { auto r = __builtin_amdgcn_permlane32_swap(F2U(x), F2U(x), false, false); return __builtin_fmaxf(U2F(r[0]), U2F(r[1])); }
DI float half_min(float x) { auto r = __builtin_amdgcn_permlane32_swap(F2U(x), F2U(x), false, false); return __builtin_fminf(U2F(r[0]), U2F(r[1])); }
DI bf16x8 pack8(const f32x16& x, int s) {
  u32x4 p = {cvtpk(x[8 * s], x[8 * s + 1]), cvtpk(x[8 * s + 2], x[8 * s + 3]), cvtpk(x[8 * s + 4], x[8 * s + 5]), cvtpk(x[8 * s + 6], x[8 * s + 7])};
  return __builtin_bit_cast(bf16x8, p);
}
DI void gather8(const f32x16& a, int gp, float (&v)[8]) {
  UNROLL for (int j = 0; j < 4; j++) {
    auto s = __builtin_amdgcn_permlane32_swap(F2U(a[8 * gp + j]), F2U(a[8 * gp + 4 + j]), false, false);
    v[j] = U2F(s[0]); v[4 + j] = U2F(s[1]);
  }
}
typedef __amdgpu_buffer_rsrc_t rsrc_t;
DI u32x4 ld16(rsrc_t rs, unsigned voff, unsigned soff) { return __builtin_amdgcn_raw_buffer_load_b128(rs, voff, soff, 16); }
DI unsigned ld4(rsrc_t rs, unsigned voff, unsigned soff) { return __builtin_amdgcn_raw_buffer_load_b32(rs, voff, soff, 16); }
DI void st16(rsrc_t rs, u32x4 v, unsigned voff, unsigned soff) { __builtin_amdgcn_raw_buffer_store_b128(v, rs, voff + soff, 0, 16); }
DI void st8(rsrc_t rs, u32x2 v, unsigned voff, unsigned soff) { __builtin_amdgcn_raw_buffer_store_b64(v, rs, voff, soff, 16); }
DI void st4(rsrc_t rs, unsigned v, unsigned voff, unsigned soff) { __builtin_amdgcn_raw_buffer_store_b32(v, rs, voff, soff, 16); }
constexpr unsigned EMPTY = 0xffffffffu;
DI u32x4 not_empty(f32x4 v) {
  u32x4 r;
  UNROLL for (int j = 0; j < 4; j++) r[j] = F2U(v[j]) < EMPTY ? F2U(v[j]) : EMPTY - 1;
  return r;
}
DI unsigned flag_at(int slot, int id) { return OFF_FL + (id >> 2) * 4096 + slot * 128 + (id & 3) * 4; }
DI void set_flag(char* ws, int slot, int id, int lane) {
  if (lane == 0) __hip_atomic_store((GAS unsigned*)(ws + flag_at(slot, id)), 1u, RLX_AGENT);
}
DI void wait_flags(char* ws, int slot, int id) {
  const unsigned off = flag_at(slot, id);
  _Pragma("nounroll") for (unsigned spins = 0; spins < SPIN_CAP; spins++) {
    const unsigned v = __hip_atomic_load((GAS unsigned*)(ws + off), RLX_AGENT);
    if (all_lanes(v == 1u)) break;
    __builtin_amdgcn_s_sleep(1);
  }
  asm volatile("" ::: "memory");
}
DI bool last_arriver(lds_ptr cnt, unsigned n, int lane) {
  drain_vm();
  unsigned old = 0;
  if (lane == 0) old = __hip_atomic_fetch_add((LAS unsigned*)cnt, 1u, __ATOMIC_RELAXED, __HIP_MEMORY_SCOPE_WORKGROUP);
  return (unsigned)__builtin_amdgcn_readfirstlane(old) == n - 1;
}
DI float swap16_add(float a, float b) {
  auto r = __builtin_amdgcn_permlane16_swap(F2U(a), F2U(b), false, false);
  return U2F(r[0]) + U2F(r[1]);
}
DI void stats_load(rsrc_t rs, unsigned base, int lane, u32x4 (&v)[4]) {
  UNROLL for (int i = 0; i < 4; i++) v[i] = ld16(rs, lane * 16 + i * 1024, base);
}
DI u32x4 stats_pair(float s, float q) {
  auto a = __builtin_amdgcn_permlane16_swap(F2U(s), F2U(s), false, false), b = __builtin_amdgcn_permlane16_swap(F2U(q), F2U(q), false, false);
  return (u32x4){a[0], b[0], a[1], b[1]};
}
DI void stats_finish(const u32x4 (&v)[4], int half, float& mean, float& rstd) {
  float t[4];
  UNROLL for (int j = 0; j < 4; j++) t[j] = (U2F(v[0][j]) + U2F(v[1][j])) + (U2F(v[2][j]) + U2F(v[3][j]));
  if (half == 0) { t[2] = t[0]; t[3] = t[1]; }
  if (half == 1) { t[0] = t[2]; t[1] = t[3]; }
  const float s = half_add(swap16_add(t[0], t[2])), q = half_add(swap16_add(t[1], t[3]));
  mean = s * (1.f / 1024.f);
  rstd = __builtin_amdgcn_rsqf(__builtin_fmaxf(q * (1.f / 1024.f) - mean * mean, 0.f) + 1e-5f);
}
DI void split3(float x, unsigned& a, unsigned& b, unsigned& c) {
  a = cvtpk(x, x) & 0xffffu; x -= bflo(a);
  b = cvtpk(x, x) & 0xffffu; x -= bflo(b);
  c = cvtpk(x, x) & 0xffffu;
}
DI bf16x8 cs_frag(float cs, int hi) {
  unsigned a, b, c; split3(cs, a, b, c);
  u32x4 p = {a | (b << 16), a | (c << 16), b | (a << 16), 0u};
  if (hi) p = (u32x4){0u, 0u, 0u, 0u};
  return __builtin_bit_cast(bf16x8, p);
}
DI bf16x8 mean_frag(float mean) {
  unsigned a, b, c; split3(-mean, a, b, c);
  u32x4 p = {a | (a << 16), b | (a << 16), b | (c << 16), 0u};
  return __builtin_bit_cast(bf16x8, p);
}
constexpr int ramp(int s, int I0, int PD) { int n = I0 + 2 * (s + 1); if (n > s + 1 + PD) n = s + 1 + PD; return n > 64 ? 64 : n; }
template <int V> struct IC { static constexpr int value = V; };
template <int N, int I = 0, class F> DI void static_for(F&& f) {
  if constexpr (I < N) { f(IC<I>{}); static_for<N, I + 1>(f); }
}
template <bool RATIO> DI float dual_softmax(const f32x16& p, float c, f32x16& ep, f32x16& em) {
  float mx = p[0], mn = p[0];
  UNROLL for (int i = 1; i < 16; i++) { mx = __builtin_fmaxf(mx, p[i]); mn = __builtin_fminf(mn, p[i]); }
  mx = half_max(mx); mn = half_min(mn);
  const float a = -mx * c, b = mn * c;
  float sp = 0.f, sm = 0.f;
  UNROLL for (int i = 0; i < 16; i++) {
    ep[i] = __builtin_amdgcn_exp2f(__builtin_fmaf(p[i], c, a));
    em[i] = __builtin_amdgcn_exp2f(__builtin_fmaf(p[i], -c, b));
    sp += ep[i]; sm += em[i];
  }
  sp = half_add(sp); sm = __builtin_amdgcn_rcpf(half_add(sm));
  if (RATIO) { sm *= sp; UNROLL for (int i = 0; i < 16; i++) em[i] *= sm; }
  else { const float rp = __builtin_amdgcn_rcpf(sp); UNROLL for (int i = 0; i < 16; i++) { ep[i] *= rp; em[i] *= sm; } }
  return sp;
}
DI bf16x8 sel_frag(int lane) {
  short v = ((lane & 3) == 0 && ((lane >> 2) & 1) == ((lane >> 4) & 1)) ? (short)0x3f80 : (short)0;
  return (bf16x8){v, v, v, v, v, v, v, v};
}

constexpr int MAIN_CODE_BYTES = 32 * 1024;
__global__ __launch_bounds__(512) void k_prep(const char* __restrict__ x0, const char* __restrict__ wqkv, const u16* __restrict__ wphi,
                                              const char* __restrict__ woA, const char* __restrict__ wq, const u16* __restrict__ wk,
                                              const u16* __restrict__ wv, const char* __restrict__ woB, char* __restrict__ ws) {
  extern __shared__ __attribute__((aligned(16))) char lds_raw[];
  const lds_ptr lds = (lds_ptr)lds_raw;
  const int tid = TID, lane = tid & 63, wid = __builtin_amdgcn_readfirstlane(tid >> 6), bid = BID;
  const int r = lane & 31, hi = lane >> 5;
  if (bid < 256) {
    const u32x4 e = {EMPTY, EMPTY, EMPTY, EMPTY};
    *(GAS u32x4*)(ws + OFF_SLAB + bid * SLAB_B + tid * 16) = e;
    if (tid < 16) *(GAS u32x4*)(ws + OFF_SLAB + bid * SLAB_B + 8192 + tid * 16) = e;
  }
  if (bid < 192) {
    int kb, cb, ld, which;
    const char* W;
    if (bid < 96) { const int t = bid >> 3, g = bid & 7; cb = (t % 3) * 4 + (g >> 1); kb = (t / 3) * 2 + (g & 1); ld = 6144; W = wqkv; which = -1; }
    else { const int t = bid - 96; which = t >> 5; kb = (t >> 2) & 7; cb = t & 3; ld = 2048; W = which == 0 ? woA : (which == 1 ? wq : woB); }
    const unsigned l0 = (unsigned)(uintptr_t)lds + wid * 8192;
    UNROLL for (int i = 0; i < 8; i++) glds16(W + (size_t)(kb * 128 + wid * 16 + i * 2) * ld + cb * 512, hi * ld + r * 16, l0 + i * 1024);
    drain_vm();
    const int step = kb * 8 + wid;
    const lds_ptr p = lds + wid * 8192 + (8 * hi + ((lane & 15) >> 2)) * 512 + ((lane >> 4) & 1) * 32 + (lane & 3) * 8;
    float cs[8];
    UNROLL for (int j = 0; j < 8; j++) {
      const u32x4 fr = __builtin_bit_cast(u32x4, lds_trfrag<2048>(p + j * 64));
      unsigned dst;
      if (which < 0) dst = OFF_WTA + (((cb & 3) * 8 + j) * 192 + step * 3 + (cb >> 2)) * 1024;
      else dst = OFF_WT64 + which * 2 * MBy + ((cb * 4 + (j >> 1)) * 128 + step * 2 + (j & 1)) * 1024;
      *(GAS u32x4*)(ws + dst + lane * 16) = fr;
      float c = 0.f;
      UNROLL for (int e = 0; e < 4; e++) c = dot2(fr[e], 0x3f803f80u, c);
      cs[j] = half_add(c);
    }
    if (which < 0 || which == 1) {
      LAS float* red = (LAS float*)(lds + 65536);
      if (hi == 0) UNROLL for (int j = 0; j < 8; j++) red[wid * 256 + j * 32 + r] = cs[j];
      lgkm_bar();
      if (tid < 256) {
        float t = 0.f;
        UNROLL for (int w = 0; w < 8; w++) t += red[w * 256 + tid];
        if (which < 0) *(GAS float*)(ws + OFF_AUX + ((cb & 3) * 8 + (tid >> 5)) * 5120 + (kb * 96 + (cb >> 2) * 32 + (tid & 31)) * 4) = t;
        else *(GAS float*)(ws + OFF_CS + (kb * 1024 + cb * 256 + tid) * 4) = t;
      }
    }
  } else if (bid < 256) {
    const int t = bid - 192, rt = (t & 1) * 32 + (t >> 1);
    u32x4 v[2][4];
    UNROLL for (int c = 0; c < 2; c++) UNROLL for (int i = 0; i < 4; i++)
      v[c][i] = *(const GAS u32x4*)(x0 + (size_t)(rt * 32 + r) * 2048 + (wid * 2 + c) * 128 + i * 32 + hi * 16);
    UNROLL for (int c = 0; c < 2; c++) {
      float s = 0, q = 0;
      UNROLL for (int i = 0; i < 4; i++) {
        *(GAS u32x4*)(ws + OFF_ACT + (size_t)rt * 65536 + ((wid * 2 + c) * 4 + i) * 1024 + lane * 16) = v[c][i];
        UNROLL for (int j = 0; j < 4; j++) { s = dot2(v[c][i][j], 0x3f803f80u, s); q = dot2(v[c][i][j], v[c][i][j], q); }
      }
      s = half_add(s); q = half_add(q);
      if (hi == 0) *(GAS f32x2*)(ws + OFF_ST + rt * 4096 + (wid * 2 + c) * 256 + (r & 15) * 16 + (r >> 4) * 8) = (f32x2){s, q};
    }
  } else if (bid < 264) {
    const int t = (bid - 256) * 512 + tid, h = t >> 7, s = (t >> 6) & 1;
    bf16x8 o;
    UNROLL for (int j = 0; j < 8; j++) o[j] = (short)wphi[h * 1024 + (16 * s + 8 * (j >> 2) + 4 * hi + (j & 3)) * 32 + r];
    *(GAS bf16x8*)(ws + OFF_AUX + h * 5120 + 3072 + (t & 127) * 16) = o;
  } else if (bid < 280) {
    const int t = (bid - 264) * 512 + tid, type = t >> 12, kk = (t >> 7) & 31, u = (t >> 6) & 1;
    const u16* W = type ? wv : wk;
    bf16x8 o;
    UNROLL for (int j = 0; j < 8; j++) o[j] = (short)W[(32 * kk + 8 * (lane >> 4) + j) * 32 + 16 * u + (lane & 15)];
    *(GAS bf16x8*)(ws + OFF_WKVF + (size_t)t * 16) = o;
  } else if (bid < 282) {
    const char* W = (const char*)(bid == 280 ? wk : wv);
    float a[8] = {0, 0, 0, 0, 0, 0, 0, 0};
    u32x4 v[8];
    UNROLL for (int i = 0; i < 8; i++) v[i] = *(const GAS u32x4*)(W + (wid * 8 + i) * 1024 + lane * 16);
    UNROLL for (int i = 0; i < 8; i++) UNROLL for (int j = 0; j < 4; j++) { a[2 * j] += bflo(v[i][j]); a[2 * j + 1] += bfhi(v[i][j]); }
    UNROLL for (int j = 0; j < 8; j++) { a[j] += shfl(a[j], lane ^ 4); a[j] += shfl(a[j], lane ^ 8); a[j] += shfl(a[j], lane ^ 16); a[j] += shfl(a[j], lane ^ 32); }
    LAS float* red = (LAS float*)lds;
    if (lane < 4) UNROLL for (int j = 0; j < 8; j++) red[wid * 32 + lane * 8 + j] = a[j];
    lgkm_bar();
    if (tid < 32) {
      float t = 0.f;
      UNROLL for (int w = 0; w < 8; w++) t += red[w * 32 + tid];
      ((float*)(ws + OFF_CSKV))[(bid - 280) * 32 + tid] = t;
    }
  } else if (bid >= 288) {
    unsigned long long pc;
    asm volatile("s_getpc_b64 s[40:41]\n\ts_add_u32 s40, s40, _Z6k_mainPcS_@rel32@lo+4\n\ts_addc_u32 s41, s41, _Z6k_mainPcS_@rel32@hi+12\n\ts_mov_b64 %0, s[40:41]"
                 : "=s"(pc) : : "s40", "s41", "scc");
    if (tid * 128 < MAIN_CODE_BYTES) { const unsigned v = *(const GAS unsigned*)(pc + tid * 128); asm volatile("" ::"v"(v)); }
  } else if (bid < 284) {
    const int t = (bid - 282) * 512 + tid;
    *(GAS u32x4*)(ws + OFF_FL + (t >> 4) * 4096 + (t & 15) * 128) = (u32x4){0u, 0u, 0u, 0u};
  }
}
constexpr int PREP_LDS = 65536 + 8192;

struct Ctx {
  char* ws; rsrc_t rs; lds_ptr lds; unsigned l0;
  int lane, wid, grp, idx, hi, ah, at;
};
DI Ctx fresh(const Ctx& x, int swap) {
  Ctx y = x;
  y.wid ^= swap;
  asm volatile("" : "+s"(y.wid), "+s"(y.grp), "+s"(y.idx), "+s"(y.ah), "+s"(y.at));
  asm volatile("" : "+v"(y.lane));
  y.lane &= 63;
  y.hi = y.lane >> 5;
  return y;
}
struct LdsLane {
  lds_ptr b0, b1, b2;
  DI LdsLane(lds_ptr lds, int lane) : b0(lds + lane * 16), b1(b0 + 65536), b2(b0 + 131072) { asm volatile("" : "+v"(b0), "+v"(b1), "+v"(b2)); }
  DI bf16x8 rd(int off) const { return lds_b128((off < 65536 ? b0 : (off < 131072 ? b1 : b2)) + (off & 65535)); }
};
constexpr int LDS_ALL = 163840;
constexpr int A_SLOT = 24576, A_ROW = 200, A_IMG = 106496, A_KVL = A_IMG + 256 * A_ROW, A_ZL = A_KVL + 4096, A_AUX = 6 * A_SLOT;
static_assert(A_ZL + 128 <= LDS_ALL, "");
DI unsigned act_off(int i) { return OFF_ACT + i * 4 * MBy; }

DI int a_head(const Ctx& x) { return x.ah; }
DI int a_tile(const Ctx& x) { return x.at; }
DI void dma_wqkv(const Ctx& x, int c, int pg) {
  gldsN<3>(x.ws + OFF_WTA + a_head(x) * 196608 + c * A_SLOT + pg * 3072, x.lane * 16, x.l0 + (c % 6) * A_SLOT + pg * 3072);
}
constexpr int w64_at(int which, int c) {
  constexpr int t[3][8] = {{48, 64, 80, 0, 16, 32, 96, 112}, {128, 144, 48, 64, 80, 0, 16, 32}, {32, 48, 64, 80, 96, 112, 128, 144}};
  return t[which][c] * 1024;
}
template <int WHICH, int N> DI void dma_w64(const Ctx& x, int c, int p) {
  gldsN<N>(x.ws + OFF_WT64 + WHICH * 2 * MBy + (x.idx >> 1) * 131072 + c * 16384 + p * 1024, x.lane * 16, x.l0 + w64_at(WHICH, c) + p * 1024);
}
DI float colsum_half(const Ctx& x, int col) {
  const GAS float* p = (const GAS float*)(x.ws + OFF_CS) + x.hi * 4 * 1024 + col;
  return (p[0] + p[1024]) + (p[2 * 1024] + p[3 * 1024]);
}

DI void phase_a(const Ctx& x, int L, u32x4 (&res)[4]) {
  char* ws = x.ws;
  const int lane = x.lane, wid = x.wid, hi = x.hi, h = a_head(x), tt = a_tile(x), r = lane & 31, rt = tt * 8 + wid;
  const lds_ptr lds = x.lds;
  constexpr int NSTEP = 64, PD = 16, I0 = 6;
  STAMP(4 * L, 0);
  auto aux = [&]() AI { if (wid < 5) gldsN<1>(ws + OFF_AUX + h * 5120 + wid * 1024, lane * 16, x.l0 + A_AUX + wid * 1024); };
  float csv[3][4];
  bf16x8 wph[2], csf[3], mf;
  float rstd;
  if (L) {
    if (wid == 0) wait_flags(ws, 4 * L, tt * 32 + (lane & 31));
    UNROLL for (int c = 4; c < 6; c++) dma_wqkv(x, c, wid);
    aux();
    wait_vm_bar<6>();
  }

  STAMP(4 * L, 10);
  const unsigned abase = act_off(4 * L) + rt * 65536;
  auto aload = [&](int s) AI { return __builtin_bit_cast(bf16x8, ld16(x.rs, lane * 16 + (s & 3) * 1024, abase + (s >> 2) * 4096)); };
  bf16x8 a[PD];
  UNROLL for (int s = 0; s < I0; s++) a[s] = aload(s);
  f32x16 acc[3] = {{}, {}, {}};
  const int q4 = (lane & 15) >> 2;
  bf16x8 bf[2][3];
  const LdsLane ll(lds, lane);
  auto bread = [&](int s) AI { UNROLL for (int i = s < 48 ? 0 : 1; i < 3; i++) bf[s & 1][i] = ll.rd(((s % 48) * 3 + i) * 1024); };
  u32x4 stv[4];
  if (L == 0) {
    UNROLL for (int c = 2; c < 6; c++) dma_wqkv(x, c, wid);
    aux();
    wait_vm_bar<I0 + 12>();
  }
  STAMP(4 * L, 1); STAMPC(4 * L, 14);
  bread(0);
  static_for<NSTEP>([&](auto sc) AI {
    constexpr int s = decltype(sc)::value;
    if (s == 15) { wait_vm_bar<ramp(14, I0, PD) - I0>(); dma_wqkv(x, 6, wid); }
    if (s == 25) { bar_only(); dma_wqkv(x, 7, wid); dma_w64<0, 2>(x, 0, wid * 2); }
    if (s == 47) {
      asm volatile("s_waitcnt vmcnt(22) lgkmcnt(0)\n\ts_barrier" ::: "memory");
      dma_w64<0, 2>(x, 1, wid * 2); dma_w64<0, 2>(x, 2, wid * 2);
    }
    if (s == 28) stats_load(x.rs, OFF_ST + 2 * L * 262144 + rt * 4096, lane, stv);
    if (s == 42) {
      UNROLL for (int i = 0; i < 3; i++) UNROLL for (int k = 0; k < 4; k++) csv[i][k] = *(LAS float*)(lds + A_AUX + ((hi * 4 + k) * 96 + i * 32 + r) * 4);
      UNROLL for (int i = 0; i < 2; i++) wph[i] = ll.rd(A_AUX + 3072 + i * 1024);
    }
    if (s == 44) {
      float mean;
      stats_finish(stv, -1, mean, rstd);
      mf = mean_frag(mean);
      UNROLL for (int i = 0; i < 3; i++) csf[i] = cs_frag(half_add((csv[i][0] + csv[i][1]) + (csv[i][2] + csv[i][3])), hi);
    }
    if (s == 45) UNROLL for (int i = 0; i < 3; i++) acc[i] = MFMA32(csf[i], mf, acc[i]);
    if (s == 48) {
      if (L == 0) UNROLL for (int i = 0; i < 4; i++)
        res[i] = ld16(x.rs, i * 1024 + lane * 16, act_off(0) + ((x.grp * 2 + (x.idx & 1)) * 4 + (wid & 3)) * 65536 + (x.idx >> 1) * 4096);
    }
    if (s + 1 < NSTEP) bread(s + 1);
    SB();
    const bf16x8 av = a[s % PD];
    UNROLL for (int i = s < 48 ? 0 : 1; i < 3; i++) acc[i] = MFMA32(bf[s & 1][i], av, acc[i]);
    if (s == 48) asm volatile("" : "+v"(acc[0]));
    if (s == 44) asm volatile("" : "+v"(rstd), "+v"(wph[0]), "+v"(wph[1]));
    SB();
    UNROLL for (int n = ramp(s - 1, I0, PD); n < ramp(s, I0, PD); n++) a[n % PD] = aload(n);
    SB();
  });
  STAMP(4 * L, 2); STAMPC(4 * L, 15);
  const f32x16 zero = {};
  {
    UNROLL for (int i = 0; i < 16; i++) acc[2][i] *= rstd;
    f32x16 p = MFMA32(wph[0], pack8(acc[1], 0), zero);
    p = MFMA32(wph[1], pack8(acc[1], 1), p);
    const lds_ptr row = lds + A_IMG + (wid * 32 + r) * A_ROW + hi * 8;
    UNROLL for (int g = 0; g < 4; g++) *(LAS u32x2*)(row + 128 + g * 16) = (u32x2){cvtpk(acc[2][4 * g], acc[2][4 * g + 1]), cvtpk(acc[2][4 * g + 2], acc[2][4 * g + 3])};
    f32x16 ep, em;
    dual_softmax<false>(p, rstd * LOG2E, ep, em);
    UNROLL for (int g = 0; g < 4; g++) {
      *(LAS u32x2*)(row + g * 16) = (u32x2){cvtpk(ep[4 * g], ep[4 * g + 1]), cvtpk(ep[4 * g + 2], ep[4 * g + 3])};
      *(LAS u32x2*)(row + 64 + g * 16) = (u32x2){cvtpk(em[4 * g], em[4 * g + 1]), cvtpk(em[4 * g + 2], em[4 * g + 3])};
    }
  }
  STAMP(4 * L, 3);
  lgkm_bar();
  STAMP(4 * L, 4);
  const unsigned slab = OFF_SLAB + (L * 256 + h * 8) * SLAB_B;
  {
    __builtin_amdgcn_s_setprio(3);
    const int ft = wid >> 1, dt = wid & 1;
    const lds_ptr ip = lds + A_IMG + (8 * (lane >> 4) + q4) * A_ROW + (lane & 3) * 8;
    bf16x8 pf[8], vf[8];
    UNROLL for (int u = 0; u < 8; u++) { pf[u] = lds_trfrag<4 * A_ROW>(ip + u * 32 * A_ROW + ft * 32); vf[u] = lds_trfrag<4 * A_ROW>(ip + u * 32 * A_ROW + 128 + dt * 32); }
    SB();
    f32x4 kv = {}, z = {};
    UNROLL for (int u = 0; u < 8; u++) kv = MFMA16(pf[u], vf[u], kv);
    st16(x.rs, not_empty(kv), lane * 16, slab + tt * SLAB_B + wid * 1024);
    if (dt == 0) {
      const short one = (lane & 15) == 0 ? (short)0x3f80 : (short)0;
      const bf16x8 ones = {one, one, one, one, one, one, one, one};
      UNROLL for (int u = 0; u < 8; u++) z = MFMA16(pf[u], ones, z);
      if ((lane & 15) == 0) st16(x.rs, not_empty(z), lane, slab + tt * SLAB_B + 8192 + ft * 64);
    }
  }
  __builtin_amdgcn_s_setprio(0);
  STAMP(4 * L, 5);
  u32x4 gv[8];
  unsigned gz;
  auto gather = [&]() AI {
    UNROLL for (int t = 0; t < 8; t++)
      asm volatile("global_load_dwordx4 %0, %1, %2 sc1" : "=v"(gv[t]) : "v"(lane * 16), "s"(ws + slab + t * SLAB_B + wid * 1024) : "memory");
    asm volatile("global_load_dword %0, %1, %2 sc1" : "=v"(gz) : "v"((lane >> 3) * SLAB_B + (lane & 7) * 4), "s"(ws + slab + 8192 + wid * 32) : "memory");
  };
#define GATHER_WAIT(N)                                                                                                                   \
  asm volatile("s_waitcnt vmcnt(" #N ")"                                                                                                 \
               : "+v"(gv[0]), "+v"(gv[1]), "+v"(gv[2]), "+v"(gv[3]), "+v"(gv[4]), "+v"(gv[5]), "+v"(gv[6]), "+v"(gv[7]), "+v"(gz)::"memory")
  bf16x8 phq[4];
  float qs;
  {
    bf16x8 qw[16];
    UNROLL for (int s = 0; s < 16; s++) qw[s] = ll.rd(s * 3072);
    SB();
    UNROLL for (int s = 0; s < 16; s++) acc[0] = MFMA32(qw[s], a[s], acc[0]);
    asm volatile("" : "+v"(acc[0]));
    gather();
    SB();
    f32x16 p = MFMA32(wph[0], pack8(acc[0], 0), zero);
    p = MFMA32(wph[1], pack8(acc[0], 1), p);
    f32x16 ep, em;
    qs = dual_softmax<true>(p, rstd * LOG2E, ep, em);
    phq[0] = pack8(ep, 0); phq[1] = pack8(ep, 1); phq[2] = pack8(em, 0); phq[3] = pack8(em, 1);
    asm volatile("" : "+v"(phq[0]), "+v"(phq[1]), "+v"(phq[2]), "+v"(phq[3]));
  }
  GATHER_WAIT(0);
  STAMP(4 * L, 6);
  _Pragma("nounroll") for (unsigned spins = 0; spins < SPIN_CAP; spins++) {
    unsigned m = gz;
    UNROLL for (int t = 0; t < 8; t++) UNROLL for (int j = 0; j < 4; j++) m = m > gv[t][j] ? m : gv[t][j];
    if (all_lanes(m != EMPTY)) break;
    asm volatile("s_sleep 2" ::: "memory");
    gather();
    GATHER_WAIT(0);
  }
  STAMP(4 * L, 7);
  {
    f32x4 s;
    UNROLL for (int j = 0; j < 4; j++)
      s[j] = ((U2F(gv[0][j]) + U2F(gv[1][j])) + (U2F(gv[2][j]) + U2F(gv[3][j]))) + ((U2F(gv[4][j]) + U2F(gv[5][j])) + (U2F(gv[6][j]) + U2F(gv[7][j])));
    *(LAS u32x2*)(lds + A_KVL + (wid >> 1) * 1024 + ((wid & 1) * 16 + (lane & 15) + ((lane >> 4) & 1) * 32) * 16 + hi * 8) = (u32x2){cvtpk(s[0], s[1]), cvtpk(s[2], s[3])};
    float zs = U2F(gz);
    zs += shfl(zs, lane ^ 8); zs += shfl(zs, lane ^ 16); zs = half_add(zs);
    if (lane < 8) *(LAS u16*)(lds + A_ZL + (wid * 8 + lane) * 2) = (u16)cvtpk(zs, zs);
  }
  lgkm_bar();
  STAMP(4 * L, 8);
  {
    f32x16 num = zero, den = zero;
    UNROLL for (int fs = 0; fs < 4; fs++) {
      const bf16x8 kf = lds_b128(lds + A_KVL + fs * 1024 + lane * 16);
      const s16x4 z0 = *(LAS s16x4*)(lds + A_ZL + fs * 32 + hi * 8), z1 = *(LAS s16x4*)(lds + A_ZL + fs * 32 + hi * 8 + 16);
      num = MFMA32(kf, phq[fs], num);
      den = MFMA32(__builtin_shufflevector(z0, z1, 0, 1, 2, 3, 4, 5, 6, 7), phq[fs], den);
    }
    const float rd = 1.f / (den[0] + 1e-6f * qs);
    UNROLL for (int gp = 0; gp < 2; gp++) {
      float v[8];
      gather8(num, gp, v);
      st16(x.rs, (u32x4){cvtpk(v[0] * rd, v[1] * rd), cvtpk(v[2] * rd, v[3] * rd), cvtpk(v[4] * rd, v[5] * rd), cvtpk(v[6] * rd, v[7] * rd)},
           gp * 1024 + lane * 16, act_off(4 * L + 1) + rt * 65536 + h * 2048);
    }
  }
  wait_vm_bar<0>();
  if (wid == 7) set_flag(ws, 4 * L + 1, tt * 32 + h, lane);
  STAMP(4 * L, 9);
}

constexpr unsigned w64_tbl(int which) { unsigned t = 0; for (int c = 0; c < 8; c++) t |= (unsigned)(w64_at(which, c) >> 14) << (4 * c); return t; }
template <int WHICH, class Hook>
DI void gemm64(const Ctx& x, unsigned abase, f32x16 (&acc)[2], Hook hook) {
  const int lane = x.lane;
  auto aload = [&](unsigned base, int j) AI { return __builtin_bit_cast(bf16x8, ld16(x.rs, lane * 16 + (j & 3) * 1024, base + (j >> 2) * 4096)); };
  bf16x8 a[16], bf[4][2];
  UNROLL for (int j = 0; j < 16; j++) a[j] = aload(abase, j);
  const lds_ptr lb = x.lds + lane * 16;
  lds_ptr b0 = lb + w64_at(WHICH, 0);
  UNROLL for (int j = 0; j < 2; j++) UNROLL for (int n = 0; n < 2; n++) bf[j][n] = lds_b128(b0 + j * 2048 + n * 1024);
  auto trip = [&](int it, auto lastc) AI {
    constexpr bool LAST = decltype(lastc)::value;
    const unsigned t = w64_tbl(WHICH) >> (8 * it);
    const lds_ptr b1 = lb + ((t >> 4) & 15) * 16384, b2 = lb + ((t >> 8) & 15) * 16384;
    abase += 16384;
    static_for<16>([&](auto jc) AI {
      constexpr int j = decltype(jc)::value, r = j + 2;
      hook(it, j);
      if (!LAST || r < 16) UNROLL for (int n = 0; n < 2; n++) bf[r & 3][n] = lds_b128((r < 8 ? b0 : (r < 16 ? b1 : b2)) + (r & 7) * 2048 + n * 1024);
      SB();
      acc[0] = MFMA32(bf[j & 3][0], a[j], acc[0]);
      acc[1] = MFMA32(bf[j & 3][1], a[j], acc[1]);
      SB();
      if (!LAST) a[j] = aload(abase, j);
      SB();
    });
    b0 = b2;
  };
  _Pragma("nounroll") for (int it = 0; it < 3; it++) trip(it, IC<0>{});
  trip(3, IC<1>{});
}

template <bool AFTER_B>
DI void out_compute(const Ctx& x, int L, u32x4 (&res)[4], char* out) {
  char* ws = x.ws;
  const int lane = x.lane, hi = x.hi, ph = 4 * L + (AFTER_B ? 3 : 1);
  const bool last = AFTER_B && L == 1;
  const int pn = x.idx >> 1, rt = (x.grp * 2 + (x.idx & 1)) * 4 + x.wid, r = lane & 31;
  STAMP(ph, 0);
  bar_only();
  STAMP(ph, 3);
  f32x16 acc[2] = {{}, {}};
  gemm64<AFTER_B ? 2 : 0>(x, act_off(ph) + rt * 65536, acc, [&](int it, int j) AI {
    if ((j == 14 && it == 2) || (!AFTER_B && j == 6 && it == 1)) bar_only();
  });
  STAMP(ph, 1);
  float ss = 0.f, sq = 0.f;
  UNROLL for (int i = 0; i < 4; i++) {
    float v[8];
    gather8(acc[i >> 1], i & 1, v);
    UNROLL for (int j = 0; j < 4; j++) res[i][j] = cvtpk(v[2 * j] + bflo(res[i][j]), v[2 * j + 1] + bfhi(res[i][j]));
    if (last) *(GAS u32x4*)(out + (size_t)(rt * 32 + r) * 2048 + pn * 128 + i * 32 + hi * 16) = res[i];
    else st16(x.rs, res[i], i * 1024 + lane * 16, act_off(ph + 1) + rt * 65536 + pn * 4096);
    UNROLL for (int j = 0; j < 4; j++) { ss = dot2(res[i][j], 0x3f803f80u, ss); sq = dot2(res[i][j], res[i][j], sq); }
  }
  if (!last) {
    ss = half_add(ss); sq = half_add(sq);
    const u32x4 sp = stats_pair(ss, sq);
    if (lane < 16) st16(x.rs, sp, lane * 16, OFF_ST + ((ph + 1) >> 1) * 262144 + rt * 4096 + pn * 256);
  }
  bar_only();
  wait_vm_bar<0>();
  STAMP(ph, 2);
}
DI void phase_out_b(const Ctx& x, int L, u32x4 (&res)[4], char* out) {
  if (x.wid < 4) {
    out_compute<true>(x, L, res, out);
    if (x.wid == 0 && L == 0) set_flag(x.ws, 4, x.grp * 32 + (x.idx & 1) * 16 + (x.idx >> 1), x.lane);
    return;
  }
  const int w4 = x.wid - 4, id0 = x.grp * 32 + (x.idx & 1) * 16;
  if (w4 == 3) wait_flags(x.ws, 4 * L + 3, id0 + (x.lane & 15));
  else {
    UNROLL for (int c = 6; c < 8; c++) { dma_w64<2, 3>(x, c, w4 * 5); dma_w64<2, 2>(x, c, w4 * 5 + 3); if (w4 == c - 6) dma_w64<2, 1>(x, c, 15); }
    if (L == 0) UNROLL for (int i = 0; i < 3; i++) { if (w4 * 3 + i < 8) dma_wqkv(x, 0, w4 * 3 + i); }
  }
  bar_only();
  drain_vm();
  bar_only();
  if (L == 0) UNROLL for (int c = 1; c < 4; c++) UNROLL for (int i = 0; i < 2; i++) dma_wqkv(x, c, w4 * 2 + i);
  bar_only();
  bar_only();
}

constexpr int B_QX = 65536, B_PART = 98304;
constexpr int kv_at(int tile) { return (((tile >> 3) + 3) % 5) * 32768 + (tile & 7) * 4096; }
constexpr float QSCALE = 0.17677669529663687f * LOG2E;
DI void phase_out_a_b(const Ctx& x, int L, u32x4 (&res)[4]) {
  char* ws = x.ws;
  const lds_ptr lds = x.lds;
  const int lane = x.lane, wid = x.wid, hi = x.hi, g4 = lane >> 4, c16 = lane & 15, ph = 4 * L + 2;
  const int pm = x.grp * 2 + (x.idx & 1), pj = x.idx >> 1;
  const int rt = pm * 4 + (wid & 3), head = pj * 2 + (wid >> 2);
  const unsigned stb = OFF_ST + (2 * L + 1) * 262144, kvf = OFF_KVF + L * 262144;
  const int id0 = x.grp * 32 + (x.idx & 1) * 16;
  auto kv_dma = [&](int ch, int first) AI {
    gldsN_sc1<4>(ws + kvf + ((ch + x.grp) & 7) * 32768 + first * 1024, lane * 16, x.l0 + kv_at(ch * 8) + first * 1024);
  };
  auto kv_dma2 = [&](int ch, int first) AI {
    gldsN_sc1<2>(ws + kvf + ((ch + x.grp) & 7) * 32768 + first * 1024, lane * 16, x.l0 + kv_at(ch * 8) + first * 1024);
  };
  auto kv_all = [&]() AI {
    const u32x4 v = ld16(x.rs, lane * 4096, OFF_FL + (10 + L) * 128);
    return all_lanes((v[0] & v[1] & v[2] & v[3]) == 1u);
  };
  bool kv_ok = true;
  bf16x8 qf[2];
  if (wid < 4) {
    out_compute<false>(x, L, res, nullptr);
    STAMP(ph, 0);
    float csv[2];
    UNROLL for (int n = 0; n < 2; n++) csv[n] = colsum_half(x, pj * 64 + n * 32 + (lane & 31));
    bar_only();
    STAMP(ph, 1);
    f32x16 acc[2] = {{}, {}};
    u32x4 stv[4];
    float rstd;
    gemm64<1>(x, act_off(ph) + rt * 65536, acc, [&](int it, int j) AI {
      if (j == 0 && it == 3) stats_load(x.rs, stb + rt * 4096, lane, stv);
      if (j == 13 && it == 3) {
        float mean;
        stats_finish(stv, -1, mean, rstd);
        const bf16x8 mf = mean_frag(mean);
        UNROLL for (int n = 0; n < 2; n++) acc[n] = MFMA32(cs_frag(half_add(csv[n]), hi), mf, acc[n]);
      }
    });
    STAMP(ph, 2);
    const float sc = rstd * QSCALE;
    UNROLL for (int i = 0; i < 16; i++) { acc[0][i] *= sc; acc[1][i] *= sc; }
    qf[0] = pack8(acc[0], 0); qf[1] = pack8(acc[0], 1);
    bar_only();
    UNROLL for (int s = 0; s < 2; s++) *(LAS bf16x8*)(lds + B_QX + (wid * 2 + s) * 1024 + lane * 16) = pack8(acc[1], s);
    lgkm_bar();
  } else {
    const int w4 = wid - 4, krt = pm * 4 + (pj >> 2), kth = (pj >> 1) & 1, ktype = pj & 1, u = wid & 1;
    bf16x8 kx[8], kw[8][2];
    auto kw_load = [&]() AI {
      UNROLL for (int i = 0; i < 8; i++) UNROLL for (int n = 0; n < 2; n++)
        kw[i][n] = *(const GAS bf16x8*)(ws + OFF_WKVF + ((ktype * 32 + w4 * 8 + i) * 2 + n) * 1024 + lane * 16);
    };
    if (wid == 7) wait_flags(ws, ph - 1, x.grp * 32 + (lane & 31));
    else {
      UNROLL for (int c = 3; c < 8; c++) { dma_w64<0, 3>(x, c, w4 * 5); dma_w64<0, 2>(x, c, w4 * 5 + 3); if (w4 == c % 3) dma_w64<0, 1>(x, c, 15); }
      UNROLL for (int c = 0; c < 2; c++) { dma_w64<1, 3>(x, c, w4 * 5); dma_w64<1, 2>(x, c, w4 * 5 + 3); if (w4 == c) dma_w64<1, 1>(x, c, 15); }
    }
    bar_only();
    wait_vm_bar<0>();
    if (L == 0) {
      const u32x4 e = {EMPTY, EMPTY, EMPTY, EMPTY};
      const int b = x.grp * 32 + x.idx;
      UNROLL for (int i = 0; i < 2; i++) st16(x.rs, e, lane * 16, OFF_SLAB + (256 + b) * SLAB_B + (w4 * 2 + i) * 1024);
      if (w4 == 0 && lane < 16) st16(x.rs, e, lane * 16, OFF_SLAB + (256 + b) * SLAB_B + 8192);
    }
    f32x4 cs;
    {
      const GAS float* CS = (const GAS float*)(ws + OFF_CSKV);
      if (ktype == 0) cs = *(const GAS f32x4*)(CS + 16 * u + 4 * g4);
      else { const float c = CS[32 + 16 * u + c16]; cs = (f32x4){c, c, c, c}; }
    }
    bar_only();
    UNROLL for (int c = 2; c < 7; c++) dma_w64<1, 4>(x, c, w4 * 4);
    kw_load();
    bar_only();
    dma_w64<1, 4>(x, 7, w4 * 4);
    volatile LAS unsigned* pcnt = (volatile LAS unsigned*)(lds + B_PART);
    if (wid == 6) { pcnt[0] = 0u; pcnt[16] = 0u; }
    bar_only();
    STAMP(ph, 0);
    if (wid == 7) { set_flag(ws, ph, id0 + pj, lane); wait_flags(ws, ph, id0 + c16); }
    asm volatile("s_waitcnt vmcnt(0) lgkmcnt(0)\n\ts_barrier" ::: "memory");
    __builtin_amdgcn_s_setprio(2);
    UNROLL for (int i = 0; i < 8; i++)
      kx[i] = __builtin_bit_cast(bf16x8, ld16(x.rs, (g4 >> 1) * 1024 + (16 * kth + c16 + 32 * (g4 & 1)) * 16 + (i & 1) * 2048,
                                              act_off(ph) + krt * 65536 + w4 * 16384 + (i >> 1) * 4096));
    u32x4 sv[4];
    if (wid >= 6) stats_load(x.rs, stb + krt * 4096, lane, sv);
    f32x4 d[2] = {{}, {}};
    if (ktype == 0) { UNROLL for (int i = 0; i < 8; i++) UNROLL for (int n = 0; n < 2; n++) d[n] = MFMA16(kw[i][n], kx[i], d[n]); }
    else { UNROLL for (int i = 0; i < 8; i++) UNROLL for (int n = 0; n < 2; n++) d[n] = MFMA16(kx[i], kw[i][n], d[n]); }
    UNROLL for (int n = 0; n < 2; n++) *(LAS f32x4*)(lds + B_PART + 1024 + (w4 * 2 + n) * 1024 + lane * 16) = d[n];
    asm volatile("s_waitcnt lgkmcnt(0)" ::: "memory");
    if (lane == 0) __hip_atomic_fetch_add((LAS unsigned*)(lds + B_PART), 1u, __ATOMIC_RELAXED, __HIP_MEMORY_SCOPE_WORKGROUP);
    if (wid >= 6) {
      float mean, rstd;
      stats_finish(sv, kth, mean, rstd);
      f32x4 m4, r4;
      UNROLL for (int j = 0; j < 4; j++) {
        const float mj = shfl(mean, 4 * g4 + j), rj = shfl(rstd, 4 * g4 + j);
        m4[j] = ktype ? mj : mean; r4[j] = ktype ? rj : rstd;
      }
      _Pragma("nounroll") for (unsigned spins = 0; spins < SPIN_CAP * 16 && pcnt[0] != 4u; spins++) __builtin_amdgcn_s_sleep(1);
      asm volatile("" ::: "memory");
      f32x4 e = -m4 * cs;
      UNROLL for (int w = 0; w < 4; w++) e += *(LAS f32x4*)(lds + B_PART + 1024 + (w * 2 + u) * 1024 + lane * 16);
      e *= r4;
      const int frag = ktype ? 2 + kth : u, lp = (ktype ? 16 * u : 16 * kth) + c16 + 32 * (g4 & 1);
      const unsigned e0 = cvtpk(e[0], e[1]), e1 = cvtpk(e[2], e[3]);
      const auto w0 = __builtin_amdgcn_permlane32_swap(e0, e0, false, false), w1 = __builtin_amdgcn_permlane32_swap(e1, e1, false, false);
      if (lane < 32) st16(x.rs, (u32x4){w0[0], w1[0], w0[1], w1[1]}, frag * 1024 + lp * 16, kvf + krt * 4096);
      if (last_arriver(lds + B_PART + 64, 2, lane)) set_flag(ws, 10 + L, x.grp * 32 + x.idx, lane);
    }
    __builtin_amdgcn_s_setprio(0);
    {
      STAMP(ph, 1);
      if (wid == 7) {
        wait_flags(ws, 10 + L, x.grp * 32 + (lane & 31));
        STAMP(ph, 2);
        UNROLL for (int i = 0; i < 8; i++) kv_dma(0, i * 4);
        kv_ok = kv_all();
        drain_vm();
        STAMP(ph, 7);
      }
    }
    __builtin_amdgcn_s_setprio(0);
    bar_only();
    if (wid == 7) *(LAS unsigned*)(lds + B_QX + 8192) = kv_ok;
    lgkm_bar();
    UNROLL for (int s = 0; s < 2; s++) qf[s] = lds_b128(lds + B_QX + ((wid & 3) * 2 + s) * 1024 + lane * 16);
  }
  __builtin_amdgcn_s_waitcnt(0x0F70);
  const bool kv_early = __builtin_amdgcn_readfirstlane(*(LAS unsigned*)(lds + B_QX + 8192)) != 0;
  STAMP(ph, 3); STAMPC(ph, 14);
  if (kv_early) kv_dma(1, wid * 4);
  const bf16x8 sel = sel_frag(lane);
  const f32x16 zero = {};
  f32x16 O = zero, negm, SA, SB_;
  f32x4 LS = {};
  bf16x8 kf[2], vf[2];
  const LdsLane ll(lds, lane);
  kf[0] = ll.rd(kv_at(0)); kf[1] = ll.rd(kv_at(0) + 1024);
  SA = MFMA32(kf[0], qf[0], zero); SA = MFMA32(kf[1], qf[1], SA);
  kf[0] = ll.rd(kv_at(1)); kf[1] = ll.rd(kv_at(1) + 1024);
  vf[0] = ll.rd(kv_at(0) + 2048); vf[1] = ll.rd(kv_at(0) + 3072);
  {
    float m = SA[0];
    UNROLL for (int i = 1; i < 16; i++) m = __builtin_fmaxf(m, SA[i]);
    m = half_max(m);
    UNROLL for (int i = 0; i < 16; i++) { negm[i] = -m; SA[i] -= m; }
  }
#define EX(i) Sc[i] = __builtin_amdgcn_exp2f(Sc[i])
#define PIN(x) asm volatile("" : "+v"(x))
  u32x4 p0, p1 = {0u, 0u, 0u, 0u};
  auto step = [&](f32x16& Sc, f32x16& Sn, int kp, int vc, int vp) AI {
    SB();
    Sn = MFMA32(kf[0], qf[0], negm);
    SB();
    kf[0] = ll.rd(kp);
    EX(0); EX(1); EX(2); PIN(Sc);
    SB();
    O = MFMA32(vf[1], __builtin_bit_cast(bf16x8, p1), O);
    SB();
    vf[1] = ll.rd(vc + 3072);
    EX(3); EX(4); EX(5); PIN(Sc);
    SB();
    Sn = MFMA32(kf[1], qf[1], Sn);
    SB();
    kf[1] = ll.rd(kp + 1024);
    EX(6); EX(7);
    p0[0] = cvtpk(Sc[0], Sc[1]); p0[1] = cvtpk(Sc[2], Sc[3]); p0[2] = cvtpk(Sc[4], Sc[5]); p0[3] = cvtpk(Sc[6], Sc[7]); PIN(p0);
    SB();
    LS = MFMA16(sel, __builtin_bit_cast(bf16x8, p1), LS);
    SB();
    EX(8); EX(9); PIN(Sc);
    SB();
    O = MFMA32(vf[0], __builtin_bit_cast(bf16x8, p0), O);
    SB();
    vf[0] = ll.rd(vp + 2048);
    EX(10); EX(11); EX(12); PIN(Sc);
    SB();
    LS = MFMA16(sel, __builtin_bit_cast(bf16x8, p0), LS);
    SB();
    EX(13); EX(14); EX(15);
    p1[0] = cvtpk(Sc[8], Sc[9]); p1[1] = cvtpk(Sc[10], Sc[11]); p1[2] = cvtpk(Sc[12], Sc[13]); p1[3] = cvtpk(Sc[14], Sc[15]); PIN(p1);
    SB();
  };
  auto hook = [&](int t) AI {
    if (t < 2) { if (kv_early) kv_dma(2 + t, wid * 4); }
    if (t == 2 && !kv_ok) {
      _Pragma("nounroll") for (unsigned spins = 0; spins < SPIN_CAP && !kv_all(); spins++) asm volatile("s_sleep 4" ::: "memory");
      _Pragma("nounroll") for (int i = 0; i < 8; i++) { kv_dma(1, i * 4); kv_dma(2, i * 4); kv_dma(3, i * 4); }
    }
    if (t == 6) wait_vm_bar<0>();
    if (t == 8 || t == 10) kv_dma2(4, wid * 4 + t - 8);
    if (t == 25) wait_vm_bar<0>();
    if (t >= 25 && t < 31) kv_dma2(5 + (t - 25) / 2, wid * 4 + ((t - 25) & 1) * 2);
    if (t == 38) wait_vm_bar<0>();
    if (t == 39 || t == 41) dma_w64<2, 2>(x, (t - 39) / 2, wid * 2);
    if (t == 49) bar_only();
    if (t >= 50 && t < 58 && !(t & 1)) dma_w64<2, 2>(x, 2 + (t - 50) / 2, wid * 2);
  };
  if (wid >= 4) __builtin_amdgcn_s_setprio(1);
  static_for<32>([&](auto tc) AI {
    constexpr int t = decltype(tc)::value * 2;
    hook(t);
    step(SA, SB_, kv_at((t + 2) & 63), kv_at(t), kv_at(t + 1));
    hook(t + 1);
    step(SB_, SA, kv_at((t + 3) & 63), kv_at(t + 1), kv_at((t + 2) & 63));
  });
  O = MFMA32(vf[1], __builtin_bit_cast(bf16x8, p1), O);
  LS = MFMA16(sel, __builtin_bit_cast(bf16x8, p1), LS);
  STAMP(ph, 4); STAMPC(ph, 15);
  __builtin_amdgcn_s_setprio(0);
  float l = LS[0];
  {
    float chk = l;
    UNROLL for (int i = 0; i < 16; i++) chk = __builtin_fmaxf(chk, __builtin_fabsf(O[i]) * 0x1p-27f);
    bool bad = !(chk < 0x1p100f);
    UNROLL for (int i = 0; i < 16; i++) bad |= (O[i] != O[i]);
    if (any_lane(bad)) {
      O = zero; l = 0.f;
      float m = -__builtin_inff();
      for (int t = 0; t < 64; t++) {
        bf16x8 f[4];
        UNROLL for (int i = 0; i < 4; i++) f[i] = __builtin_bit_cast(bf16x8, ld16(x.rs, i * 1024 + lane * 16, kvf + t * 4096));
        f32x16 s = MFMA32(f[0], qf[0], zero);
        s = MFMA32(f[1], qf[1], s);
        float mt = s[0];
        UNROLL for (int i = 1; i < 16; i++) mt = __builtin_fmaxf(mt, s[i]);
        const float mn = __builtin_fmaxf(m, half_max(mt));
        const float al = __builtin_amdgcn_exp2f(m - mn);
        m = mn;
        UNROLL for (int i = 0; i < 16; i++) { s[i] = __builtin_amdgcn_exp2f(s[i] - mn); O[i] *= al; }
        const bf16x8 p0 = pack8(s, 0), p1 = pack8(s, 1);
        f32x4 lt = MFMA16(sel, p0, ((f32x4){0.f, 0.f, 0.f, 0.f}));
        lt = MFMA16(sel, p1, lt);
        l = l * al + lt[0];
        O = MFMA32(f[2], p0, O);
        O = MFMA32(f[3], p1, O);
      }
    }
  }
  const float rl = 1.f / l;
  UNROLL for (int gp = 0; gp < 2; gp++) {
    float v[8];
    gather8(O, gp, v);
    st16(x.rs, (u32x4){cvtpk(v[0] * rl, v[1] * rl), cvtpk(v[2] * rl, v[3] * rl), cvtpk(v[4] * rl, v[5] * rl), cvtpk(v[6] * rl, v[7] * rl)},
         gp * 1024 + lane * 16, act_off(ph + 1) + rt * 65536 + head * 2048);
  }
  asm volatile("s_waitcnt vmcnt(0) lgkmcnt(0)\n\ts_barrier" ::: "memory");
  if (wid == 7) set_flag(ws, ph + 1, id0 + pj, lane);
  STAMP(ph, 5);
}

__global__ __launch_bounds__(512, 2) void k_main(char* ws, char* out) {
  extern __shared__ __attribute__((aligned(16))) char lds_raw[];
  Ctx x;
  x.ws = ws;
  x.rs = __builtin_amdgcn_make_buffer_rsrc(ws, 0, WS_USED, 0x00020000);
  x.lds = (lds_ptr)lds_raw;
  x.l0 = (unsigned)(uintptr_t)x.lds;
  x.lane = TID & 63;
  x.wid = __builtin_amdgcn_readfirstlane(TID >> 6);
  const int pg = BID & 7, pi = BID >> 3;
  x.ah = (pg >> 1) * 8 + (pi >> 2); x.at = (pg & 1) * 4 + (pi & 3);
  x.grp = (pg & 6) | (pi & 1); x.idx = (((pg & 1) * 8 + (pi >> 2)) << 1) | ((pi >> 1) & 1);
  x.hi = x.lane >> 5;
  { const Ctx y = fresh(x, 0); UNROLL for (int c = 0; c < 2; c++) dma_wqkv(y, c, y.wid); }
  u32x4 res[4] = {};
  for (int L = 0; L < 2; L++) {
    phase_a(fresh(x, 0), L, res);
    phase_out_a_b(fresh(x, 0), L, res);
    phase_out_b(fresh(x, 0), L, res, out);
  }
}

#if defined(__HIP_DEVICE_COMPILE__)
#pragma clang attribute pop
#endif

extern "C" void kernel_launch(void* const* d_in, const int* in_sizes, int n_in, void* d_out, int out_size, void* d_ws, size_t ws_size,
                              hipStream_t stream) {
  char* ws = (char*)d_ws;
  (void)hipFuncSetAttribute((const void*)k_main, hipFuncAttributeMaxDynamicSharedMemorySize, LDS_ALL);
  (void)hipFuncSetAttribute((const void*)k_prep, hipFuncAttributeMaxDynamicSharedMemorySize, PREP_LDS);
  k_prep<<<296, 512, PREP_LDS, stream>>>((const char*)d_in[0], (const char*)d_in[1], (const u16*)d_in[2], (const char*)d_in[3], (const char*)d_in[5],
                                         (const u16*)d_in[6], (const u16*)d_in[7], (const char*)d_in[8], ws);
  k_main<<<256, 512, LDS_ALL, stream>>>(ws, (char*)d_out);
}
```

```cpp
#include <hip/hip_runtime.h>
#include <hip/hip_bf16.h>
#include <stdint.h>

typedef unsigned short u16;
using bf16x8 = __attribute__((ext_vector_type(8))) short;
using s16x4 = __attribute__((ext_vector_type(4))) short;
using f32x16 = __attribute__((ext_vector_type(16))) float;
using f32x4 = __attribute__((ext_vector_type(4))) float;
using f32x2 = __attribute__((ext_vector_type(2))) float;
using u32x4 = __attribute__((ext_vector_type(4))) unsigned;
using u32x2 = __attribute__((ext_vector_type(2))) unsigned;
typedef __bf16 bf16x2_t __attribute__((ext_vector_type(2)));
#define LAS __attribute__((address_space(3)))
#define GAS __attribute__((address_space(1)))
typedef LAS char* lds_ptr;
typedef short v4i16_t __attribute__((ext_vector_type(4)));
#define DI __device__ __forceinline__
#define MFMA32(a, b, c) __builtin_amdgcn_mfma_f32_32x32x16_bf16((a), (b), (c), 0, 0, 0)
#define MFMA16(a, b, c) __builtin_amdgcn_mfma_f32_16x16x32_bf16((a), (b), (c), 0, 0, 0)
#define SB() __builtin_amdgcn_sched_barrier(0)
#define RLX_AGENT __ATOMIC_RELAXED, __HIP_MEMORY_SCOPE_AGENT
#define UNROLL _Pragma("unroll")
#define AI __attribute__((always_inline))

constexpr float LOG2E = 1.4426950408889634f;
constexpr unsigned SPIN_CAP = 1u << 20;

constexpr unsigned MBy = 1u << 20;
constexpr unsigned OFF_EPOCH = 0;
constexpr unsigned OFF_FL = 768 * 1024;
constexpr unsigned OFF_CSKV = 120 * 1024;
constexpr unsigned OFF_CS = 512 * 1024;
constexpr unsigned OFF_WKVF = 128 * 1024;
constexpr unsigned OFF_AUX = 256 * 1024;
constexpr unsigned AUX_B = 8192;
constexpr unsigned OFF_ST = 1 * MBy;
constexpr unsigned OFF_KVF = 2 * MBy;
constexpr unsigned OFF_SLAB = 4 * MBy;
constexpr unsigned SLAB_B = 8448;
constexpr unsigned OFF_ACT = 16 * MBy;
constexpr unsigned OFF_WTA = 48 * MBy;
constexpr unsigned OFF_WT64 = 54 * MBy;
constexpr unsigned WS_USED = 60 * MBy;

#if defined(__HIP_DEVICE_COMPILE__)
#pragma clang attribute push(__attribute__((target("no-packed-fp32-ops"))), apply_to = function)
#endif
#define U2F(x) __builtin_bit_cast(float, (unsigned)(x))
#define F2U(x) __builtin_bit_cast(unsigned, (float)(x))
#define TID ((int)__builtin_amdgcn_workitem_id_x())
#define BID ((int)__builtin_amdgcn_workgroup_id_x())
DI bool all_lanes(bool p) { return __builtin_amdgcn_ballot_w64(!p) == 0; }
DI bool any_lane(bool p) { return __builtin_amdgcn_ballot_w64(p) != 0; }
DI float shfl(float v, int src) { return U2F(__builtin_amdgcn_ds_bpermute(src * 4, (int)F2U(v))); }
#define STAMP(k, i)
#define STAMPC(k, i)
DI void glds16(const void* sbase, unsigned voff, unsigned lds_base) {
  unsigned sv;
  asm volatile("s_mov_b32 %0, m0\n\ts_mov_b32 m0, %3\n\ts_nop 0\n\tglobal_load_lds_dwordx4 %1, %2\n\ts_mov_b32 m0, %0"
               : "=&s"(sv) : "v"(voff), "s"(sbase), "s"(lds_base) : "memory");
}
#define GL_(o, sc) "global_load_lds_dwordx4 %1, %2 offset:" #o sc "\n\t"
#define GLDS_N(NAME, SC)                                                                                                   \
  template <int N> DI void NAME(const void* sbase, unsigned voff, unsigned lds_base) {                                     \
    unsigned sv;                                                                                                           \
    if constexpr (N == 1)                                                                                                  \
      asm volatile("s_mov_b32 %0, m0\n\ts_mov_b32 m0, %3\n\ts_nop 0\n\t" GL_(0, SC) "s_mov_b32 m0, %0"                    \
                   : "=&s"(sv) : "v"(voff), "s"(sbase), "s"(lds_base) : "memory");                                         \
    else if constexpr (N == 2)                                                                                                  \
      asm volatile("s_mov_b32 %0, m0\n\ts_mov_b32 m0, %3\n\ts_nop 0\n\t" GL_(0, SC) GL_(1024, SC) "s_mov_b32 m0, %0"       \
                   : "=&s"(sv) : "v"(voff), "s"(sbase), "s"(lds_base) : "memory");                                         \
    else if constexpr (N == 3)                                                                                             \
      asm volatile("s_mov_b32 %0, m0\n\ts_mov_b32 m0, %3\n\ts_nop 0\n\t" GL_(0, SC) GL_(1024, SC) GL_(2048, SC) "s_mov_b32 m0, %0" \
                   : "=&s"(sv) : "v"(voff), "s"(sbase), "s"(lds_base) : "memory");                                         \
    else                                                                                                                   \
      asm volatile("s_mov_b32 %0, m0\n\ts_mov_b32 m0, %3\n\ts_nop 0\n\t" GL_(0, SC) GL_(1024, SC) GL_(2048, SC) GL_(3072, SC) "s_mov_b32 m0, %0" \
                   : "=&s"(sv) : "v"(voff), "s"(sbase), "s"(lds_base) : "memory");                                         \
  }
GLDS_N(gldsN_sc1, " sc1")
DI void glds16_sc1(const void* sbase, unsigned voff, unsigned lds_base) {
  unsigned sv;
  asm volatile("s_mov_b32 %0, m0\n\ts_mov_b32 m0, %3\n\ts_nop 0\n\tglobal_load_lds_dwordx4 %1, %2 sc1\n\ts_mov_b32 m0, %0"
               : "=&s"(sv) : "v"(voff), "s"(sbase), "s"(lds_base) : "memory");
}
template <int N> DI void wait_vm_bar() { asm volatile("s_waitcnt vmcnt(%0)\n\ts_barrier" ::"n"(N) : "memory"); }
DI void bar_only() { asm volatile("s_barrier" ::: "memory"); }
DI void lgkm_bar() { asm volatile("s_waitcnt lgkmcnt(0)\n\ts_barrier" ::: "memory"); }
DI void drain_vm() { asm volatile("s_waitcnt vmcnt(0)" ::: "memory"); }
DI s16x4 lds_tr(lds_ptr p) { return __builtin_bit_cast(s16x4, __builtin_amdgcn_ds_read_tr16_b64_v4i16((LAS v4i16_t*)p)); }
template <int E> DI bf16x8 lds_trfrag(lds_ptr p) { s16x4 lo = lds_tr(p), hi = lds_tr(p + E); return __builtin_shufflevector(lo, hi, 0, 1, 2, 3, 4, 5, 6, 7); }
DI bf16x8 lds_b128(lds_ptr p) { return *(LAS bf16x8*)p; }
DI unsigned cvtpk(float lo, float hi) {
  bf16x2_t v = __builtin_convertvector((f32x2){lo, hi}, bf16x2_t);
  return __builtin_bit_cast(unsigned, v);
}
DI float bflo(unsigned u) { return U2F(u << 16); }
DI float bfhi(unsigned u) { return U2F(u & 0xffff0000u); }
DI float dot2(unsigned a, unsigned b, float c) {
  return __builtin_amdgcn_fdot2_f32_bf16(__builtin_bit_cast(bf16x2_t, a), __builtin_bit_cast(bf16x2_t, b), c, false);
}
DI float half_add(float x) { auto r = __builtin_amdgcn_permlane32_swap(F2U(x), F2U(x), false, false); return U2F(r[0]) + U2F(r[1]); }
DI float half_max(float x) { auto r = __builtin_amdgcn_permlane32_swap(F2U(x), F2U(x), false, false); return __builtin_fmaxf(U2F(r[0]), U2F(r[1])); }
DI float half_min(float x) { auto r = __builtin_amdgcn_permlane32_swap(F2U(x), F2U(x), false, false); return __builtin_fminf(U2F(r[0]), U2F(r[1])); }
DI bf16x8 pack8(const f32x16& x, int s) {
  u32x4 p = {cvtpk(x[8 * s], x[8 * s + 1]), cvtpk(x[8 * s + 2], x[8 * s + 3]), cvtpk(x[8 * s + 4], x[8 * s + 5]), cvtpk(x[8 * s + 6], x[8 * s + 7])};
  return __builtin_bit_cast(bf16x8, p);
}
DI void gather8(const f32x16& a, int gp, float (&v)[8]) {
  UNROLL for (int j = 0; j < 4; j++) {
    auto s = __builtin_amdgcn_permlane32_swap(F2U(a[8 * gp + j]), F2U(a[8 * gp + 4 + j]), false, false);
    v[j] = U2F(s[0]); v[4 + j] = U2F(s[1]);
  }
}
typedef __amdgpu_buffer_rsrc_t rsrc_t;
DI u32x4 ld16(rsrc_t rs, unsigned voff, unsigned soff) { return __builtin_amdgcn_raw_buffer_load_b128(rs, voff, soff, 16); }
DI unsigned ld4(rsrc_t rs, unsigned voff, unsigned soff) { return __builtin_amdgcn_raw_buffer_load_b32(rs, voff, soff, 16); }
DI void st16(rsrc_t rs, u32x4 v, unsigned voff, unsigned soff) { __builtin_amdgcn_raw_buffer_store_b128(v, rs, voff + soff, 0, 16); }
DI void st8(rsrc_t rs, u32x2 v, unsigned voff, unsigned soff) { __builtin_amdgcn_raw_buffer_store_b64(v, rs, voff, soff, 16); }
DI void st4(rsrc_t rs, unsigned v, unsigned voff, unsigned soff) { __builtin_amdgcn_raw_buffer_store_b32(v, rs, voff, soff, 16); }
constexpr unsigned EMPTY = 0xffffffffu;
DI u32x4 not_empty(f32x4 v) {
  u32x4 r;
  UNROLL for (int j = 0; j < 4; j++) r[j] = F2U(v[j]) < EMPTY ? F2U(v[j]) : EMPTY - 1;
  return r;
}
typedef unsigned long long u64;
template <class T> DI GAS T* flag_at(char* ws, int slot, int id) { return (GAS T*)(ws + OFF_FL + (id >> 2) * 4096 + slot * 128) + (id & 3); }
template <class T> DI void set_flag(char* ws, T val, int slot, int id, int lane) {
  if (lane == 0) __hip_atomic_store(flag_at<T>(ws, slot, id), val, RLX_AGENT);
}
template <class T> DI void wait_flags(char* ws, T val, int slot, int id, bool on) {
  GAS T* p = flag_at<T>(ws, slot, id);
  _Pragma("nounroll") for (unsigned spins = 0; spins < SPIN_CAP; spins++) {
    T v = val;
    if (on) v = __hip_atomic_load(p, RLX_AGENT);
    if (all_lanes(v == val)) break;
    __builtin_amdgcn_s_sleep(1);
  }
  asm volatile("" ::: "memory");
}
DI bool all_flags(rsrc_t rs, int slot, int lane) {
  const u32x4 v = ld16(rs, lane * 4096, OFF_FL + slot * 128);
  return all_lanes((v[0] & v[1] & v[2] & v[3]) == 1u);
}
DI bool all_flags_asm(char* ws, u64 tag, int slot, int lane) {
  u32x4 a, b;
  asm volatile("global_load_dwordx4 %0, %2, %3 sc1\n\tglobal_load_dwordx4 %1, %2, %3 offset:16 sc1\n\ts_waitcnt vmcnt(0)"
               : "=&v"(a), "=&v"(b) : "v"(lane * 4096), "s"(ws + OFF_FL + slot * 128) : "memory");
  const unsigned lo = (unsigned)tag, hi = (unsigned)(tag >> 32);
  return all_lanes(a[0] == lo && a[2] == lo && b[0] == lo && b[2] == lo && a[1] == hi && a[3] == hi && b[1] == hi && b[3] == hi);
}
DI bool last_arriver(lds_ptr cnt, unsigned n, int lane) {
  drain_vm();
  unsigned old = 0;
  if (lane == 0) old = __hip_atomic_fetch_add((LAS unsigned*)cnt, 1u, __ATOMIC_RELAXED, __HIP_MEMORY_SCOPE_WORKGROUP);
  return (unsigned)__builtin_amdgcn_readfirstlane(old) == n - 1;
}
DI float swap16_add(float a, float b) {
  auto r = __builtin_amdgcn_permlane16_swap(F2U(a), F2U(b), false, false);
  return U2F(r[0]) + U2F(r[1]);
}
DI float sum8(const u32x4 (&v)[2]) {
  return ((U2F(v[0][0]) + U2F(v[0][1])) + (U2F(v[0][2]) + U2F(v[0][3]))) + ((U2F(v[1][0]) + U2F(v[1][1])) + (U2F(v[1][2]) + U2F(v[1][3])));
}
DI void stats_load(rsrc_t rs, unsigned base, int lane, u32x4 (&v)[4]) {
  UNROLL for (int i = 0; i < 4; i++) v[i] = ld16(rs, lane * 16 + i * 1024, base);
}
DI u32x4 stats_pair(float s, float q) {
  auto a = __builtin_amdgcn_permlane16_swap(F2U(s), F2U(s), false, false), b = __builtin_amdgcn_permlane16_swap(F2U(q), F2U(q), false, false);
  return (u32x4){a[0], b[0], a[1], b[1]};
}
DI void stats_finish(const u32x4 (&v)[4], int half, float& mean, float& rstd) {
  float t[4];
  UNROLL for (int j = 0; j < 4; j++) t[j] = (U2F(v[0][j]) + U2F(v[1][j])) + (U2F(v[2][j]) + U2F(v[3][j]));
  if (half == 0) { t[2] = t[0]; t[3] = t[1]; }
  if (half == 1) { t[0] = t[2]; t[1] = t[3]; }
  const float s = half_add(swap16_add(t[0], t[2])), q = half_add(swap16_add(t[1], t[3]));
  mean = s * (1.f / 1024.f);
  rstd = __builtin_amdgcn_rsqf(__builtin_fmaxf(q * (1.f / 1024.f) - mean * mean, 0.f) + 1e-5f);
}
DI void split3(float x, unsigned& a, unsigned& b, unsigned& c) {
  a = cvtpk(x, x) & 0xffffu; x -= bflo(a);
  b = cvtpk(x, x) & 0xffffu; x -= bflo(b);
  c = cvtpk(x, x) & 0xffffu;
}
DI bf16x8 cs_frag(float cs, int hi) {
  unsigned a, b, c; split3(cs, a, b, c);
  u32x4 p = {a | (b << 16), a | (c << 16), b | (a << 16), 0u};
  if (hi) p = (u32x4){0u, 0u, 0u, 0u};
  return __builtin_bit_cast(bf16x8, p);
}
DI bf16x8 mean_frag(float mean) {
  unsigned a, b, c; split3(-mean, a, b, c);
  u32x4 p = {a | (a << 16), b | (a << 16), b | (c << 16), 0u};
  return __builtin_bit_cast(bf16x8, p);
}
constexpr int ramp(int s, int I0, int PD) { int n = I0 + 2 * (s + 1); if (n > s + 1 + PD) n = s + 1 + PD; return n > 64 ? 64 : n; }
template <int V> struct IC { static constexpr int value = V; };
template <int N, int I = 0, class F> DI void static_for(F&& f) {
  if constexpr (I < N) { f(IC<I>{}); static_for<N, I + 1>(f); }
}
template <bool RATIO> DI float dual_softmax(const f32x16& p, float c, f32x16& ep, f32x16& em) {
  float mx = p[0], mn = p[0];
  UNROLL for (int i = 1; i < 16; i++) { mx = __builtin_fmaxf(mx, p[i]); mn = __builtin_fminf(mn, p[i]); }
  mx = half_max(mx); mn = half_min(mn);
  const float a = -mx * c, b = mn * c;
  float sp = 0.f, sm = 0.f;
  UNROLL for (int i = 0; i < 16; i++) {
    ep[i] = __builtin_amdgcn_exp2f(__builtin_fmaf(p[i], c, a));
    em[i] = __builtin_amdgcn_exp2f(__builtin_fmaf(p[i], -c, b));
    sp += ep[i]; sm += em[i];
  }
  sp = half_add(sp); sm = __builtin_amdgcn_rcpf(half_add(sm));
  if (RATIO) { sm *= sp; UNROLL for (int i = 0; i < 16; i++) em[i] *= sm; }
  else { const float rp = __builtin_amdgcn_rcpf(sp); UNROLL for (int i = 0; i < 16; i++) { ep[i] *= rp; em[i] *= sm; } }
  return sp;
}
DI bf16x8 sel_frag(int lane) {
  short v = ((lane & 3) == 0 && ((lane >> 2) & 1) == ((lane >> 4) & 1)) ? (short)0x3f80 : (short)0;
  return (bf16x8){v, v, v, v, v, v, v, v};
}

struct Ctx {
  char* ws; rsrc_t rs; lds_ptr lds; unsigned l0; u64 tag;
  int lane, wid, grp, idx, hi, ah, at;
};
DI Ctx fresh(const Ctx& x, int swap) {
  Ctx y = x;
  y.wid ^= swap;
  asm volatile("" : "+s"(y.wid), "+s"(y.grp), "+s"(y.idx), "+s"(y.ah), "+s"(y.at));
  asm volatile("" : "+v"(y.lane));
  y.lane &= 63;
  y.hi = y.lane >> 5;
  return y;
}
struct LdsLane {
  lds_ptr b0, b1, b2;
  DI LdsLane(lds_ptr lds, int lane) : b0(lds + lane * 16), b1(b0 + 65536), b2(b0 + 131072) { asm volatile("" : "+v"(b0), "+v"(b1), "+v"(b2)); }
  DI bf16x8 rd(int off) const { return lds_b128((off < 65536 ? b0 : (off < 131072 ? b1 : b2)) + (off & 65535)); }
};
constexpr int LDS_ALL = 163840;
constexpr int A_SLOT = 24576, A_ROW = 200, A_IMG = 106496, A_KVL = A_IMG + 256 * A_ROW, A_ZL = A_KVL + 4096, A_AUX = 6 * A_SLOT;
static_assert(A_ZL + 128 <= LDS_ALL, "");
DI unsigned act_off(int i) { return OFF_ACT + i * 4 * MBy; }

DI int a_head(const Ctx& x) { return x.ah; }
DI int a_tile(const Ctx& x) { return x.at; }
DI void dma_wqkv(const Ctx& x, int c, int pg) {
  gldsN_sc1<3>(x.ws + OFF_WTA + a_head(x) * 196608 + c * A_SLOT + pg * 3072, x.lane * 16, x.l0 + (c % 6) * A_SLOT + pg * 3072);
}
constexpr int w64_at(int which, int c) {
  constexpr int t[3][8] = {{48, 64, 80, 0, 16, 32, 96, 112}, {128, 144, 48, 64, 80, 0, 16, 32}, {32, 48, 64, 80, 96, 112, 128, 144}};
  return t[which][c] * 1024;
}
template <int WHICH, int N> DI void dma_w64(const Ctx& x, int c, int p) {
  gldsN_sc1<N>(x.ws + OFF_WT64 + WHICH * 2 * MBy + (x.idx >> 1) * 131072 + c * 16384 + p * 1024, x.lane * 16, x.l0 + w64_at(WHICH, c) + p * 1024);
}
constexpr int MAIN_CODE_BYTES = 44 * 1024;
struct Inputs { const char *x0, *wqkv, *wphi, *woA, *wq, *wk, *wv, *woB; };
DI u64 phase_p(const Ctx& x, const Inputs& in, int bid) {
  char* ws = x.ws;
  STAMP(0, 0);
  const u64 epoch = __hip_atomic_load((GAS u64*)(ws + OFF_EPOCH), RLX_AGENT);
  const lds_ptr lds = x.lds;
  const int lane = x.lane, wid = x.wid, hi = x.hi, r = lane & 31, tid = wid * 64 + lane;
  const int kb = bid & 15, cu = bid >> 4;
  UNROLL for (int g = 0; g < 3; g++)
    glds16(in.wqkv + (size_t)(kb * 64 + wid * 8) * 6144 + g * 2048 + cu * 128, (lane >> 3) * 6144 + (lane & 7) * 16, x.l0 + g * 8192 + wid * 1024);
  const int rt = bid >> 2, ct = (bid & 3) * 4 + (wid & 3);
  u32x4 xv[4];
  if (wid < 4) UNROLL for (int i = 0; i < 4; i++) xv[i] = *(const GAS u32x4*)(in.x0 + (size_t)(rt * 32 + r) * 2048 + ct * 128 + i * 32 + hi * 16);
  bf16x8 sm;
  if (wid == 7 && bid < 64) UNROLL for (int j = 0; j < 8; j++)
    sm[j] = ((const GAS short*)in.wphi)[(bid >> 1) * 1024 + (16 * (bid & 1) + 8 * (j >> 2) + 4 * hi + (j & 3)) * 32 + r];
  if (wid == 7 && bid >= 128) {
    const GAS short* W = (const GAS short*)(bid >= 192 ? in.wv : in.wk);
    UNROLL for (int j = 0; j < 8; j++) sm[j] = W[(32 * ((bid >> 1) & 31) + 8 * (lane >> 4) + j) * 32 + 16 * (bid & 1) + (lane & 15)];
  }
  if (wid == 5 && bid < 64) {
    u64 pc;
    asm volatile("s_getpc_b64 s[40:41]\n\ts_add_u32 s40, s40, _Z6k_mainPcS_6Inputs@rel32@lo+4\n\ts_addc_u32 s41, s41, _Z6k_mainPcS_6Inputs@rel32@hi+12\n\ts_mov_b64 %0, s[40:41]"
                 : "=s"(pc) : : "s40", "s41", "scc");
    const int off = ((bid >> 3) * 64 + lane) * 128;
    if (off < MAIN_CODE_BYTES) { const unsigned t = *(const GAS unsigned*)(pc + off); asm volatile("" ::"v"(t)); }
  }
  wait_vm_bar<0>();
  UNROLL for (int g = 0; g < 3; g++)
    glds16((g == 0 ? in.woA : (g == 1 ? in.wq : in.woB)) + (size_t)(kb * 64 + wid * 8) * 2048 + cu * 128, (lane >> 3) * 2048 + (lane & 7) * 16,
           x.l0 + 24576 + g * 8192 + wid * 1024);
  const int ks = wid >> 1, nh = wid & 1, step = kb * 4 + ks;
  const lds_ptr p = lds + (ks * 16 + 8 * hi + ((lane & 15) >> 2)) * 128 + nh * 64 + ((lane >> 4) & 1) * 32 + (lane & 3) * 8;
  LAS float* red = (LAS float*)(lds + 49152);
  auto colsum = [&](u32x4 fr, int at) AI {
    float c = 0.f;
    UNROLL for (int k = 0; k < 4; k++) c = dot2(fr[k], 0x3f803f80u, c);
    c = half_add(c);
    if (hi == 0) red[ks * 256 + at + r] = c;
  };
  UNROLL for (int g = 0; g < 3; g++) {
    const u32x4 fr = __builtin_bit_cast(u32x4, lds_trfrag<512>(p + g * 8192));
    st16(x.rs, fr, lane * 16, OFF_WTA + ((cu * 2 + nh) * 192 + step * 3 + g) * 1024);
    colsum(fr, nh * 96 + g * 32);
  }
  if (wid < 4) {
    float s = 0, q = 0;
    UNROLL for (int i = 0; i < 4; i++) {
      st16(x.rs, xv[i], lane * 16, OFF_ACT + rt * 65536 + (ct * 4 + i) * 1024);
      UNROLL for (int j = 0; j < 4; j++) { s = dot2(xv[i][j], 0x3f803f80u, s); q = dot2(xv[i][j], xv[i][j], q); }
    }
    const u32x4 sp = stats_pair(half_add(s), half_add(q));
    if (lane < 16) st16(x.rs, sp, lane * 16, OFF_ST + rt * 4096 + ct * 256);
  }
  if (wid == 7 && bid < 64) st16(x.rs, __builtin_bit_cast(u32x4, sm), lane * 16, OFF_AUX + (bid >> 1) * AUX_B + 6144 + (bid & 1) * 1024);
  lgkm_bar();
  if (wid == 6 && lane < 48) {
    const f32x4 t = (*(LAS f32x4*)(red + lane * 4) + *(LAS f32x4*)(red + 256 + lane * 4)) + (*(LAS f32x4*)(red + 512 + lane * 4) + *(LAS f32x4*)(red + 768 + lane * 4));
    st16(x.rs, __builtin_bit_cast(u32x4, t), (lane >= 24 ? AUX_B - 384 : 0) + lane * 16, OFF_AUX + cu * 2 * AUX_B + kb * 384);
  }
  asm volatile("s_waitcnt vmcnt(0) lgkmcnt(0)\n\ts_barrier" ::: "memory");
  STAMP(0, 11);
  const u64 tag = (((u64)(unsigned)__builtin_amdgcn_readfirstlane((unsigned)(epoch >> 32)) << 32) | (unsigned)__builtin_amdgcn_readfirstlane((unsigned)epoch)) *
                      6364136223846793005ull + 1442695040888963407ull;
  if (wid == 0) set_flag(ws, tag, 0, bid, lane);
  UNROLL for (int g = 0; g < 3; g++) {
    const u32x4 fr = __builtin_bit_cast(u32x4, lds_trfrag<512>(p + 24576 + g * 8192));
    st16(x.rs, fr, lane * 16, OFF_WT64 + g * 2 * MBy + (cu * 128 + step * 2 + nh) * 1024);
    if (g == 1) colsum(fr, nh * 32);
  }
  if (wid == 5 && lane < 24) {
    const int slot = (bid & 3) * 3 + (lane >> 3);
    if (slot != 0 && slot != 8) st16(x.rs, (u32x4){0u, 0u, 0u, 0u}, slot * 128 + (lane & 7) * 16, OFF_FL + (bid >> 2) * 4096);
  }
  const u32x4 e = {EMPTY, EMPTY, EMPTY, EMPTY};
  st16(x.rs, e, tid * 16, OFF_SLAB + (x.ah * 8 + x.at) * SLAB_B);
  if (tid < 16) st16(x.rs, e, tid * 16, OFF_SLAB + (x.ah * 8 + x.at) * SLAB_B + 8192);
  if (wid == 7 && bid >= 128) {
    st16(x.rs, __builtin_bit_cast(u32x4, sm), lane * 16, OFF_WKVF + (bid - 128) * 1024);
    float c = 0.f;
    UNROLL for (int k = 0; k < 4; k++) c = dot2(__builtin_bit_cast(u32x4, sm)[k], 0x3f803f80u, c);
    c = half_add(swap16_add(c, c));
    if (lane < 16) st4(x.rs, F2U(c), lane * 4, OFF_CSKV + ((bid >= 192) * 2 + (bid & 1)) * 2048 + ((bid >> 1) & 31) * 64);
  }
  lgkm_bar();
  if (wid == 6 && lane < 16) {
    u32x4 t;
    UNROLL for (int j = 0; j < 4; j++) t[j] = F2U((red[j * 16 + lane] + red[256 + j * 16 + lane]) + (red[512 + j * 16 + lane] + red[768 + j * 16 + lane]));
    st16(x.rs, t, lane * 16, OFF_CS + cu * 4096 + kb * 256);
  }
  if (wid == 0) wait_flags(ws, tag, 0, lane < 16 ? (x.ah >> 1) * 16 + lane : (lane < 48 ? x.at * 32 + lane - 16 : x.ah * 2 + (lane & 1)), lane < 50);
  asm volatile("s_waitcnt vmcnt(0) lgkmcnt(0)\n\ts_barrier" ::: "memory");
  if (wid == 0) set_flag(ws, tag, 8, bid, lane);
  return tag;
}

DI void phase_a(const Ctx& x, int L, u32x4 (&res)[4]) {
  char* ws = x.ws;
  const int lane = x.lane, wid = x.wid, hi = x.hi, h = a_head(x), tt = a_tile(x), r = lane & 31, rt = tt * 8 + wid;
  const lds_ptr lds = x.lds;
  constexpr int NSTEP = 64, PD = 16, I0 = 6;
  if (L) { STAMP(4 * L, 0); }
  auto aux = [&]() AI { gldsN_sc1<1>(ws + OFF_AUX + h * AUX_B + wid * 1024, lane * 16, x.l0 + A_AUX + wid * 1024); };
  float csv[3][8];
  bf16x8 wph[2], csf[3], mf;
  float rstd;
  if (L) {
    if (wid == 0) wait_flags(ws, 1u, 4 * L, tt * 32 + lane, lane < 32);
    UNROLL for (int c = 4; c < 6; c++) dma_wqkv(x, c, wid);
    aux();
    wait_vm_bar<6>();
  }

  STAMP(4 * L, 10);
  const unsigned abase = act_off(4 * L) + rt * 65536;
  auto aload = [&](int s) AI { return __builtin_bit_cast(bf16x8, ld16(x.rs, lane * 16 + (s & 3) * 1024, abase + (s >> 2) * 4096)); };
  bf16x8 a[PD];
  UNROLL for (int s = 0; s < I0; s++) a[s] = aload(s);
  f32x16 acc[3] = {{}, {}, {}};
  const int q4 = (lane & 15) >> 2;
  bf16x8 bf[2][3];
  const LdsLane ll(lds, lane);
  auto bread = [&](int s) AI { UNROLL for (int i = s < 48 ? 0 : 1; i < 3; i++) bf[s & 1][i] = ll.rd(((s % 48) * 3 + i) * 1024); };
  u32x4 stv[4];
  if (L == 0) {
    UNROLL for (int c = 2; c < 6; c++) dma_wqkv(x, c, wid);
    aux();
    wait_vm_bar<I0 + 12>();
  }
  STAMP(4 * L, 1); STAMPC(4 * L, 14);
  bread(0);
  static_for<NSTEP>([&](auto sc) AI {
    constexpr int s = decltype(sc)::value;
    if (s == 15) { wait_vm_bar<ramp(14, I0, PD) - I0>(); dma_wqkv(x, 6, wid); }
    if (s == 25) {
      if (L == 0 && wid == 0)
        _Pragma("nounroll") for (unsigned spins = 0; spins < SPIN_CAP && !all_flags_asm(ws, x.tag, 8, lane); spins++) {}
      bar_only(); dma_wqkv(x, 7, wid); dma_w64<0, 2>(x, 0, wid * 2); }
    if (s == 47) {
      asm volatile("s_waitcnt vmcnt(22) lgkmcnt(0)\n\ts_barrier" ::: "memory");
      dma_w64<0, 2>(x, 1, wid * 2); dma_w64<0, 2>(x, 2, wid * 2);
    }
    if (s == 28) stats_load(x.rs, OFF_ST + 2 * L * 262144 + rt * 4096, lane, stv);
    if (s == 42) {
      UNROLL for (int i = 0; i < 3; i++) UNROLL for (int k = 0; k < 8; k++) csv[i][k] = *(LAS float*)(lds + A_AUX + ((hi * 8 + k) * 96 + i * 32 + r) * 4);
      UNROLL for (int i = 0; i < 2; i++) wph[i] = ll.rd(A_AUX + 6144 + i * 1024);
    }
    if (s == 44) {
      float mean;
      stats_finish(stv, -1, mean, rstd);
      mf = mean_frag(mean);
      UNROLL for (int i = 0; i < 3; i++) csf[i] = cs_frag(half_add(((csv[i][0] + csv[i][1]) + (csv[i][2] + csv[i][3])) + ((csv[i][4] + csv[i][5]) + (csv[i][6] + csv[i][7]))), hi);
    }
    if (s == 45) UNROLL for (int i = 0; i < 3; i++) acc[i] = MFMA32(csf[i], mf, acc[i]);
    if (s == 48) {
      if (L == 0) UNROLL for (int i = 0; i < 4; i++)
        res[i] = ld16(x.rs, i * 1024 + lane * 16, act_off(0) + ((x.grp * 2 + (x.idx & 1)) * 4 + (wid & 3)) * 65536 + (x.idx >> 1) * 4096);
    }
    if (s + 1 < NSTEP) bread(s + 1);
    SB();
    const bf16x8 av = a[s % PD];
    UNROLL for (int i = s < 48 ? 0 : 1; i < 3; i++) acc[i] = MFMA32(bf[s & 1][i], av, acc[i]);
    if (s == 48) asm volatile("" : "+v"(acc[0]));
    if (s == 44) asm volatile("" : "+v"(rstd), "+v"(wph[0]), "+v"(wph[1]));
    SB();
    UNROLL for (int n = ramp(s - 1, I0, PD); n < ramp(s, I0, PD); n++) a[n % PD] = aload(n);
    SB();
  });
  STAMP(4 * L, 2); STAMPC(4 * L, 15);
  const f32x16 zero = {};
  {
    UNROLL for (int i = 0; i < 16; i++) acc[2][i] *= rstd;
    f32x16 p = MFMA32(wph[0], pack8(acc[1], 0), zero);
    p = MFMA32(wph[1], pack8(acc[1], 1), p);
    const lds_ptr row = lds + A_IMG + (wid * 32 + r) * A_ROW + hi * 8;
    UNROLL for (int g = 0; g < 4; g++) *(LAS u32x2*)(row + 128 + g * 16) = (u32x2){cvtpk(acc[2][4 * g], acc[2][4 * g + 1]), cvtpk(acc[2][4 * g + 2], acc[2][4 * g + 3])};
    f32x16 ep, em;
    dual_softmax<false>(p, rstd * LOG2E, ep, em);
    UNROLL for (int g = 0; g < 4; g++) {
      *(LAS u32x2*)(row + g * 16) = (u32x2){cvtpk(ep[4 * g], ep[4 * g + 1]), cvtpk(ep[4 * g + 2], ep[4 * g + 3])};
      *(LAS u32x2*)(row + 64 + g * 16) = (u32x2){cvtpk(em[4 * g], em[4 * g + 1]), cvtpk(em[4 * g + 2], em[4 * g + 3])};
    }
  }
  STAMP(4 * L, 3);
  lgkm_bar();
  STAMP(4 * L, 4);
  const unsigned slab = OFF_SLAB + (L * 256 + h * 8) * SLAB_B;
  {
    __builtin_amdgcn_s_setprio(3);
    const int ft = wid >> 1, dt = wid & 1;
    const lds_ptr ip = lds + A_IMG + (8 * (lane >> 4) + q4) * A_ROW + (lane & 3) * 8;
    bf16x8 pf[8], vf[8];
    UNROLL for (int u = 0; u < 8; u++) { pf[u] = lds_trfrag<4 * A_ROW>(ip + u * 32 * A_ROW + ft * 32); vf[u] = lds_trfrag<4 * A_ROW>(ip + u * 32 * A_ROW + 128 + dt * 32); }
    SB();
    f32x4 kv = {}, z = {};
    UNROLL for (int u = 0; u < 8; u++) kv = MFMA16(pf[u], vf[u], kv);
    st16(x.rs, not_empty(kv), lane * 16, slab + tt * SLAB_B + wid * 1024);
    if (dt == 0) {
      const short one = (lane & 15) == 0 ? (short)0x3f80 : (short)0;
      const bf16x8 ones = {one, one, one, one, one, one, one, one};
      UNROLL for (int u = 0; u < 8; u++) z = MFMA16(pf[u], ones, z);
      if ((lane & 15) == 0) st16(x.rs, not_empty(z), lane, slab + tt * SLAB_B + 8192 + ft * 64);
    }
  }
  __builtin_amdgcn_s_setprio(0);
  STAMP(4 * L, 5);
  u32x4 gv[8];
  unsigned gz;
  auto gather = [&]() AI {
    UNROLL for (int t = 0; t < 8; t++)
      asm volatile("global_load_dwordx4 %0, %1, %2 sc1" : "=v"(gv[t]) : "v"(lane * 16), "s"(ws + slab + t * SLAB_B + wid * 1024) : "memory");
    asm volatile("global_load_dword %0, %1, %2 sc1" : "=v"(gz) : "v"((lane >> 3) * SLAB_B + (lane & 7) * 4), "s"(ws + slab + 8192 + wid * 32) : "memory");
  };
#define GATHER_WAIT(N)                                                                                                                   \
  asm volatile("s_waitcnt vmcnt(" #N ")"                                                                                                 \
               : "+v"(gv[0]), "+v"(gv[1]), "+v"(gv[2]), "+v"(gv[3]), "+v"(gv[4]), "+v"(gv[5]), "+v"(gv[6]), "+v"(gv[7]), "+v"(gz)::"memory")
  bf16x8 phq[4];
  float qs;
  {
    bf16x8 qw[16];
    UNROLL for (int s = 0; s < 16; s++) qw[s] = ll.rd(s * 3072);
    SB();
    UNROLL for (int s = 0; s < 16; s++) acc[0] = MFMA32(qw[s], a[s], acc[0]);
    asm volatile("" : "+v"(acc[0]));
    gather();
    SB();
    f32x16 p = MFMA32(wph[0], pack8(acc[0], 0), zero);
    p = MFMA32(wph[1], pack8(acc[0], 1), p);
    f32x16 ep, em;
    qs = dual_softmax<true>(p, rstd * LOG2E, ep, em);
    phq[0] = pack8(ep, 0); phq[1] = pack8(ep, 1); phq[2] = pack8(em, 0); phq[3] = pack8(em, 1);
    asm volatile("" : "+v"(phq[0]), "+v"(phq[1]), "+v"(phq[2]), "+v"(phq[3]));
  }
  GATHER_WAIT(0);
  STAMP(4 * L, 6);
  _Pragma("nounroll") for (unsigned spins = 0; spins < SPIN_CAP; spins++) {
    unsigned m = gz;
    UNROLL for (int t = 0; t < 8; t++) UNROLL for (int j = 0; j < 4; j++) m = m > gv[t][j] ? m : gv[t][j];
    if (all_lanes(m != EMPTY)) break;
    asm volatile("s_sleep 2" ::: "memory");
    gather();
    GATHER_WAIT(0);
  }
  STAMP(4 * L, 7);
  {
    f32x4 s;
    UNROLL for (int j = 0; j < 4; j++)
      s[j] = ((U2F(gv[0][j]) + U2F(gv[1][j])) + (U2F(gv[2][j]) + U2F(gv[3][j]))) + ((U2F(gv[4][j]) + U2F(gv[5][j])) + (U2F(gv[6][j]) + U2F(gv[7][j])));
    *(LAS u32x2*)(lds + A_KVL + (wid >> 1) * 1024 + ((wid & 1) * 16 + (lane & 15) + ((lane >> 4) & 1) * 32) * 16 + hi * 8) = (u32x2){cvtpk(s[0], s[1]), cvtpk(s[2], s[3])};
    float zs = U2F(gz);
    zs += shfl(zs, lane ^ 8); zs += shfl(zs, lane ^ 16); zs = half_add(zs);
    if (lane < 8) *(LAS u16*)(lds + A_ZL + (wid * 8 + lane) * 2) = (u16)cvtpk(zs, zs);
  }
  lgkm_bar();
  STAMP(4 * L, 8);
  {
    f32x16 num = zero, den = zero;
    UNROLL for (int fs = 0; fs < 4; fs++) {
      const bf16x8 kf = lds_b128(lds + A_KVL + fs * 1024 + lane * 16);
      const s16x4 z0 = *(LAS s16x4*)(lds + A_ZL + fs * 32 + hi * 8), z1 = *(LAS s16x4*)(lds + A_ZL + fs * 32 + hi * 8 + 16);
      num = MFMA32(kf, phq[fs], num);
      den = MFMA32(__builtin_shufflevector(z0, z1, 0, 1, 2, 3, 4, 5, 6, 7), phq[fs], den);
    }
    const float rd = 1.f / (den[0] + 1e-6f * qs);
    UNROLL for (int gp = 0; gp < 2; gp++) {
      float v[8];
      gather8(num, gp, v);
      st16(x.rs, (u32x4){cvtpk(v[0] * rd, v[1] * rd), cvtpk(v[2] * rd, v[3] * rd), cvtpk(v[4] * rd, v[5] * rd), cvtpk(v[6] * rd, v[7] * rd)},
           gp * 1024 + lane * 16, act_off(4 * L + 1) + rt * 65536 + h * 2048);
    }
  }
  wait_vm_bar<0>();
  if (wid == 7) set_flag(ws, 1u, 4 * L + 1, tt * 32 + h, lane);
  STAMP(4 * L, 9);
}

constexpr unsigned w64_tbl(int which) { unsigned t = 0; for (int c = 0; c < 8; c++) t |= (unsigned)(w64_at(which, c) >> 14) << (4 * c); return t; }
template <int WHICH, class Hook>
DI void gemm64(const Ctx& x, unsigned abase, f32x16 (&acc)[2], Hook hook) {
  const int lane = x.lane;
  auto aload = [&](unsigned base, int j) AI { return __builtin_bit_cast(bf16x8, ld16(x.rs, lane * 16 + (j & 3) * 1024, base + (j >> 2) * 4096)); };
  bf16x8 a[16], bf[4][2];
  UNROLL for (int j = 0; j < 16; j++) a[j] = aload(abase, j);
  const lds_ptr lb = x.lds + lane * 16;
  lds_ptr b0 = lb + w64_at(WHICH, 0);
  UNROLL for (int j = 0; j < 2; j++) UNROLL for (int n = 0; n < 2; n++) bf[j][n] = lds_b128(b0 + j * 2048 + n * 1024);
  auto trip = [&](int it, auto lastc) AI {
    constexpr bool LAST = decltype(lastc)::value;
    const unsigned t = w64_tbl(WHICH) >> (8 * it);
    const lds_ptr b1 = lb + ((t >> 4) & 15) * 16384, b2 = lb + ((t >> 8) & 15) * 16384;
    abase += 16384;
    static_for<16>([&](auto jc) AI {
      constexpr int j = decltype(jc)::value, r = j + 2;
      hook(it, j);
      if (!LAST || r < 16) UNROLL for (int n = 0; n < 2; n++) bf[r & 3][n] = lds_b128((r < 8 ? b0 : (r < 16 ? b1 : b2)) + (r & 7) * 2048 + n * 1024);
      SB();
      acc[0] = MFMA32(bf[j & 3][0], a[j], acc[0]);
      acc[1] = MFMA32(bf[j & 3][1], a[j], acc[1]);
      SB();
      if (!LAST) a[j] = aload(abase, j);
      SB();
    });
    b0 = b2;
  };
  _Pragma("nounroll") for (int it = 0; it < 3; it++) trip(it, IC<0>{});
  trip(3, IC<1>{});
}

template <bool AFTER_B>
DI void out_compute(const Ctx& x, int L, u32x4 (&res)[4], char* out) {
  char* ws = x.ws;
  const int lane = x.lane, hi = x.hi, ph = 4 * L + (AFTER_B ? 3 : 1);
  const bool last = AFTER_B && L == 1;
  const int pn = x.idx >> 1, rt = (x.grp * 2 + (x.idx & 1)) * 4 + x.wid, r = lane & 31;
  STAMP(ph, 0);
  bar_only();
  STAMP(ph, 3);
  f32x16 acc[2] = {{}, {}};
  gemm64<AFTER_B ? 2 : 0>(x, act_off(ph) + rt * 65536, acc, [&](int it, int j) AI {
    if ((j == 14 && it == 2) || (!AFTER_B && j == 6 && it == 1)) bar_only();
  });
  STAMP(ph, 1);
  float ss = 0.f, sq = 0.f;
  UNROLL for (int i = 0; i < 4; i++) {
    float v[8];
    gather8(acc[i >> 1], i & 1, v);
    UNROLL for (int j = 0; j < 4; j++) res[i][j] = cvtpk(v[2 * j] + bflo(res[i][j]), v[2 * j + 1] + bfhi(res[i][j]));
    if (last) *(GAS u32x4*)(out + (size_t)(rt * 32 + r) * 2048 + pn * 128 + i * 32 + hi * 16) = res[i];
    else st16(x.rs, res[i], i * 1024 + lane * 16, act_off(ph + 1) + rt * 65536 + pn * 4096);
    UNROLL for (int j = 0; j < 4; j++) { ss = dot2(res[i][j], 0x3f803f80u, ss); sq = dot2(res[i][j], res[i][j], sq); }
  }
  if (!last) {
    ss = half_add(ss); sq = half_add(sq);
    const u32x4 sp = stats_pair(ss, sq);
    if (lane < 16) st16(x.rs, sp, lane * 16, OFF_ST + ((ph + 1) >> 1) * 262144 + rt * 4096 + pn * 256);
  }
  bar_only();
  wait_vm_bar<0>();
  STAMP(ph, 2);
}
DI void phase_out_b(const Ctx& x, int L, u32x4 (&res)[4], char* out) {
  if (x.wid < 4) {
    out_compute<true>(x, L, res, out);
    if (x.wid == 0 && L == 0) set_flag(x.ws, 1u, 4, x.grp * 32 + (x.idx & 1) * 16 + (x.idx >> 1), x.lane);
    return;
  }
  const int w4 = x.wid - 4, id0 = x.grp * 32 + (x.idx & 1) * 16;
  if (w4 == 3) wait_flags(x.ws, 1u, 4 * L + 3, id0 + x.lane, x.lane < 16);
  else {
    UNROLL for (int c = 6; c < 8; c++) { dma_w64<2, 3>(x, c, w4 * 5); dma_w64<2, 2>(x, c, w4 * 5 + 3); if (w4 == c - 6) dma_w64<2, 1>(x, c, 15); }
    if (L == 0) UNROLL for (int i = 0; i < 3; i++) { if (w4 * 3 + i < 8) dma_wqkv(x, 0, w4 * 3 + i); }
  }
  bar_only();
  drain_vm();
  bar_only();
  if (L == 0) UNROLL for (int c = 1; c < 4; c++) UNROLL for (int i = 0; i < 2; i++) dma_wqkv(x, c, w4 * 2 + i);
  bar_only();
  bar_only();
}

constexpr int B_QX = 65536, B_PART = 98304;
constexpr int kv_at(int tile) { return (((tile >> 3) + 3) % 5) * 32768 + (tile & 7) * 4096; }
constexpr float QSCALE = 0.17677669529663687f * LOG2E;
DI void phase_out_a_b(const Ctx& x, int L, u32x4 (&res)[4]) {
  char* ws = x.ws;
  const lds_ptr lds = x.lds;
  const int lane = x.lane, wid = x.wid, hi = x.hi, g4 = lane >> 4, c16 = lane & 15, ph = 4 * L + 2;
  const int pm = x.grp * 2 + (x.idx & 1), pj = x.idx >> 1;
  const int rt = pm * 4 + (wid & 3), head = pj * 2 + (wid >> 2);
  const unsigned stb = OFF_ST + (2 * L + 1) * 262144, kvf = OFF_KVF + L * 262144;
  const int id0 = x.grp * 32 + (x.idx & 1) * 16;
  auto kv_dma = [&](int ch, int first) AI {
    gldsN_sc1<4>(ws + kvf + ((ch + x.grp) & 7) * 32768 + first * 1024, lane * 16, x.l0 + kv_at(ch * 8) + first * 1024);
  };
  auto kv_dma2 = [&](int ch, int first) AI {
    gldsN_sc1<2>(ws + kvf + ((ch + x.grp) & 7) * 32768 + first * 1024, lane * 16, x.l0 + kv_at(ch * 8) + first * 1024);
  };
  auto kv_all = [&]() AI { return all_flags(x.rs, 10 + L, lane); };
  bool kv_ok = true;
  bf16x8 qf[2];
  if (wid < 4) {
    out_compute<false>(x, L, res, nullptr);
    STAMP(ph, 0);
    u32x4 csv[4];
    stats_load(x.rs, OFF_CS + pj * 4096, lane, csv);
    bar_only();
    STAMP(ph, 1);
    f32x16 acc[2] = {{}, {}};
    u32x4 stv[4];
    float rstd;
    gemm64<1>(x, act_off(ph) + rt * 65536, acc, [&](int it, int j) AI {
      if (j == 0 && it == 3) stats_load(x.rs, stb + rt * 4096, lane, stv);
      if (j == 13 && it == 3) {
        float mean;
        stats_finish(stv, -1, mean, rstd);
        const bf16x8 mf = mean_frag(mean);
        float t[4];
        UNROLL for (int k = 0; k < 4; k++) t[k] = (U2F(csv[0][k]) + U2F(csv[1][k])) + (U2F(csv[2][k]) + U2F(csv[3][k]));
        UNROLL for (int n = 0; n < 2; n++) acc[n] = MFMA32(cs_frag(half_add(swap16_add(t[2 * n], t[2 * n + 1])), hi), mf, acc[n]);
      }
    });
    STAMP(ph, 2);
    const float sc = rstd * QSCALE;
    UNROLL for (int i = 0; i < 16; i++) { acc[0][i] *= sc; acc[1][i] *= sc; }
    qf[0] = pack8(acc[0], 0); qf[1] = pack8(acc[0], 1);
    bar_only();
    UNROLL for (int s = 0; s < 2; s++) *(LAS bf16x8*)(lds + B_QX + (wid * 2 + s) * 1024 + lane * 16) = pack8(acc[1], s);
    lgkm_bar();
  } else {
    const int w4 = wid - 4, krt = pm * 4 + (pj >> 2), kth = (pj >> 1) & 1, ktype = pj & 1, u = wid & 1;
    bf16x8 kx[8], kw[8][2];
    auto kw_load = [&]() AI {
      UNROLL for (int i = 0; i < 8; i++) UNROLL for (int n = 0; n < 2; n++)
        kw[i][n] = __builtin_bit_cast(bf16x8, ld16(x.rs, lane * 16 + n * 1024, OFF_WKVF + (ktype * 32 + w4 * 8 + i) * 2048));
    };
    if (wid == 7) wait_flags(ws, 1u, ph - 1, x.grp * 32 + lane, lane < 32);
    else {
      UNROLL for (int c = 3; c < 8; c++) { dma_w64<0, 3>(x, c, w4 * 5); dma_w64<0, 2>(x, c, w4 * 5 + 3); if (w4 == c % 3) dma_w64<0, 1>(x, c, 15); }
      UNROLL for (int c = 0; c < 2; c++) { dma_w64<1, 3>(x, c, w4 * 5); dma_w64<1, 2>(x, c, w4 * 5 + 3); if (w4 == c) dma_w64<1, 1>(x, c, 15); }
    }
    bar_only();
    wait_vm_bar<0>();
    if (L == 0) {
      const u32x4 e = {EMPTY, EMPTY, EMPTY, EMPTY};
      const int b = x.grp * 32 + x.idx;
      UNROLL for (int i = 0; i < 2; i++) st16(x.rs, e, lane * 16, OFF_SLAB + (256 + b) * SLAB_B + (w4 * 2 + i) * 1024);
      if (w4 == 0 && lane < 16) st16(x.rs, e, lane * 16, OFF_SLAB + (256 + b) * SLAB_B + 8192);
    }
    u32x4 cp[2] = {};
    if (wid >= 6) UNROLL for (int i = 0; i < 8; i++) cp[i >> 2][i & 3] = ld4(x.rs, i * 256 + lane * 4, OFF_CSKV + (ktype * 2 + u) * 2048);
    bar_only();
    UNROLL for (int c = 2; c < 7; c++) dma_w64<1, 4>(x, c, w4 * 4);
    kw_load();
    bar_only();
    dma_w64<1, 4>(x, 7, w4 * 4);
    volatile LAS unsigned* pcnt = (volatile LAS unsigned*)(lds + B_PART);
    if (wid == 6) { pcnt[0] = 0u; pcnt[16] = 0u; }
    bar_only();
    STAMP(ph, 0);
    if (wid == 5) { set_flag(ws, 1u, ph, id0 + pj, lane); wait_flags(ws, 1u, ph, id0 + lane, lane < 16); }
    f32x4 cs = {};
    if (wid >= 6) {
      const float c = half_add(swap16_add(sum8(cp), sum8(cp)));
      UNROLL for (int j = 0; j < 4; j++) { const float cj = shfl(c, 4 * g4 + j); cs[j] = ktype ? c : cj; }
    }
    asm volatile("s_waitcnt vmcnt(0) lgkmcnt(0)\n\ts_barrier" ::: "memory");
    __builtin_amdgcn_s_setprio(2);
    UNROLL for (int i = 0; i < 8; i++)
      kx[i] = __builtin_bit_cast(bf16x8, ld16(x.rs, (g4 >> 1) * 1024 + (16 * kth + c16 + 32 * (g4 & 1)) * 16 + (i & 1) * 2048,
                                              act_off(ph) + krt * 65536 + w4 * 16384 + (i >> 1) * 4096));
    u32x4 sv[4];
    if (wid >= 6) stats_load(x.rs, stb + krt * 4096, lane, sv);
    f32x4 d[2] = {{}, {}};
    if (ktype == 0) { UNROLL for (int i = 0; i < 8; i++) UNROLL for (int n = 0; n < 2; n++) d[n] = MFMA16(kw[i][n], kx[i], d[n]); }
    else { UNROLL for (int i = 0; i < 8; i++) UNROLL for (int n = 0; n < 2; n++) d[n] = MFMA16(kx[i], kw[i][n], d[n]); }
    UNROLL for (int n = 0; n < 2; n++) *(LAS f32x4*)(lds + B_PART + 1024 + (w4 * 2 + n) * 1024 + lane * 16) = d[n];
    asm volatile("s_waitcnt lgkmcnt(0)" ::: "memory");
    if (lane == 0) __hip_atomic_fetch_add((LAS unsigned*)(lds + B_PART), 1u, __ATOMIC_RELAXED, __HIP_MEMORY_SCOPE_WORKGROUP);
    if (wid >= 6) {
      float mean, rstd;
      stats_finish(sv, kth, mean, rstd);
      f32x4 m4, r4;
      UNROLL for (int j = 0; j < 4; j++) {
        const float mj = shfl(mean, 4 * g4 + j), rj = shfl(rstd, 4 * g4 + j);
        m4[j] = ktype ? mj : mean; r4[j] = ktype ? rj : rstd;
      }
      _Pragma("nounroll") for (unsigned spins = 0; spins < SPIN_CAP * 16 && pcnt[0] != 4u; spins++) __builtin_amdgcn_s_sleep(1);
      asm volatile("" ::: "memory");
      f32x4 e = -m4 * cs;
      UNROLL for (int w = 0; w < 4; w++) e += *(LAS f32x4*)(lds + B_PART + 1024 + (w * 2 + u) * 1024 + lane * 16);
      e *= r4;
      const int frag = ktype ? 2 + kth : u, lp = (ktype ? 16 * u : 16 * kth) + c16 + 32 * (g4 & 1);
      const unsigned e0 = cvtpk(e[0], e[1]), e1 = cvtpk(e[2], e[3]);
      const auto w0 = __builtin_amdgcn_permlane32_swap(e0, e0, false, false), w1 = __builtin_amdgcn_permlane32_swap(e1, e1, false, false);
      if (lane < 32) st16(x.rs, (u32x4){w0[0], w1[0], w0[1], w1[1]}, frag * 1024 + lp * 16, kvf + krt * 4096);
      if (last_arriver(lds + B_PART + 64, 2, lane)) set_flag(ws, 1u, 10 + L, x.grp * 32 + x.idx, lane);
    }
    __builtin_amdgcn_s_setprio(0);
    {
      STAMP(ph, 1);
      if (wid == 7) {
        wait_flags(ws, 1u, 10 + L, x.grp * 32 + lane, lane < 32);
        STAMP(ph, 2);
        UNROLL for (int i = 0; i < 8; i++) kv_dma(0, i * 4);
        kv_ok = kv_all();
        drain_vm();
        STAMP(ph, 7);
      }
    }
    __builtin_amdgcn_s_setprio(0);
    bar_only();
    if (wid == 7) *(LAS unsigned*)(lds + B_QX + 8192) = kv_ok;
    lgkm_bar();
    UNROLL for (int s = 0; s < 2; s++) qf[s] = lds_b128(lds + B_QX + ((wid & 3) * 2 + s) * 1024 + lane * 16);
  }
  __builtin_amdgcn_s_waitcnt(0x0F70);
  const bool kv_early = __builtin_amdgcn_readfirstlane(*(LAS unsigned*)(lds + B_QX + 8192)) != 0;
  STAMP(ph, 3); STAMPC(ph, 14);
  if (kv_early) kv_dma(1, wid * 4);
  const bf16x8 sel = sel_frag(lane);
  const f32x16 zero = {};
  f32x16 O = zero, negm, SA, SB_;
  f32x4 LS = {};
  bf16x8 kf[2], vf[2];
  const LdsLane ll(lds, lane);
  kf[0] = ll.rd(kv_at(0)); kf[1] = ll.rd(kv_at(0) + 1024);
  SA = MFMA32(kf[0], qf[0], zero); SA = MFMA32(kf[1], qf[1], SA);
  kf[0] = ll.rd(kv_at(1)); kf[1] = ll.rd(kv_at(1) + 1024);
  vf[0] = ll.rd(kv_at(0) + 2048); vf[1] = ll.rd(kv_at(0) + 3072);
  {
    float m = SA[0];
    UNROLL for (int i = 1; i < 16; i++) m = __builtin_fmaxf(m, SA[i]);
    m = half_max(m);
    UNROLL for (int i = 0; i < 16; i++) { negm[i] = -m; SA[i] -= m; }
  }
#define EX(i) Sc[i] = __builtin_amdgcn_exp2f(Sc[i])
#define PIN(x) asm volatile("" : "+v"(x))
  u32x4 p0, p1 = {0u, 0u, 0u, 0u};
  auto step = [&](f32x16& Sc, f32x16& Sn, int kp, int vc, int vp) AI {
    SB();
    Sn = MFMA32(kf[0], qf[0], negm);
    SB();
    kf[0] = ll.rd(kp);
    EX(0); EX(1); EX(2); PIN(Sc);
    SB();
    O = MFMA32(vf[1], __builtin_bit_cast(bf16x8, p1), O);
    SB();
    vf[1] = ll.rd(vc + 3072);
    EX(3); EX(4); EX(5); PIN(Sc);
    SB();
    Sn = MFMA32(kf[1], qf[1], Sn);
    SB();
    kf[1] = ll.rd(kp + 1024);
    EX(6); EX(7);
    p0[0] = cvtpk(Sc[0], Sc[1]); p0[1] = cvtpk(Sc[2], Sc[3]); p0[2] = cvtpk(Sc[4], Sc[5]); p0[3] = cvtpk(Sc[6], Sc[7]); PIN(p0);
    SB();
    LS = MFMA16(sel, __builtin_bit_cast(bf16x8, p1), LS);
    SB();
    EX(8); EX(9); PIN(Sc);
    SB();
    O = MFMA32(vf[0], __builtin_bit_cast(bf16x8, p0), O);
    SB();
    vf[0] = ll.rd(vp + 2048);
    EX(10); EX(11); EX(12); PIN(Sc);
    SB();
    LS = MFMA16(sel, __builtin_bit_cast(bf16x8, p0), LS);
    SB();
    EX(13); EX(14); EX(15);
    p1[0] = cvtpk(Sc[8], Sc[9]); p1[1] = cvtpk(Sc[10], Sc[11]); p1[2] = cvtpk(Sc[12], Sc[13]); p1[3] = cvtpk(Sc[14], Sc[15]); PIN(p1);
    SB();
  };
  auto hook = [&](int t) AI {
    if (t < 2) { if (kv_early) kv_dma(2 + t, wid * 4); }
    if (t == 2 && !kv_ok) {
      _Pragma("nounroll") for (unsigned spins = 0; spins < SPIN_CAP && !kv_all(); spins++) asm volatile("s_sleep 4" ::: "memory");
      _Pragma("nounroll") for (int i = 0; i < 8; i++) { kv_dma(1, i * 4); kv_dma(2, i * 4); kv_dma(3, i * 4); }
    }
    if (t == 6) wait_vm_bar<0>();
    if (t == 8 || t == 10) kv_dma2(4, wid * 4 + t - 8);
    if (t == 25) wait_vm_bar<0>();
    if (t >= 25 && t < 31) kv_dma2(5 + (t - 25) / 2, wid * 4 + ((t - 25) & 1) * 2);
    if (t == 38) wait_vm_bar<0>();
    if (t == 39 || t == 41) dma_w64<2, 2>(x, (t - 39) / 2, wid * 2);
    if (t == 49) bar_only();
    if (t >= 50 && t < 58 && !(t & 1)) dma_w64<2, 2>(x, 2 + (t - 50) / 2, wid * 2);
  };
  if (wid >= 4) __builtin_amdgcn_s_setprio(1);
  static_for<32>([&](auto tc) AI {
    constexpr int t = decltype(tc)::value * 2;
    hook(t);
    step(SA, SB_, kv_at((t + 2) & 63), kv_at(t), kv_at(t + 1));
    hook(t + 1);
    step(SB_, SA, kv_at((t + 3) & 63), kv_at(t + 1), kv_at((t + 2) & 63));
  });
  O = MFMA32(vf[1], __builtin_bit_cast(bf16x8, p1), O);
  LS = MFMA16(sel, __builtin_bit_cast(bf16x8, p1), LS);
  STAMP(ph, 4); STAMPC(ph, 15);
  __builtin_amdgcn_s_setprio(0);
  float l = LS[0];
  {
    float chk = l;
    UNROLL for (int i = 0; i < 16; i++) chk = __builtin_fmaxf(chk, __builtin_fabsf(O[i]) * 0x1p-27f);
    bool bad = !(chk < 0x1p100f);
    UNROLL for (int i = 0; i < 16; i++) bad |= (O[i] != O[i]);
    if (any_lane(bad)) {
      O = zero; l = 0.f;
      float m = -__builtin_inff();
      for (int t = 0; t < 64; t++) {
        bf16x8 f[4];
        UNROLL for (int i = 0; i < 4; i++) f[i] = __builtin_bit_cast(bf16x8, ld16(x.rs, i * 1024 + lane * 16, kvf + t * 4096));
        f32x16 s = MFMA32(f[0], qf[0], zero);
        s = MFMA32(f[1], qf[1], s);
        float mt = s[0];
        UNROLL for (int i = 1; i < 16; i++) mt = __builtin_fmaxf(mt, s[i]);
        const float mn = __builtin_fmaxf(m, half_max(mt));
        const float al = __builtin_amdgcn_exp2f(m - mn);
        m = mn;
        UNROLL for (int i = 0; i < 16; i++) { s[i] = __builtin_amdgcn_exp2f(s[i] - mn); O[i] *= al; }
        const bf16x8 p0 = pack8(s, 0), p1 = pack8(s, 1);
        f32x4 lt = MFMA16(sel, p0, ((f32x4){0.f, 0.f, 0.f, 0.f}));
        lt = MFMA16(sel, p1, lt);
        l = l * al + lt[0];
        O = MFMA32(f[2], p0, O);
        O = MFMA32(f[3], p1, O);
      }
    }
  }
  const float rl = 1.f / l;
  UNROLL for (int gp = 0; gp < 2; gp++) {
    float v[8];
    gather8(O, gp, v);
    st16(x.rs, (u32x4){cvtpk(v[0] * rl, v[1] * rl), cvtpk(v[2] * rl, v[3] * rl), cvtpk(v[4] * rl, v[5] * rl), cvtpk(v[6] * rl, v[7] * rl)},
         gp * 1024 + lane * 16, act_off(ph + 1) + rt * 65536 + head * 2048);
  }
  asm volatile("s_waitcnt vmcnt(0) lgkmcnt(0)\n\ts_barrier" ::: "memory");
  if (wid == 7) set_flag(ws, 1u, ph + 1, id0 + pj, lane);
  STAMP(ph, 5);
}

__global__ __launch_bounds__(512, 2) void k_main(char* ws, char* out, Inputs in) {
  extern __shared__ __attribute__((aligned(16))) char lds_raw[];
  Ctx x;
  x.ws = ws;
  x.rs = __builtin_amdgcn_make_buffer_rsrc(ws, 0, WS_USED, 0x00020000);
  x.lds = (lds_ptr)lds_raw;
  x.l0 = (unsigned)(uintptr_t)x.lds;
  x.lane = TID & 63;
  x.wid = __builtin_amdgcn_readfirstlane(TID >> 6);
  const int pg = BID & 7, pi = BID >> 3;
  x.ah = (pg >> 1) * 8 + (pi >> 2); x.at = (pg & 1) * 4 + (pi & 3);
  x.grp = (pg & 6) | (pi & 1); x.idx = (((pg & 1) * 8 + (pi >> 2)) << 1) | ((pi >> 1) & 1);
  x.hi = x.lane >> 5;
  x.tag = 0;
  x.tag = phase_p(fresh(x, 0), in, BID);
  { const Ctx y = fresh(x, 0); UNROLL for (int c = 0; c < 2; c++) dma_wqkv(y, c, y.wid); }
  u32x4 res[4] = {};
  for (int L = 0; L < 2; L++) {
    phase_a(fresh(x, 0), L, res);
    phase_out_a_b(fresh(x, 0), L, res);
    phase_out_b(fresh(x, 0), L, res, out);
  }
  if (BID == 0 && TID == 0) __hip_atomic_store((GAS u64*)(ws + OFF_EPOCH), x.tag, RLX_AGENT);
}

#if defined(__HIP_DEVICE_COMPILE__)
#pragma clang attribute pop
#endif

extern "C" void kernel_launch(void* const* d_in, const int* in_sizes, int n_in, void* d_out, int out_size, void* d_ws, size_t ws_size,
                              hipStream_t stream) {
  char* ws = (char*)d_ws;
  (void)hipFuncSetAttribute((const void*)k_main, hipFuncAttributeMaxDynamicSharedMemorySize, LDS_ALL);
  const Inputs in = {(const char*)d_in[0], (const char*)d_in[1], (const char*)d_in[2], (const char*)d_in[3],
                     (const char*)d_in[5], (const char*)d_in[6], (const char*)d_in[7], (const char*)d_in[8]};
  k_main<<<256, 512, LDS_ALL, stream>>>(ws, (char*)d_out, in);
}
```

```cpp
#include <hip/hip_runtime.h>
#include <hip/hip_bf16.h>
#include <stdint.h>

typedef unsigned short u16;
using bf16x8 = __attribute__((ext_vector_type(8))) short;
using s16x4 = __attribute__((ext_vector_type(4))) short;
using f32x16 = __attribute__((ext_vector_type(16))) float;
using f32x4 = __attribute__((ext_vector_type(4))) float;
using f32x2 = __attribute__((ext_vector_type(2))) float;
using u32x4 = __attribute__((ext_vector_type(4))) unsigned;
using u32x2 = __attribute__((ext_vector_type(2))) unsigned;
typedef __bf16 bf16x2_t __attribute__((ext_vector_type(2)));
#define LAS __attribute__((address_space(3)))
#define GAS __attribute__((address_space(1)))
typedef LAS char* lds_ptr;
typedef short v4i16_t __attribute__((ext_vector_type(4)));
#define DI __device__ __forceinline__
#define MFMA32(a, b, c) __builtin_amdgcn_mfma_f32_32x32x16_bf16((a), (b), (c), 0, 0, 0)
#define MFMA16(a, b, c) __builtin_amdgcn_mfma_f32_16x16x32_bf16((a), (b), (c), 0, 0, 0)
#define SB() __builtin_amdgcn_sched_barrier(0)
#define RLX_AGENT __ATOMIC_RELAXED, __HIP_MEMORY_SCOPE_AGENT
#define UNROLL _Pragma("unroll")
#define AI __attribute__((always_inline))

constexpr float LOG2E = 1.4426950408889634f;
constexpr unsigned SPIN_CAP = 1u << 20;

constexpr unsigned MBy = 1u << 20;
constexpr unsigned OFF_EPOCH = 0;
constexpr unsigned OFF_FL = 768 * 1024;
constexpr unsigned OFF_CSKV = 120 * 1024;
constexpr unsigned OFF_CS = 512 * 1024;
constexpr unsigned OFF_WKVF = 128 * 1024;
constexpr unsigned OFF_AUX = 256 * 1024;
constexpr unsigned AUX_B = 8192;
constexpr unsigned OFF_ST = 1 * MBy;
constexpr unsigned OFF_KVF = 2 * MBy;
constexpr unsigned OFF_SLAB = 4 * MBy;
constexpr unsigned SLAB_B = 8448;
constexpr unsigned OFF_ACT = 16 * MBy;
constexpr unsigned OFF_WTA = 48 * MBy;
constexpr unsigned OFF_WT64 = 54 * MBy;
constexpr unsigned WS_USED = 60 * MBy;

#if defined(__HIP_DEVICE_COMPILE__)
#pragma clang attribute push(__attribute__((target("no-packed-fp32-ops"))), apply_to = function)
#endif
#define U2F(x) __builtin_bit_cast(float, (unsigned)(x))
#define F2U(x) __builtin_bit_cast(unsigned, (float)(x))
#define TID ((int)__builtin_amdgcn_workitem_id_x())
#define BID ((int)__builtin_amdgcn_workgroup_id_x())
DI bool all_lanes(bool p) { return __builtin_amdgcn_ballot_w64(!p) == 0; }
DI bool any_lane(bool p) { return __builtin_amdgcn_ballot_w64(p) != 0; }
DI float shfl(float v, int src) { return U2F(__builtin_amdgcn_ds_bpermute(src * 4, (int)F2U(v))); }
#define SEGW(slot) do { if (TID == 0) __hip_atomic_store((GAS unsigned long long*)(ws + 200 * (size_t)MBy) + BID * 16 + (slot), __builtin_amdgcn_s_memrealtime(), RLX_AGENT); } while (0)
#define STAMP(k, i) do { if ((k) == 0 && (i) == 0) SEGW(0); if ((k) == 0 && (i) == 0) SEGW(1); } while (0)
#define STAMPC(k, i)
DI void glds16(const void* sbase, unsigned voff, unsigned lds_base) {
  unsigned sv;
  asm volatile("s_mov_b32 %0, m0\n\ts_mov_b32 m0, %3\n\ts_nop 0\n\tglobal_load_lds_dwordx4 %1, %2\n\ts_mov_b32 m0, %0"
               : "=&s"(sv) : "v"(voff), "s"(sbase), "s"(lds_base) : "memory");
}
#define GL_(o, sc) "global_load_lds_dwordx4 %1, %2 offset:" #o sc "\n\t"
#define GLDS_N(NAME, SC)                                                                                                   \
  template <int N> DI void NAME(const void* sbase, unsigned voff, unsigned lds_base) {                                     \
    unsigned sv;                                                                                                           \
    if constexpr (N == 1)                                                                                                  \
      asm volatile("s_mov_b32 %0, m0\n\ts_mov_b32 m0, %3\n\ts_nop 0\n\t" GL_(0, SC) "s_mov_b32 m0, %0"                    \
                   : "=&s"(sv) : "v"(voff), "s"(sbase), "s"(lds_base) : "memory");                                         \
    else if constexpr (N == 2)                                                                                                  \
      asm volatile("s_mov_b32 %0, m0\n\ts_mov_b32 m0, %3\n\ts_nop 0\n\t" GL_(0, SC) GL_(1024, SC) "s_mov_b32 m0, %0"       \
                   : "=&s"(sv) : "v"(voff), "s"(sbase), "s"(lds_base) : "memory");                                         \
    else if constexpr (N == 3)                                                                                             \
      asm volatile("s_mov_b32 %0, m0\n\ts_mov_b32 m0, %3\n\ts_nop 0\n\t" GL_(0, SC) GL_(1024, SC) GL_(2048, SC) "s_mov_b32 m0, %0" \
                   : "=&s"(sv) : "v"(voff), "s"(sbase), "s"(lds_base) : "memory");                                         \
    else                                                                                                                   \
      asm volatile("s_mov_b32 %0, m0\n\ts_mov_b32 m0, %3\n\ts_nop 0\n\t" GL_(0, SC) GL_(1024, SC) GL_(2048, SC) GL_(3072, SC) "s_mov_b32 m0, %0" \
                   : "=&s"(sv) : "v"(voff), "s"(sbase), "s"(lds_base) : "memory");                                         \
  }
GLDS_N(gldsN_sc1, " sc1")
DI void glds16_sc1(const void* sbase, unsigned voff, unsigned lds_base) {
  unsigned sv;
  asm volatile("s_mov_b32 %0, m0\n\ts_mov_b32 m0, %3\n\ts_nop 0\n\tglobal_load_lds_dwordx4 %1, %2 sc1\n\ts_mov_b32 m0, %0"
               : "=&s"(sv) : "v"(voff), "s"(sbase), "s"(lds_base) : "memory");
}
template <int N> DI void wait_vm_bar() { asm volatile("s_waitcnt vmcnt(%0)\n\ts_barrier" ::"n"(N) : "memory"); }
DI void bar_only() { asm volatile("s_barrier" ::: "memory"); }
DI void lgkm_bar() { asm volatile("s_waitcnt lgkmcnt(0)\n\ts_barrier" ::: "memory"); }
DI void drain_vm() { asm volatile("s_waitcnt vmcnt(0)" ::: "memory"); }
DI s16x4 lds_tr(lds_ptr p) { return __builtin_bit_cast(s16x4, __builtin_amdgcn_ds_read_tr16_b64_v4i16((LAS v4i16_t*)p)); }
template <int E> DI bf16x8 lds_trfrag(lds_ptr p) { s16x4 lo = lds_tr(p), hi = lds_tr(p + E); return __builtin_shufflevector(lo, hi, 0, 1, 2, 3, 4, 5, 6, 7); }
DI bf16x8 lds_b128(lds_ptr p) { return *(LAS bf16x8*)p; }
DI unsigned cvtpk(float lo, float hi) {
  bf16x2_t v = __builtin_convertvector((f32x2){lo, hi}, bf16x2_t);
  return __builtin_bit_cast(unsigned, v);
}
DI float bflo(unsigned u) { return U2F(u << 16); }
DI float bfhi(unsigned u) { return U2F(u & 0xffff0000u); }
DI float dot2(unsigned a, unsigned b, float c) {
  return __builtin_amdgcn_fdot2_f32_bf16(__builtin_bit_cast(bf16x2_t, a), __builtin_bit_cast(bf16x2_t, b), c, false);
}
DI float half_add(float x) { auto r = __builtin_amdgcn_permlane32_swap(F2U(x), F2U(x), false, false); return U2F(r[0]) + U2F(r[1]); }
DI float half_max(float x) { auto r = __builtin_amdgcn_permlane32_swap(F2U(x), F2U(x), false, false); return __builtin_fmaxf(U2F(r[0]), U2F(r[1])); }
DI float half_min(float x) { auto r = __builtin_amdgcn_permlane32_swap(F2U(x), F2U(x), false, false); return __builtin_fminf(U2F(r[0]), U2F(r[1])); }
DI bf16x8 pack8(const f32x16& x, int s) {
  u32x4 p = {cvtpk(x[8 * s], x[8 * s + 1]), cvtpk(x[8 * s + 2], x[8 * s + 3]), cvtpk(x[8 * s + 4], x[8 * s + 5]), cvtpk(x[8 * s + 6], x[8 * s + 7])};
  return __builtin_bit_cast(bf16x8, p);
}
DI void gather8(const f32x16& a, int gp, float (&v)[8]) {
  UNROLL for (int j = 0; j < 4; j++) {
    auto s = __builtin_amdgcn_permlane32_swap(F2U(a[8 * gp + j]), F2U(a[8 * gp + 4 + j]), false, false);
    v[j] = U2F(s[0]); v[4 + j] = U2F(s[1]);
  }
}
typedef __amdgpu_buffer_rsrc_t rsrc_t;
DI u32x4 ld16(rsrc_t rs, unsigned voff, unsigned soff) { return __builtin_amdgcn_raw_buffer_load_b128(rs, voff, soff, 16); }
DI unsigned ld4(rsrc_t rs, unsigned voff, unsigned soff) { return __builtin_amdgcn_raw_buffer_load_b32(rs, voff, soff, 16); }
DI void st16(rsrc_t rs, u32x4 v, unsigned voff, unsigned soff) { __builtin_amdgcn_raw_buffer_store_b128(v, rs, voff + soff, 0, 16); }
DI void st8(rsrc_t rs, u32x2 v, unsigned voff, unsigned soff) { __builtin_amdgcn_raw_buffer_store_b64(v, rs, voff, soff, 16); }
DI void st4(rsrc_t rs, unsigned v, unsigned voff, unsigned soff) { __builtin_amdgcn_raw_buffer_store_b32(v, rs, voff, soff, 16); }
constexpr unsigned EMPTY = 0xffffffffu;
DI u32x4 not_empty(f32x4 v) {
  u32x4 r;
  UNROLL for (int j = 0; j < 4; j++) r[j] = F2U(v[j]) < EMPTY ? F2U(v[j]) : EMPTY - 1;
  return r;
}
typedef unsigned long long u64;
template <class T> DI GAS T* flag_at(char* ws, int slot, int id) { return (GAS T*)(ws + OFF_FL + (id >> 2) * 4096 + slot * 128) + (id & 3); }
template <class T> DI void set_flag(char* ws, T val, int slot, int id, int lane) {
  if (lane == 0) __hip_atomic_store(flag_at<T>(ws, slot, id), val, RLX_AGENT);
}
template <class T> DI void wait_flags(char* ws, T val, int slot, int id, bool on) {
  GAS T* p = flag_at<T>(ws, slot, id);
  _Pragma("nounroll") for (unsigned spins = 0; spins < SPIN_CAP; spins++) {
    T v = val;
    if (on) v = __hip_atomic_load(p, RLX_AGENT);
    if (all_lanes(v == val)) break;
    __builtin_amdgcn_s_sleep(1);
  }
  asm volatile("" ::: "memory");
}
DI bool all_flags(rsrc_t rs, int slot, int lane) {
  const u32x4 v = ld16(rs, lane * 4096, OFF_FL + slot * 128);
  return all_lanes((v[0] & v[1] & v[2] & v[3]) == 1u);
}
DI bool all_flags_asm(char* ws, u64 tag, int slot, int lane) {
  u32x4 a, b;
  asm volatile("global_load_dwordx4 %0, %2, %3 sc1\n\tglobal_load_dwordx4 %1, %2, %3 offset:16 sc1\n\ts_waitcnt vmcnt(0)"
               : "=&v"(a), "=&v"(b) : "v"(lane * 4096), "s"(ws + OFF_FL + slot * 128) : "memory");
  const unsigned lo = (unsigned)tag, hi = (unsigned)(tag >> 32);
  return all_lanes(a[0] == lo && a[2] == lo && b[0] == lo && b[2] == lo && a[1] == hi && a[3] == hi && b[1] == hi && b[3] == hi);
}
DI bool last_arriver(lds_ptr cnt, unsigned n, int lane) {
  drain_vm();
  unsigned old = 0;
  if (lane == 0) old = __hip_atomic_fetch_add((LAS unsigned*)cnt, 1u, __ATOMIC_RELAXED, __HIP_MEMORY_SCOPE_WORKGROUP);
  return (unsigned)__builtin_amdgcn_readfirstlane(old) == n - 1;
}
DI float swap16_add(float a, float b) {
  auto r = __builtin_amdgcn_permlane16_swap(F2U(a), F2U(b), false, false);
  return U2F(r[0]) + U2F(r[1]);
}
DI float sum8(const u32x4 (&v)[2]) {
  return ((U2F(v[0][0]) + U2F(v[0][1])) + (U2F(v[0][2]) + U2F(v[0][3]))) + ((U2F(v[1][0]) + U2F(v[1][1])) + (U2F(v[1][2]) + U2F(v[1][3])));
}
DI void stats_load(rsrc_t rs, unsigned base, int lane, u32x4 (&v)[4]) {
  UNROLL for (int i = 0; i < 4; i++) v[i] = ld16(rs, lane * 16 + i * 1024, base);
}
DI u32x4 stats_pair(float s, float q) {
  auto a = __builtin_amdgcn_permlane16_swap(F2U(s), F2U(s), false, false), b = __builtin_amdgcn_permlane16_swap(F2U(q), F2U(q), false, false);
  return (u32x4){a[0], b[0], a[1], b[1]};
}
DI void stats_finish(const u32x4 (&v)[4], int half, float& mean, float& rstd) {
  float t[4];
  UNROLL for (int j = 0; j < 4; j++) t[j] = (U2F(v[0][j]) + U2F(v[1][j])) + (U2F(v[2][j]) + U2F(v[3][j]));
  if (half == 0) { t[2] = t[0]; t[3] = t[1]; }
  if (half == 1) { t[0] = t[2]; t[1] = t[3]; }
  const float s = half_add(swap16_add(t[0], t[2])), q = half_add(swap16_add(t[1], t[3]));
  mean = s * (1.f / 1024.f);
  rstd = __builtin_amdgcn_rsqf(__builtin_fmaxf(q * (1.f / 1024.f) - mean * mean, 0.f) + 1e-5f);
}
DI void split3(float x, unsigned& a, unsigned& b, unsigned& c) {
  a = cvtpk(x, x) & 0xffffu; x -= bflo(a);
  b = cvtpk(x, x) & 0xffffu; x -= bflo(b);
  c = cvtpk(x, x) & 0xffffu;
}
DI bf16x8 cs_frag(float cs, int hi) {
  unsigned a, b, c; split3(cs, a, b, c);
  u32x4 p = {a | (b << 16), a | (c << 16), b | (a << 16), 0u};
  if (hi) p = (u32x4){0u, 0u, 0u, 0u};
  return __builtin_bit_cast(bf16x8, p);
}
DI bf16x8 mean_frag(float mean) {
  unsigned a, b, c; split3(-mean, a, b, c);
  u32x4 p = {a | (a << 16), b | (a << 16), b | (c << 16), 0u};
  return __builtin_bit_cast(bf16x8, p);
}
constexpr int ramp(int s, int I0, int PD) { int n = I0 + 2 * (s + 1); if (n > s + 1 + PD) n = s + 1 + PD; return n > 64 ? 64 : n; }
template <int V> struct IC { static constexpr int value = V; };
template <int N, int I = 0, class F> DI void static_for(F&& f) {
  if constexpr (I < N) { f(IC<I>{}); static_for<N, I + 1>(f); }
}
template <bool RATIO> DI float dual_softmax(const f32x16& p, float c, f32x16& ep, f32x16& em) {
  float mx = p[0], mn = p[0];
  UNROLL for (int i = 1; i < 16; i++) { mx = __builtin_fmaxf(mx, p[i]); mn = __builtin_fminf(mn, p[i]); }
  mx = half_max(mx); mn = half_min(mn);
  const float a = -mx * c, b = mn * c;
  float sp = 0.f, sm = 0.f;
  UNROLL for (int i = 0; i < 16; i++) {
    ep[i] = __builtin_amdgcn_exp2f(__builtin_fmaf(p[i], c, a));
    em[i] = __builtin_amdgcn_exp2f(__builtin_fmaf(p[i], -c, b));
    sp += ep[i]; sm += em[i];
  }
  sp = half_add(sp); sm = __builtin_amdgcn_rcpf(half_add(sm));
  if (RATIO) { sm *= sp; UNROLL for (int i = 0; i < 16; i++) em[i] *= sm; }
  else { const float rp = __builtin_amdgcn_rcpf(sp); UNROLL for (int i = 0; i < 16; i++) { ep[i] *= rp; em[i] *= sm; } }
  return sp;
}
DI bf16x8 sel_frag(int lane) {
  short v = ((lane & 3) == 0 && ((lane >> 2) & 1) == ((lane >> 4) & 1)) ? (short)0x3f80 : (short)0;
  return (bf16x8){v, v, v, v, v, v, v, v};
}

struct Ctx {
  char* ws; rsrc_t rs; lds_ptr lds; unsigned l0; u64 tag;
  int lane, wid, grp, idx, hi, ah, at;
};
DI Ctx fresh(const Ctx& x, int swap) {
  Ctx y = x;
  y.wid ^= swap;
  asm volatile("" : "+s"(y.wid), "+s"(y.grp), "+s"(y.idx), "+s"(y.ah), "+s"(y.at));
  asm volatile("" : "+v"(y.lane));
  y.lane &= 63;
  y.hi = y.lane >> 5;
  return y;
}
struct LdsLane {
  lds_ptr b0, b1, b2;
  DI LdsLane(lds_ptr lds, int lane) : b0(lds + lane * 16), b1(b0 + 65536), b2(b0 + 131072) { asm volatile("" : "+v"(b0), "+v"(b1), "+v"(b2)); }
  DI bf16x8 rd(int off) const { return lds_b128((off < 65536 ? b0 : (off < 131072 ? b1 : b2)) + (off & 65535)); }
};
constexpr int LDS_ALL = 163840;
constexpr int A_SLOT = 24576, A_ROW = 200, A_IMG = 106496, A_KVL = A_IMG + 256 * A_ROW, A_ZL = A_KVL + 4096, A_AUX = 6 * A_SLOT;
static_assert(A_ZL + 128 <= LDS_ALL, "");
DI unsigned act_off(int i) { return OFF_ACT + i * 4 * MBy; }

DI int a_head(const Ctx& x) { return x.ah; }
DI int a_tile(const Ctx& x) { return x.at; }
DI void dma_wqkv(const Ctx& x, int c, int pg) {
  gldsN_sc1<3>(x.ws + OFF_WTA + a_head(x) * 196608 + c * A_SLOT + pg * 3072, x.lane * 16, x.l0 + (c % 6) * A_SLOT + pg * 3072);
}
constexpr int w64_at(int which, int c) {
  constexpr int t[3][8] = {{48, 64, 80, 0, 16, 32, 96, 112}, {128, 144, 48, 64, 80, 0, 16, 32}, {32, 48, 64, 80, 96, 112, 128, 144}};
  return t[which][c] * 1024;
}
template <int WHICH, int N> DI void dma_w64(const Ctx& x, int c, int p) {
  gldsN_sc1<N>(x.ws + OFF_WT64 + WHICH * 2 * MBy + (x.idx >> 1) * 131072 + c * 16384 + p * 1024, x.lane * 16, x.l0 + w64_at(WHICH, c) + p * 1024);
}
constexpr int MAIN_CODE_BYTES = 44 * 1024;
struct Inputs { const char *x0, *wqkv, *wphi, *woA, *wq, *wk, *wv, *woB; };
DI u64 phase_p(const Ctx& x, const Inputs& in, int bid) {
  char* ws = x.ws;
  STAMP(0, 0);
  const u64 epoch = __hip_atomic_load((GAS u64*)(ws + OFF_EPOCH), RLX_AGENT);
  const lds_ptr lds = x.lds;
  const int lane = x.lane, wid = x.wid, hi = x.hi, r = lane & 31, tid = wid * 64 + lane;
  const int kb = bid & 15, cu = bid >> 4;
  UNROLL for (int g = 0; g < 3; g++)
    glds16(in.wqkv + (size_t)(kb * 64 + wid * 8) * 6144 + g * 2048 + cu * 128, (lane >> 3) * 6144 + (lane & 7) * 16, x.l0 + g * 8192 + wid * 1024);
  const int rt = bid >> 2, ct = (bid & 3) * 4 + (wid & 3);
  u32x4 xv[4];
  if (wid < 4) UNROLL for (int i = 0; i < 4; i++) xv[i] = *(const GAS u32x4*)(in.x0 + (size_t)(rt * 32 + r) * 2048 + ct * 128 + i * 32 + hi * 16);
  bf16x8 sm;
  if (wid == 7 && bid < 64) UNROLL for (int j = 0; j < 8; j++)
    sm[j] = ((const GAS short*)in.wphi)[(bid >> 1) * 1024 + (16 * (bid & 1) + 8 * (j >> 2) + 4 * hi + (j & 3)) * 32 + r];
  if (wid == 7 && bid >= 128) {
    const GAS short* W = (const GAS short*)(bid >= 192 ? in.wv : in.wk);
    UNROLL for (int j = 0; j < 8; j++) sm[j] = W[(32 * ((bid >> 1) & 31) + 8 * (lane >> 4) + j) * 32 + 16 * (bid & 1) + (lane & 15)];
  }
  if (wid == 5 && bid < 64) {
    u64 pc;
    asm volatile("s_getpc_b64 s[40:41]\n\ts_add_u32 s40, s40, _Z6k_mainPcS_6Inputs@rel32@lo+4\n\ts_addc_u32 s41, s41, _Z6k_mainPcS_6Inputs@rel32@hi+12\n\ts_mov_b64 %0, s[40:41]"
                 : "=s"(pc) : : "s40", "s41", "scc");
    const int off = ((bid >> 3) * 64 + lane) * 128;
    if (off < MAIN_CODE_BYTES) { const unsigned t = *(const GAS unsigned*)(pc + off); asm volatile("" ::"v"(t)); }
  }
  wait_vm_bar<0>();
  UNROLL for (int g = 0; g < 3; g++)
    glds16((g == 0 ? in.woA : (g == 1 ? in.wq : in.woB)) + (size_t)(kb * 64 + wid * 8) * 2048 + cu * 128, (lane >> 3) * 2048 + (lane & 7) * 16,
           x.l0 + 24576 + g * 8192 + wid * 1024);
  const int ks = wid >> 1, nh = wid & 1, step = kb * 4 + ks;
  const lds_ptr p = lds + (ks * 16 + 8 * hi + ((lane & 15) >> 2)) * 128 + nh * 64 + ((lane >> 4) & 1) * 32 + (lane & 3) * 8;
  LAS float* red = (LAS float*)(lds + 49152);
  auto colsum = [&](u32x4 fr, int at) AI {
    float c = 0.f;
    UNROLL for (int k = 0; k < 4; k++) c = dot2(fr[k], 0x3f803f80u, c);
    c = half_add(c);
    if (hi == 0) red[ks * 256 + at + r] = c;
  };
  UNROLL for (int g = 0; g < 3; g++) {
    const u32x4 fr = __builtin_bit_cast(u32x4, lds_trfrag<512>(p + g * 8192));
    st16(x.rs, fr, lane * 16, OFF_WTA + ((cu * 2 + nh) * 192 + step * 3 + g) * 1024);
    colsum(fr, nh * 96 + g * 32);
  }
  if (wid < 4) {
    float s = 0, q = 0;
    UNROLL for (int i = 0; i < 4; i++) {
      st16(x.rs, xv[i], lane * 16, OFF_ACT + rt * 65536 + (ct * 4 + i) * 1024);
      UNROLL for (int j = 0; j < 4; j++) { s = dot2(xv[i][j], 0x3f803f80u, s); q = dot2(xv[i][j], xv[i][j], q); }
    }
    const u32x4 sp = stats_pair(half_add(s), half_add(q));
    if (lane < 16) st16(x.rs, sp, lane * 16, OFF_ST + rt * 4096 + ct * 256);
  }
  if (wid == 7 && bid < 64) st16(x.rs, __builtin_bit_cast(u32x4, sm), lane * 16, OFF_AUX + (bid >> 1) * AUX_B + 6144 + (bid & 1) * 1024);
  lgkm_bar();
  if (wid == 6 && lane < 48) {
    const f32x4 t = (*(LAS f32x4*)(red + lane * 4) + *(LAS f32x4*)(red + 256 + lane * 4)) + (*(LAS f32x4*)(red + 512 + lane * 4) + *(LAS f32x4*)(red + 768 + lane * 4));
    st16(x.rs, __builtin_bit_cast(u32x4, t), (lane >= 24 ? AUX_B - 384 : 0) + lane * 16, OFF_AUX + cu * 2 * AUX_B + kb * 384);
  }
  asm volatile("s_waitcnt vmcnt(0) lgkmcnt(0)\n\ts_barrier" ::: "memory");
  STAMP(0, 11);
  const u64 tag = (((u64)(unsigned)__builtin_amdgcn_readfirstlane((unsigned)(epoch >> 32)) << 32) | (unsigned)__builtin_amdgcn_readfirstlane((unsigned)epoch)) *
                      6364136223846793005ull + 1442695040888963407ull;
  if (wid == 0) set_flag(ws, tag, 0, bid, lane);
  UNROLL for (int g = 0; g < 3; g++) {
    const u32x4 fr = __builtin_bit_cast(u32x4, lds_trfrag<512>(p + 24576 + g * 8192));
    st16(x.rs, fr, lane * 16, OFF_WT64 + g * 2 * MBy + (cu * 128 + step * 2 + nh) * 1024);
    if (g == 1) colsum(fr, nh * 32);
  }
  if (wid == 5 && lane < 24) {
    const int slot = (bid & 3) * 3 + (lane >> 3);
    if (slot != 0 && slot != 8) st16(x.rs, (u32x4){0u, 0u, 0u, 0u}, slot * 128 + (lane & 7) * 16, OFF_FL + (bid >> 2) * 4096);
  }
  const u32x4 e = {EMPTY, EMPTY, EMPTY, EMPTY};
  st16(x.rs, e, tid * 16, OFF_SLAB + (x.ah * 8 + x.at) * SLAB_B);
  if (tid < 16) st16(x.rs, e, tid * 16, OFF_SLAB + (x.ah * 8 + x.at) * SLAB_B + 8192);
  if (wid == 7 && bid >= 128) {
    st16(x.rs, __builtin_bit_cast(u32x4, sm), lane * 16, OFF_WKVF + (bid - 128) * 1024);
    float c = 0.f;
    UNROLL for (int k = 0; k < 4; k++) c = dot2(__builtin_bit_cast(u32x4, sm)[k], 0x3f803f80u, c);
    c = half_add(swap16_add(c, c));
    if (lane < 16) st4(x.rs, F2U(c), lane * 4, OFF_CSKV + ((bid >= 192) * 2 + (bid & 1)) * 2048 + ((bid >> 1) & 31) * 64);
  }
  lgkm_bar();
  if (wid == 6 && lane < 16) {
    u32x4 t;
    UNROLL for (int j = 0; j < 4; j++) t[j] = F2U((red[j * 16 + lane] + red[256 + j * 16 + lane]) + (red[512 + j * 16 + lane] + red[768 + j * 16 + lane]));
    st16(x.rs, t, lane * 16, OFF_CS + cu * 4096 + kb * 256);
  }
  if (wid == 0) wait_flags(ws, tag, 0, lane < 16 ? (x.ah >> 1) * 16 + lane : (lane < 48 ? x.at * 32 + lane - 16 : x.ah * 2 + (lane & 1)), lane < 50);
  asm volatile("s_waitcnt vmcnt(0) lgkmcnt(0)\n\ts_barrier" ::: "memory");
  if (wid == 0) set_flag(ws, tag, 8, bid, lane);
  return tag;
}

DI void phase_a(const Ctx& x, int L, u32x4 (&res)[4]) {
  char* ws = x.ws;
  const int lane = x.lane, wid = x.wid, hi = x.hi, h = a_head(x), tt = a_tile(x), r = lane & 31, rt = tt * 8 + wid;
  const lds_ptr lds = x.lds;
  constexpr int NSTEP = 64, PD = 16, I0 = 6;
  if (L) { STAMP(4 * L, 0); }
  auto aux = [&]() AI { gldsN_sc1<1>(ws + OFF_AUX + h * AUX_B + wid * 1024, lane * 16, x.l0 + A_AUX + wid * 1024); };
  float csv[3][8];
  bf16x8 wph[2], csf[3], mf;
  float rstd;
  if (L) {
    if (wid == 0) wait_flags(ws, 1u, 4 * L, tt * 32 + lane, lane < 32);
    UNROLL for (int c = 4; c < 6; c++) dma_wqkv(x, c, wid);
    aux();
    wait_vm_bar<6>();
  }

  STAMP(4 * L, 10);
  const unsigned abase = act_off(4 * L) + rt * 65536;
  auto aload = [&](int s) AI { return __builtin_bit_cast(bf16x8, ld16(x.rs, lane * 16 + (s & 3) * 1024, abase + (s >> 2) * 4096)); };
  bf16x8 a[PD];
  UNROLL for (int s = 0; s < I0; s++) a[s] = aload(s);
  f32x16 acc[3] = {{}, {}, {}};
  const int q4 = (lane & 15) >> 2;
  bf16x8 bf[2][3];
  const LdsLane ll(lds, lane);
  auto bread = [&](int s) AI { UNROLL for (int i = s < 48 ? 0 : 1; i < 3; i++) bf[s & 1][i] = ll.rd(((s % 48) * 3 + i) * 1024); };
  u32x4 stv[4];
  if (L == 0) {
    UNROLL for (int c = 2; c < 6; c++) dma_wqkv(x, c, wid);
    aux();
    wait_vm_bar<I0 + 12>();
  }
  STAMP(4 * L, 1); STAMPC(4 * L, 14);
  bread(0);
  static_for<NSTEP>([&](auto sc) AI {
    constexpr int s = decltype(sc)::value;
    if (s == 15) { wait_vm_bar<ramp(14, I0, PD) - I0>(); dma_wqkv(x, 6, wid); }
    if (s == 25) {
      if (L == 0 && wid == 0)
        _Pragma("nounroll") for (unsigned spins = 0; spins < SPIN_CAP && !all_flags_asm(ws, x.tag, 8, lane); spins++) {}
      bar_only(); dma_wqkv(x, 7, wid); dma_w64<0, 2>(x, 0, wid * 2); }
    if (s == 47) {
      asm volatile("s_waitcnt vmcnt(22) lgkmcnt(0)\n\ts_barrier" ::: "memory");
      dma_w64<0, 2>(x, 1, wid * 2); dma_w64<0, 2>(x, 2, wid * 2);
    }
    if (s == 28) stats_load(x.rs, OFF_ST + 2 * L * 262144 + rt * 4096, lane, stv);
    if (s == 42) {
      UNROLL for (int i = 0; i < 3; i++) UNROLL for (int k = 0; k < 8; k++) csv[i][k] = *(LAS float*)(lds + A_AUX + ((hi * 8 + k) * 96 + i * 32 + r) * 4);
      UNROLL for (int i = 0; i < 2; i++) wph[i] = ll.rd(A_AUX + 6144 + i * 1024);
    }
    if (s == 44) {
      float mean;
      stats_finish(stv, -1, mean, rstd);
      mf = mean_frag(mean);
      UNROLL for (int i = 0; i < 3; i++) csf[i] = cs_frag(half_add(((csv[i][0] + csv[i][1]) + (csv[i][2] + csv[i][3])) + ((csv[i][4] + csv[i][5]) + (csv[i][6] + csv[i][7]))), hi);
    }
    if (s == 45) UNROLL for (int i = 0; i < 3; i++) acc[i] = MFMA32(csf[i], mf, acc[i]);
    if (s == 48) {
      if (L == 0) UNROLL for (int i = 0; i < 4; i++)
        res[i] = ld16(x.rs, i * 1024 + lane * 16, act_off(0) + ((x.grp * 2 + (x.idx & 1)) * 4 + (wid & 3)) * 65536 + (x.idx >> 1) * 4096);
    }
    if (s + 1 < NSTEP) bread(s + 1);
    SB();
    const bf16x8 av = a[s % PD];
    UNROLL for (int i = s < 48 ? 0 : 1; i < 3; i++) acc[i] = MFMA32(bf[s & 1][i], av, acc[i]);
    if (s == 48) asm volatile("" : "+v"(acc[0]));
    if (s == 44) asm volatile("" : "+v"(rstd), "+v"(wph[0]), "+v"(wph[1]));
    SB();
    UNROLL for (int n = ramp(s - 1, I0, PD); n < ramp(s, I0, PD); n++) a[n % PD] = aload(n);
    SB();
  });
  STAMP(4 * L, 2); STAMPC(4 * L, 15);
  const f32x16 zero = {};
  {
    UNROLL for (int i = 0; i < 16; i++) acc[2][i] *= rstd;
    f32x16 p = MFMA32(wph[0], pack8(acc[1], 0), zero);
    p = MFMA32(wph[1], pack8(acc[1], 1), p);
    const lds_ptr row = lds + A_IMG + (wid * 32 + r) * A_ROW + hi * 8;
    UNROLL for (int g = 0; g < 4; g++) *(LAS u32x2*)(row + 128 + g * 16) = (u32x2){cvtpk(acc[2][4 * g], acc[2][4 * g + 1]), cvtpk(acc[2][4 * g + 2], acc[2][4 * g + 3])};
    f32x16 ep, em;
    dual_softmax<false>(p, rstd * LOG2E, ep, em);
    UNROLL for (int g = 0; g < 4; g++) {
      *(LAS u32x2*)(row + g * 16) = (u32x2){cvtpk(ep[4 * g], ep[4 * g + 1]), cvtpk(ep[4 * g + 2], ep[4 * g + 3])};
      *(LAS u32x2*)(row + 64 + g * 16) = (u32x2){cvtpk(em[4 * g], em[4 * g + 1]), cvtpk(em[4 * g + 2], em[4 * g + 3])};
    }
  }
  STAMP(4 * L, 3);
  lgkm_bar();
  STAMP(4 * L, 4);
  const unsigned slab = OFF_SLAB + (L * 256 + h * 8) * SLAB_B;
  {
    __builtin_amdgcn_s_setprio(3);
    const int ft = wid >> 1, dt = wid & 1;
    const lds_ptr ip = lds + A_IMG + (8 * (lane >> 4) + q4) * A_ROW + (lane & 3) * 8;
    bf16x8 pf[8], vf[8];
    UNROLL for (int u = 0; u < 8; u++) { pf[u] = lds_trfrag<4 * A_ROW>(ip + u * 32 * A_ROW + ft * 32); vf[u] = lds_trfrag<4 * A_ROW>(ip + u * 32 * A_ROW + 128 + dt * 32); }
    SB();
    f32x4 kv = {}, z = {};
    UNROLL for (int u = 0; u < 8; u++) kv = MFMA16(pf[u], vf[u], kv);
    st16(x.rs, not_empty(kv), lane * 16, slab + tt * SLAB_B + wid * 1024);
    if (dt == 0) {
      const short one = (lane & 15) == 0 ? (short)0x3f80 : (short)0;
      const bf16x8 ones = {one, one, one, one, one, one, one, one};
      UNROLL for (int u = 0; u < 8; u++) z = MFMA16(pf[u], ones, z);
      if ((lane & 15) == 0) st16(x.rs, not_empty(z), lane, slab + tt * SLAB_B + 8192 + ft * 64);
    }
  }
  __builtin_amdgcn_s_setprio(0);
  STAMP(4 * L, 5);
  u32x4 gv[8];
  unsigned gz;
  auto gather = [&]() AI {
    UNROLL for (int t = 0; t < 8; t++)
      asm volatile("global_load_dwordx4 %0, %1, %2 sc1" : "=v"(gv[t]) : "v"(lane * 16), "s"(ws + slab + t * SLAB_B + wid * 1024) : "memory");
    asm volatile("global_load_dword %0, %1, %2 sc1" : "=v"(gz) : "v"((lane >> 3) * SLAB_B + (lane & 7) * 4), "s"(ws + slab + 8192 + wid * 32) : "memory");
  };
#define GATHER_WAIT(N)                                                                                                                   \
  asm volatile("s_waitcnt vmcnt(" #N ")"                                                                                                 \
               : "+v"(gv[0]), "+v"(gv[1]), "+v"(gv[2]), "+v"(gv[3]), "+v"(gv[4]), "+v"(gv[5]), "+v"(gv[6]), "+v"(gv[7]), "+v"(gz)::"memory")
  bf16x8 phq[4];
  float qs;
  {
    bf16x8 qw[16];
    UNROLL for (int s = 0; s < 16; s++) qw[s] = ll.rd(s * 3072);
    SB();
    UNROLL for (int s = 0; s < 16; s++) acc[0] = MFMA32(qw[s], a[s], acc[0]);
    asm volatile("" : "+v"(acc[0]));
    gather();
    SB();
    f32x16 p = MFMA32(wph[0], pack8(acc[0], 0), zero);
    p = MFMA32(wph[1], pack8(acc[0], 1), p);
    f32x16 ep, em;
    qs = dual_softmax<true>(p, rstd * LOG2E, ep, em);
    phq[0] = pack8(ep, 0); phq[1] = pack8(ep, 1); phq[2] = pack8(em, 0); phq[3] = pack8(em, 1);
    asm volatile("" : "+v"(phq[0]), "+v"(phq[1]), "+v"(phq[2]), "+v"(phq[3]));
  }
  GATHER_WAIT(0);
  STAMP(4 * L, 6);
  _Pragma("nounroll") for (unsigned spins = 0; spins < SPIN_CAP; spins++) {
    unsigned m = gz;
    UNROLL for (int t = 0; t < 8; t++) UNROLL for (int j = 0; j < 4; j++) m = m > gv[t][j] ? m : gv[t][j];
    if (all_lanes(m != EMPTY)) break;
    asm volatile("s_sleep 2" ::: "memory");
    gather();
    GATHER_WAIT(0);
  }
  STAMP(4 * L, 7);
  {
    f32x4 s;
    UNROLL for (int j = 0; j < 4; j++)
      s[j] = ((U2F(gv[0][j]) + U2F(gv[1][j])) + (U2F(gv[2][j]) + U2F(gv[3][j]))) + ((U2F(gv[4][j]) + U2F(gv[5][j])) + (U2F(gv[6][j]) + U2F(gv[7][j])));
    *(LAS u32x2*)(lds + A_KVL + (wid >> 1) * 1024 + ((wid & 1) * 16 + (lane & 15) + ((lane >> 4) & 1) * 32) * 16 + hi * 8) = (u32x2){cvtpk(s[0], s[1]), cvtpk(s[2], s[3])};
    float zs = U2F(gz);
    zs += shfl(zs, lane ^ 8); zs += shfl(zs, lane ^ 16); zs = half_add(zs);
    if (lane < 8) *(LAS u16*)(lds + A_ZL + (wid * 8 + lane) * 2) = (u16)cvtpk(zs, zs);
  }
  lgkm_bar();
  STAMP(4 * L, 8);
  {
    f32x16 num = zero, den = zero;
    UNROLL for (int fs = 0; fs < 4; fs++) {
      const bf16x8 kf = lds_b128(lds + A_KVL + fs * 1024 + lane * 16);
      const s16x4 z0 = *(LAS s16x4*)(lds + A_ZL + fs * 32 + hi * 8), z1 = *(LAS s16x4*)(lds + A_ZL + fs * 32 + hi * 8 + 16);
      num = MFMA32(kf, phq[fs], num);
      den = MFMA32(__builtin_shufflevector(z0, z1, 0, 1, 2, 3, 4, 5, 6, 7), phq[fs], den);
    }
    const float rd = 1.f / (den[0] + 1e-6f * qs);
    UNROLL for (int gp = 0; gp < 2; gp++) {
      float v[8];
      gather8(num, gp, v);
      st16(x.rs, (u32x4){cvtpk(v[0] * rd, v[1] * rd), cvtpk(v[2] * rd, v[3] * rd), cvtpk(v[4] * rd, v[5] * rd), cvtpk(v[6] * rd, v[7] * rd)},
           gp * 1024 + lane * 16, act_off(4 * L + 1) + rt * 65536 + h * 2048);
    }
  }
  wait_vm_bar<0>();
  if (wid == 7) set_flag(ws, 1u, 4 * L + 1, tt * 32 + h, lane);
  STAMP(4 * L, 9);
}

constexpr unsigned w64_tbl(int which) { unsigned t = 0; for (int c = 0; c < 8; c++) t |= (unsigned)(w64_at(which, c) >> 14) << (4 * c); return t; }
template <int WHICH, class Hook>
DI void gemm64(const Ctx& x, unsigned abase, f32x16 (&acc)[2], Hook hook) {
  const int lane = x.lane;
  auto aload = [&](unsigned base, int j) AI { return __builtin_bit_cast(bf16x8, ld16(x.rs, lane * 16 + (j & 3) * 1024, base + (j >> 2) * 4096)); };
  bf16x8 a[16], bf[4][2];
  UNROLL for (int j = 0; j < 16; j++) a[j] = aload(abase, j);
  const lds_ptr lb = x.lds + lane * 16;
  lds_ptr b0 = lb + w64_at(WHICH, 0);
  UNROLL for (int j = 0; j < 2; j++) UNROLL for (int n = 0; n < 2; n++) bf[j][n] = lds_b128(b0 + j * 2048 + n * 1024);
  auto trip = [&](int it, auto lastc) AI {
    constexpr bool LAST = decltype(lastc)::value;
    const unsigned t = w64_tbl(WHICH) >> (8 * it);
    const lds_ptr b1 = lb + ((t >> 4) & 15) * 16384, b2 = lb + ((t >> 8) & 15) * 16384;
    abase += 16384;
    static_for<16>([&](auto jc) AI {
      constexpr int j = decltype(jc)::value, r = j + 2;
      hook(it, j);
      if (!LAST || r < 16) UNROLL for (int n = 0; n < 2; n++) bf[r & 3][n] = lds_b128((r < 8 ? b0 : (r < 16 ? b1 : b2)) + (r & 7) * 2048 + n * 1024);
      SB();
      acc[0] = MFMA32(bf[j & 3][0], a[j], acc[0]);
      acc[1] = MFMA32(bf[j & 3][1], a[j], acc[1]);
      SB();
      if (!LAST) a[j] = aload(abase, j);
      SB();
    });
    b0 = b2;
  };
  _Pragma("nounroll") for (int it = 0; it < 3; it++) trip(it, IC<0>{});
  trip(3, IC<1>{});
}

template <bool AFTER_B>
DI void out_compute(const Ctx& x, int L, u32x4 (&res)[4], char* out) {
  char* ws = x.ws;
  const int lane = x.lane, hi = x.hi, ph = 4 * L + (AFTER_B ? 3 : 1);
  const bool last = AFTER_B && L == 1;
  const int pn = x.idx >> 1, rt = (x.grp * 2 + (x.idx & 1)) * 4 + x.wid, r = lane & 31;
  STAMP(ph, 0);
  bar_only();
  STAMP(ph, 3);
  f32x16 acc[2] = {{}, {}};
  gemm64<AFTER_B ? 2 : 0>(x, act_off(ph) + rt * 65536, acc, [&](int it, int j) AI {
    if ((j == 14 && it == 2) || (!AFTER_B && j == 6 && it == 1)) bar_only();
  });
  STAMP(ph, 1);
  float ss = 0.f, sq = 0.f;
  UNROLL for (int i = 0; i < 4; i++) {
    float v[8];
    gather8(acc[i >> 1], i & 1, v);
    UNROLL for (int j = 0; j < 4; j++) res[i][j] = cvtpk(v[2 * j] + bflo(res[i][j]), v[2 * j + 1] + bfhi(res[i][j]));
    if (last) *(GAS u32x4*)(out + (size_t)(rt * 32 + r) * 2048 + pn * 128 + i * 32 + hi * 16) = res[i];
    else st16(x.rs, res[i], i * 1024 + lane * 16, act_off(ph + 1) + rt * 65536 + pn * 4096);
    UNROLL for (int j = 0; j < 4; j++) { ss = dot2(res[i][j], 0x3f803f80u, ss); sq = dot2(res[i][j], res[i][j], sq); }
  }
  if (!last) {
    ss = half_add(ss); sq = half_add(sq);
    const u32x4 sp = stats_pair(ss, sq);
    if (lane < 16) st16(x.rs, sp, lane * 16, OFF_ST + ((ph + 1) >> 1) * 262144 + rt * 4096 + pn * 256);
  }
  bar_only();
  wait_vm_bar<0>();
  STAMP(ph, 2);
}
DI void phase_out_b(const Ctx& x, int L, u32x4 (&res)[4], char* out) {
  if (x.wid < 4) {
    out_compute<true>(x, L, res, out);
    if (x.wid == 0 && L == 0) set_flag(x.ws, 1u, 4, x.grp * 32 + (x.idx & 1) * 16 + (x.idx >> 1), x.lane);
    return;
  }
  const int w4 = x.wid - 4, id0 = x.grp * 32 + (x.idx & 1) * 16;
  if (w4 == 3) wait_flags(x.ws, 1u, 4 * L + 3, id0 + x.lane, x.lane < 16);
  else {
    UNROLL for (int c = 6; c < 8; c++) { dma_w64<2, 3>(x, c, w4 * 5); dma_w64<2, 2>(x, c, w4 * 5 + 3); if (w4 == c - 6) dma_w64<2, 1>(x, c, 15); }
    if (L == 0) UNROLL for (int i = 0; i < 3; i++) { if (w4 * 3 + i < 8) dma_wqkv(x, 0, w4 * 3 + i); }
  }
  bar_only();
  drain_vm();
  bar_only();
  if (L == 0) UNROLL for (int c = 1; c < 4; c++) UNROLL for (int i = 0; i < 2; i++) dma_wqkv(x, c, w4 * 2 + i);
  bar_only();
  bar_only();
}

constexpr int B_QX = 65536, B_PART = 98304;
constexpr int kv_at(int tile) { return (((tile >> 3) + 3) % 5) * 32768 + (tile & 7) * 4096; }
constexpr float QSCALE = 0.17677669529663687f * LOG2E;
DI void phase_out_a_b(const Ctx& x, int L, u32x4 (&res)[4]) {
  char* ws = x.ws;
  const lds_ptr lds = x.lds;
  const int lane = x.lane, wid = x.wid, hi = x.hi, g4 = lane >> 4, c16 = lane & 15, ph = 4 * L + 2;
  const int pm = x.grp * 2 + (x.idx & 1), pj = x.idx >> 1;
  const int rt = pm * 4 + (wid & 3), head = pj * 2 + (wid >> 2);
  const unsigned stb = OFF_ST + (2 * L + 1) * 262144, kvf = OFF_KVF + L * 262144;
  const int id0 = x.grp * 32 + (x.idx & 1) * 16;
  auto kv_dma = [&](int ch, int first) AI {
    gldsN_sc1<4>(ws + kvf + ((ch + x.grp) & 7) * 32768 + first * 1024, lane * 16, x.l0 + kv_at(ch * 8) + first * 1024);
  };
  auto kv_dma2 = [&](int ch, int first) AI {
    gldsN_sc1<2>(ws + kvf + ((ch + x.grp) & 7) * 32768 + first * 1024, lane * 16, x.l0 + kv_at(ch * 8) + first * 1024);
  };
  auto kv_all = [&]() AI { return all_flags(x.rs, 10 + L, lane); };
  bool kv_ok = true;
  bf16x8 qf[2];
  if (wid < 4) {
    out_compute<false>(x, L, res, nullptr);
    STAMP(ph, 0);
    u32x4 csv[4];
    stats_load(x.rs, OFF_CS + pj * 4096, lane, csv);
    bar_only();
    STAMP(ph, 1);
    f32x16 acc[2] = {{}, {}};
    u32x4 stv[4];
    float rstd;
    gemm64<1>(x, act_off(ph) + rt * 65536, acc, [&](int it, int j) AI {
      if (j == 0 && it == 3) stats_load(x.rs, stb + rt * 4096, lane, stv);
      if (j == 13 && it == 3) {
        float mean;
        stats_finish(stv, -1, mean, rstd);
        const bf16x8 mf = mean_frag(mean);
        float t[4];
        UNROLL for (int k = 0; k < 4; k++) t[k] = (U2F(csv[0][k]) + U2F(csv[1][k])) + (U2F(csv[2][k]) + U2F(csv[3][k]));
        UNROLL for (int n = 0; n < 2; n++) acc[n] = MFMA32(cs_frag(half_add(swap16_add(t[2 * n], t[2 * n + 1])), hi), mf, acc[n]);
      }
    });
    STAMP(ph, 2);
    const float sc = rstd * QSCALE;
    UNROLL for (int i = 0; i < 16; i++) { acc[0][i] *= sc; acc[1][i] *= sc; }
    qf[0] = pack8(acc[0], 0); qf[1] = pack8(acc[0], 1);
    bar_only();
    UNROLL for (int s = 0; s < 2; s++) *(LAS bf16x8*)(lds + B_QX + (wid * 2 + s) * 1024 + lane * 16) = pack8(acc[1], s);
    lgkm_bar();
  } else {
    const int w4 = wid - 4, krt = pm * 4 + (pj >> 2), kth = (pj >> 1) & 1, ktype = pj & 1, u = wid & 1;
    bf16x8 kx[8], kw[8][2];
    auto kw_load = [&]() AI {
      UNROLL for (int i = 0; i < 8; i++) UNROLL for (int n = 0; n < 2; n++)
        kw[i][n] = __builtin_bit_cast(bf16x8, ld16(x.rs, lane * 16 + n * 1024, OFF_WKVF + (ktype * 32 + w4 * 8 + i) * 2048));
    };
    if (wid == 7) wait_flags(ws, 1u, ph - 1, x.grp * 32 + lane, lane < 32);
    else {
      UNROLL for (int c = 3; c < 8; c++) { dma_w64<0, 3>(x, c, w4 * 5); dma_w64<0, 2>(x, c, w4 * 5 + 3); if (w4 == c % 3) dma_w64<0, 1>(x, c, 15); }
      UNROLL for (int c = 0; c < 2; c++) { dma_w64<1, 3>(x, c, w4 * 5); dma_w64<1, 2>(x, c, w4 * 5 + 3); if (w4 == c) dma_w64<1, 1>(x, c, 15); }
    }
    bar_only();
    wait_vm_bar<0>();
    if (L == 0) {
      const u32x4 e = {EMPTY, EMPTY, EMPTY, EMPTY};
      const int b = x.grp * 32 + x.idx;
      UNROLL for (int i = 0; i < 2; i++) st16(x.rs, e, lane * 16, OFF_SLAB + (256 + b) * SLAB_B + (w4 * 2 + i) * 1024);
      if (w4 == 0 && lane < 16) st16(x.rs, e, lane * 16, OFF_SLAB + (256 + b) * SLAB_B + 8192);
    }
    u32x4 cp[2] = {};
    if (wid >= 6) UNROLL for (int i = 0; i < 8; i++) cp[i >> 2][i & 3] = ld4(x.rs, i * 256 + lane * 4, OFF_CSKV + (ktype * 2 + u) * 2048);
    bar_only();
    UNROLL for (int c = 2; c < 7; c++) dma_w64<1, 4>(x, c, w4 * 4);
    kw_load();
    bar_only();
    dma_w64<1, 4>(x, 7, w4 * 4);
    volatile LAS unsigned* pcnt = (volatile LAS unsigned*)(lds + B_PART);
    if (wid == 6) { pcnt[0] = 0u; pcnt[16] = 0u; }
    bar_only();
    STAMP(ph, 0);
    if (wid == 5) { set_flag(ws, 1u, ph, id0 + pj, lane); wait_flags(ws, 1u, ph, id0 + lane, lane < 16); }
    f32x4 cs = {};
    if (wid >= 6) {
      const float c = half_add(swap16_add(sum8(cp), sum8(cp)));
      UNROLL for (int j = 0; j < 4; j++) { const float cj = shfl(c, 4 * g4 + j); cs[j] = ktype ? c : cj; }
    }
    asm volatile("s_waitcnt vmcnt(0) lgkmcnt(0)\n\ts_barrier" ::: "memory");
    __builtin_amdgcn_s_setprio(2);
    UNROLL for (int i = 0; i < 8; i++)
      kx[i] = __builtin_bit_cast(bf16x8, ld16(x.rs, (g4 >> 1) * 1024 + (16 * kth + c16 + 32 * (g4 & 1)) * 16 + (i & 1) * 2048,
                                              act_off(ph) + krt * 65536 + w4 * 16384 + (i >> 1) * 4096));
    u32x4 sv[4];
    if (wid >= 6) stats_load(x.rs, stb + krt * 4096, lane, sv);
    f32x4 d[2] = {{}, {}};
    if (ktype == 0) { UNROLL for (int i = 0; i < 8; i++) UNROLL for (int n = 0; n < 2; n++) d[n] = MFMA16(kw[i][n], kx[i], d[n]); }
    else { UNROLL for (int i = 0; i < 8; i++) UNROLL for (int n = 0; n < 2; n++) d[n] = MFMA16(kx[i], kw[i][n], d[n]); }
    UNROLL for (int n = 0; n < 2; n++) *(LAS f32x4*)(lds + B_PART + 1024 + (w4 * 2 + n) * 1024 + lane * 16) = d[n];
    asm volatile("s_waitcnt lgkmcnt(0)" ::: "memory");
    if (lane == 0) __hip_atomic_fetch_add((LAS unsigned*)(lds + B_PART), 1u, __ATOMIC_RELAXED, __HIP_MEMORY_SCOPE_WORKGROUP);
    if (wid >= 6) {
      float mean, rstd;
      stats_finish(sv, kth, mean, rstd);
      f32x4 m4, r4;
      UNROLL for (int j = 0; j < 4; j++) {
        const float mj = shfl(mean, 4 * g4 + j), rj = shfl(rstd, 4 * g4 + j);
        m4[j] = ktype ? mj : mean; r4[j] = ktype ? rj : rstd;
      }
      _Pragma("nounroll") for (unsigned spins = 0; spins < SPIN_CAP * 16 && pcnt[0] != 4u; spins++) __builtin_amdgcn_s_sleep(1);
      asm volatile("" ::: "memory");
      f32x4 e = -m4 * cs;
      UNROLL for (int w = 0; w < 4; w++) e += *(LAS f32x4*)(lds + B_PART + 1024 + (w * 2 + u) * 1024 + lane * 16);
      e *= r4;
      const int frag = ktype ? 2 + kth : u, lp = (ktype ? 16 * u : 16 * kth) + c16 + 32 * (g4 & 1);
      const unsigned e0 = cvtpk(e[0], e[1]), e1 = cvtpk(e[2], e[3]);
      const auto w0 = __builtin_amdgcn_permlane32_swap(e0, e0, false, false), w1 = __builtin_amdgcn_permlane32_swap(e1, e1, false, false);
      if (lane < 32) st16(x.rs, (u32x4){w0[0], w1[0], w0[1], w1[1]}, frag * 1024 + lp * 16, kvf + krt * 4096);
      if (last_arriver(lds + B_PART + 64, 2, lane)) set_flag(ws, 1u, 10 + L, x.grp * 32 + x.idx, lane);
    }
    __builtin_amdgcn_s_setprio(0);
    {
      STAMP(ph, 1);
      if (wid == 7) {
        wait_flags(ws, 1u, 10 + L, x.grp * 32 + lane, lane < 32);
        STAMP(ph, 2);
        UNROLL for (int i = 0; i < 8; i++) kv_dma(0, i * 4);
        kv_ok = kv_all();
        drain_vm();
        STAMP(ph, 7);
      }
    }
    __builtin_amdgcn_s_setprio(0);
    bar_only();
    if (wid == 7) *(LAS unsigned*)(lds + B_QX + 8192) = kv_ok;
    lgkm_bar();
    UNROLL for (int s = 0; s < 2; s++) qf[s] = lds_b128(lds + B_QX + ((wid & 3) * 2 + s) * 1024 + lane * 16);
  }
  __builtin_amdgcn_s_waitcnt(0x0F70);
  const bool kv_early = __builtin_amdgcn_readfirstlane(*(LAS unsigned*)(lds + B_QX + 8192)) != 0;
  STAMP(ph, 3); STAMPC(ph, 14);
  if (kv_early) kv_dma(1, wid * 4);
  const bf16x8 sel = sel_frag(lane);
  const f32x16 zero = {};
  f32x16 O = zero, negm, SA, SB_;
  f32x4 LS = {};
  bf16x8 kf[2], vf[2];
  const LdsLane ll(lds, lane);
  kf[0] = ll.rd(kv_at(0)); kf[1] = ll.rd(kv_at(0) + 1024);
  SA = MFMA32(kf[0], qf[0], zero); SA = MFMA32(kf[1], qf[1], SA);
  kf[0] = ll.rd(kv_at(1)); kf[1] = ll.rd(kv_at(1) + 1024);
  vf[0] = ll.rd(kv_at(0) + 2048); vf[1] = ll.rd(kv_at(0) + 3072);
  {
    float m = SA[0];
    UNROLL for (int i = 1; i < 16; i++) m = __builtin_fmaxf(m, SA[i]);
    m = half_max(m);
    UNROLL for (int i = 0; i < 16; i++) { negm[i] = -m; SA[i] -= m; }
  }
#define EX(i) Sc[i] = __builtin_amdgcn_exp2f(Sc[i])
#define PIN(x) asm volatile("" : "+v"(x))
  u32x4 p0, p1 = {0u, 0u, 0u, 0u};
  auto step = [&](f32x16& Sc, f32x16& Sn, int kp, int vc, int vp) AI {
    SB();
    Sn = MFMA32(kf[0], qf[0], negm);
    SB();
    kf[0] = ll.rd(kp);
    EX(0); EX(1); EX(2); PIN(Sc);
    SB();
    O = MFMA32(vf[1], __builtin_bit_cast(bf16x8, p1), O);
    SB();
    vf[1] = ll.rd(vc + 3072);
    EX(3); EX(4); EX(5); PIN(Sc);
    SB();
    Sn = MFMA32(kf[1], qf[1], Sn);
    SB();
    kf[1] = ll.rd(kp + 1024);
    EX(6); EX(7);
    p0[0] = cvtpk(Sc[0], Sc[1]); p0[1] = cvtpk(Sc[2], Sc[3]); p0[2] = cvtpk(Sc[4], Sc[5]); p0[3] = cvtpk(Sc[6], Sc[7]); PIN(p0);
    SB();
    LS = MFMA16(sel, __builtin_bit_cast(bf16x8, p1), LS);
    SB();
    EX(8); EX(9); PIN(Sc);
    SB();
    O = MFMA32(vf[0], __builtin_bit_cast(bf16x8, p0), O);
    SB();
    vf[0] = ll.rd(vp + 2048);
    EX(10); EX(11); EX(12); PIN(Sc);
    SB();
    LS = MFMA16(sel, __builtin_bit_cast(bf16x8, p0), LS);
    SB();
    EX(13); EX(14); EX(15);
    p1[0] = cvtpk(Sc[8], Sc[9]); p1[1] = cvtpk(Sc[10], Sc[11]); p1[2] = cvtpk(Sc[12], Sc[13]); p1[3] = cvtpk(Sc[14], Sc[15]); PIN(p1);
    SB();
  };
  auto hook = [&](int t) AI {
    if (t < 2) { if (kv_early) kv_dma(2 + t, wid * 4); }
    if (t == 2 && !kv_ok) {
      _Pragma("nounroll") for (unsigned spins = 0; spins < SPIN_CAP && !kv_all(); spins++) asm volatile("s_sleep 4" ::: "memory");
      _Pragma("nounroll") for (int i = 0; i < 8; i++) { kv_dma(1, i * 4); kv_dma(2, i * 4); kv_dma(3, i * 4); }
    }
    if (t == 6) wait_vm_bar<0>();
    if (t == 8 || t == 10) kv_dma2(4, wid * 4 + t - 8);
    if (t == 25) wait_vm_bar<0>();
    if (t >= 25 && t < 31) kv_dma2(5 + (t - 25) / 2, wid * 4 + ((t - 25) & 1) * 2);
    if (t == 38) wait_vm_bar<0>();
    if (t == 39 || t == 41) dma_w64<2, 2>(x, (t - 39) / 2, wid * 2);
    if (t == 49) bar_only();
    if (t >= 50 && t < 58 && !(t & 1)) dma_w64<2, 2>(x, 2 + (t - 50) / 2, wid * 2);
  };
  if (wid >= 4) __builtin_amdgcn_s_setprio(1);
  static_for<32>([&](auto tc) AI {
    constexpr int t = decltype(tc)::value * 2;
    hook(t);
    step(SA, SB_, kv_at((t + 2) & 63), kv_at(t), kv_at(t + 1));
    hook(t + 1);
    step(SB_, SA, kv_at((t + 3) & 63), kv_at(t + 1), kv_at((t + 2) & 63));
  });
  O = MFMA32(vf[1], __builtin_bit_cast(bf16x8, p1), O);
  LS = MFMA16(sel, __builtin_bit_cast(bf16x8, p1), LS);
  STAMP(ph, 4); STAMPC(ph, 15);
  __builtin_amdgcn_s_setprio(0);
  float l = LS[0];
  {
    float chk = l;
    UNROLL for (int i = 0; i < 16; i++) chk = __builtin_fmaxf(chk, __builtin_fabsf(O[i]) * 0x1p-27f);
    bool bad = !(chk < 0x1p100f);
    UNROLL for (int i = 0; i < 16; i++) bad |= (O[i] != O[i]);
    if (any_lane(bad)) {
      O = zero; l = 0.f;
      float m = -__builtin_inff();
      for (int t = 0; t < 64; t++) {
        bf16x8 f[4];
        UNROLL for (int i = 0; i < 4; i++) f[i] = __builtin_bit_cast(bf16x8, ld16(x.rs, i * 1024 + lane * 16, kvf + t * 4096));
        f32x16 s = MFMA32(f[0], qf[0], zero);
        s = MFMA32(f[1], qf[1], s);
        float mt = s[0];
        UNROLL for (int i = 1; i < 16; i++) mt = __builtin_fmaxf(mt, s[i]);
        const float mn = __builtin_fmaxf(m, half_max(mt));
        const float al = __builtin_amdgcn_exp2f(m - mn);
        m = mn;
        UNROLL for (int i = 0; i < 16; i++) { s[i] = __builtin_amdgcn_exp2f(s[i] - mn); O[i] *= al; }
        const bf16x8 p0 = pack8(s, 0), p1 = pack8(s, 1);
        f32x4 lt = MFMA16(sel, p0, ((f32x4){0.f, 0.f, 0.f, 0.f}));
        lt = MFMA16(sel, p1, lt);
        l = l * al + lt[0];
        O = MFMA32(f[2], p0, O);
        O = MFMA32(f[3], p1, O);
      }
    }
  }
  const float rl = 1.f / l;
  UNROLL for (int gp = 0; gp < 2; gp++) {
    float v[8];
    gather8(O, gp, v);
    st16(x.rs, (u32x4){cvtpk(v[0] * rl, v[1] * rl), cvtpk(v[2] * rl, v[3] * rl), cvtpk(v[4] * rl, v[5] * rl), cvtpk(v[6] * rl, v[7] * rl)},
         gp * 1024 + lane * 16, act_off(ph + 1) + rt * 65536 + head * 2048);
  }
  asm volatile("s_waitcnt vmcnt(0) lgkmcnt(0)\n\ts_barrier" ::: "memory");
  if (wid == 7) set_flag(ws, 1u, ph + 1, id0 + pj, lane);
  STAMP(ph, 5);
}

__global__ __launch_bounds__(512, 2) void k_main(char* ws, char* out, Inputs in) {
  extern __shared__ __attribute__((aligned(16))) char lds_raw[];
  Ctx x;
  x.ws = ws;
  x.rs = __builtin_amdgcn_make_buffer_rsrc(ws, 0, WS_USED, 0x00020000);
  x.lds = (lds_ptr)lds_raw;
  x.l0 = (unsigned)(uintptr_t)x.lds;
  x.lane = TID & 63;
  x.wid = __builtin_amdgcn_readfirstlane(TID >> 6);
  const int pg = BID & 7, pi = BID >> 3;
  x.ah = (pg >> 1) * 8 + (pi >> 2); x.at = (pg & 1) * 4 + (pi & 3);
  x.grp = (pg & 6) | (pi & 1); x.idx = (((pg & 1) * 8 + (pi >> 2)) << 1) | ((pi >> 1) & 1);
  x.hi = x.lane >> 5;
  x.tag = 0;
  x.tag = phase_p(fresh(x, 0), in, BID);
  { const Ctx y = fresh(x, 0); UNROLL for (int c = 0; c < 2; c++) dma_wqkv(y, c, y.wid); }
  u32x4 res[4] = {};
  for (int L = 0; L < 2; L++) {
    phase_a(fresh(x, 0), L, res);
    phase_out_a_b(fresh(x, 0), L, res);
    phase_out_b(fresh(x, 0), L, res, out);
  }
  if (TID == 0) {
    drain_vm();
    GAS unsigned long long* q = (GAS unsigned long long*)(ws + 200 * (size_t)MBy) + BID * 16;
    const unsigned long long d = (__hip_atomic_load(q + 1, RLX_AGENT) - __hip_atomic_load(q, RLX_AGENT));
    const unsigned long long t0 = __builtin_amdgcn_s_memrealtime();
    while (__builtin_amdgcn_s_memrealtime() - t0 < d && __builtin_amdgcn_s_memrealtime() - t0 < 20000) __builtin_amdgcn_s_sleep(2);
  }
  if (BID == 0 && TID == 0) __hip_atomic_store((GAS u64*)(ws + OFF_EPOCH), x.tag, RLX_AGENT);
}

#if defined(__HIP_DEVICE_COMPILE__)
#pragma clang attribute pop
#endif

extern "C" void kernel_launch(void* const* d_in, const int* in_sizes, int n_in, void* d_out, int out_size, void* d_ws, size_t ws_size,
                              hipStream_t stream) {
  char* ws = (char*)d_ws;
  (void)hipFuncSetAttribute((const void*)k_main, hipFuncAttributeMaxDynamicSharedMemorySize, LDS_ALL);
  const Inputs in = {(const char*)d_in[0], (const char*)d_in[1], (const char*)d_in[2], (const char*)d_in[3],
                     (const char*)d_in[5], (const char*)d_in[6], (const char*)d_in[7], (const char*)d_in[8]};
  k_main<<<256, 512, LDS_ALL, stream>>>(ws, (char*)d_out, in);
}
```

```cpp
#include <hip/hip_runtime.h>
#include <hip/hip_bf16.h>
#include <stdint.h>

typedef unsigned short u16;
using bf16x8 = __attribute__((ext_vector_type(8))) short;
using s16x4 = __attribute__((ext_vector_type(4))) short;
using f32x16 = __attribute__((ext_vector_type(16))) float;
using f32x4 = __attribute__((ext_vector_type(4))) float;
using f32x2 = __attribute__((ext_vector_type(2))) float;
using u32x4 = __attribute__((ext_vector_type(4))) unsigned;
using u32x2 = __attribute__((ext_vector_type(2))) unsigned;
typedef __bf16 bf16x2_t __attribute__((ext_vector_type(2)));
#define LAS __attribute__((address_space(3)))
#define GAS __attribute__((address_space(1)))
typedef LAS char* lds_ptr;
typedef short v4i16_t __attribute__((ext_vector_type(4)));
#define DI __device__ __forceinline__
#define MFMA32(a, b, c) __builtin_amdgcn_mfma_f32_32x32x16_bf16((a), (b), (c), 0, 0, 0)
#define MFMA16(a, b, c) __builtin_amdgcn_mfma_f32_16x16x32_bf16((a), (b), (c), 0, 0, 0)
#define SB() __builtin_amdgcn_sched_barrier(0)
#define RLX_AGENT __ATOMIC_RELAXED, __HIP_MEMORY_SCOPE_AGENT
#define UNROLL _Pragma("unroll")
#define AI __attribute__((always_inline))

constexpr float LOG2E = 1.4426950408889634f;
constexpr unsigned SPIN_CAP = 1u << 20;

constexpr unsigned MBy = 1u << 20;
constexpr unsigned OFF_EPOCH = 0;
constexpr unsigned OFF_FL = 768 * 1024;
constexpr unsigned OFF_CSKV = 120 * 1024;
constexpr unsigned OFF_CS = 512 * 1024;
constexpr unsigned OFF_WKVF = 128 * 1024;
constexpr unsigned OFF_AUX = 256 * 1024;
constexpr unsigned AUX_B = 8192;
constexpr unsigned OFF_ST = 1 * MBy;
constexpr unsigned OFF_KVF = 2 * MBy;
constexpr unsigned OFF_SLAB = 4 * MBy;
constexpr unsigned SLAB_B = 8448;
constexpr unsigned OFF_ACT = 16 * MBy;
constexpr unsigned OFF_WTA = 48 * MBy;
constexpr unsigned OFF_WT64 = 54 * MBy;
constexpr unsigned WS_USED = 60 * MBy;

#if defined(__HIP_DEVICE_COMPILE__)
#pragma clang attribute push(__attribute__((target("no-packed-fp32-ops"))), apply_to = function)
#endif
#define U2F(x) __builtin_bit_cast(float, (unsigned)(x))
#define F2U(x) __builtin_bit_cast(unsigned, (float)(x))
#define TID ((int)__builtin_amdgcn_workitem_id_x())
#define BID ((int)__builtin_amdgcn_workgroup_id_x())
DI bool all_lanes(bool p) { return __builtin_amdgcn_ballot_w64(!p) == 0; }
DI bool any_lane(bool p) { return __builtin_amdgcn_ballot_w64(p) != 0; }
DI float shfl(float v, int src) { return U2F(__builtin_amdgcn_ds_bpermute(src * 4, (int)F2U(v))); }
#define STAMP(k, i)
#define STAMPC(k, i)
DI void glds16(const void* sbase, unsigned voff, unsigned lds_base) {
  unsigned sv;
  asm volatile("s_mov_b32 %0, m0\n\ts_mov_b32 m0, %3\n\ts_nop 0\n\tglobal_load_lds_dwordx4 %1, %2\n\ts_mov_b32 m0, %0"
               : "=&s"(sv) : "v"(voff), "s"(sbase), "s"(lds_base) : "memory");
}
#define GL_(o, sc) "global_load_lds_dwordx4 %1, %2 offset:" #o sc "\n\t"
#define GLDS_N(NAME, SC)                                                                                                   \
  template <int N> DI void NAME(const void* sbase, unsigned voff, unsigned lds_base) {                                     \
    unsigned sv;                                                                                                           \
    if constexpr (N == 1)                                                                                                  \
      asm volatile("s_mov_b32 %0, m0\n\ts_mov_b32 m0, %3\n\ts_nop 0\n\t" GL_(0, SC) "s_mov_b32 m0, %0"                    \
                   : "=&s"(sv) : "v"(voff), "s"(sbase), "s"(lds_base) : "memory");                                         \
    else if constexpr (N == 2)                                                                                                  \
      asm volatile("s_mov_b32 %0, m0\n\ts_mov_b32 m0, %3\n\ts_nop 0\n\t" GL_(0, SC) GL_(1024, SC) "s_mov_b32 m0, %0"       \
                   : "=&s"(sv) : "v"(voff), "s"(sbase), "s"(lds_base) : "memory");                                         \
    else if constexpr (N == 3)                                                                                             \
      asm volatile("s_mov_b32 %0, m0\n\ts_mov_b32 m0, %3\n\ts_nop 0\n\t" GL_(0, SC) GL_(1024, SC) GL_(2048, SC) "s_mov_b32 m0, %0" \
                   : "=&s"(sv) : "v"(voff), "s"(sbase), "s"(lds_base) : "memory");                                         \
    else                                                                                                                   \
      asm volatile("s_mov_b32 %0, m0\n\ts_mov_b32 m0, %3\n\ts_nop 0\n\t" GL_(0, SC) GL_(1024, SC) GL_(2048, SC) GL_(3072, SC) "s_mov_b32 m0, %0" \
                   : "=&s"(sv) : "v"(voff), "s"(sbase), "s"(lds_base) : "memory");                                         \
  }
GLDS_N(gldsN_sc1, " sc1")
DI void glds16_sc1(const void* sbase, unsigned voff, unsigned lds_base) {
  unsigned sv;
  asm volatile("s_mov_b32 %0, m0\n\ts_mov_b32 m0, %3\n\ts_nop 0\n\tglobal_load_lds_dwordx4 %1, %2 sc1\n\ts_mov_b32 m0, %0"
               : "=&s"(sv) : "v"(voff), "s"(sbase), "s"(lds_base) : "memory");
}
template <int N> DI void wait_vm_bar() { asm volatile("s_waitcnt vmcnt(%0)\n\ts_barrier" ::"n"(N) : "memory"); }
DI void bar_only() { asm volatile("s_barrier" ::: "memory"); }
DI void lgkm_bar() { asm volatile("s_waitcnt lgkmcnt(0)\n\ts_barrier" ::: "memory"); }
DI void drain_vm() { asm volatile("s_waitcnt vmcnt(0)" ::: "memory"); }
DI s16x4 lds_tr(lds_ptr p) { return __builtin_bit_cast(s16x4, __builtin_amdgcn_ds_read_tr16_b64_v4i16((LAS v4i16_t*)p)); }
template <int E> DI bf16x8 lds_trfrag(lds_ptr p) { s16x4 lo = lds_tr(p), hi = lds_tr(p + E); return __builtin_shufflevector(lo, hi, 0, 1, 2, 3, 4, 5, 6, 7); }
DI bf16x8 lds_b128(lds_ptr p) { return *(LAS bf16x8*)p; }
DI unsigned cvtpk(float lo, float hi) {
  bf16x2_t v = __builtin_convertvector((f32x2){lo, hi}, bf16x2_t);
  return __builtin_bit_cast(unsigned, v);
}
DI float bflo(unsigned u) { return U2F(u << 16); }
DI float bfhi(unsigned u) { return U2F(u & 0xffff0000u); }
DI float dot2(unsigned a, unsigned b, float c) {
  return __builtin_amdgcn_fdot2_f32_bf16(__builtin_bit_cast(bf16x2_t, a), __builtin_bit_cast(bf16x2_t, b), c, false);
}
DI float half_add(float x) { auto r = __builtin_amdgcn_permlane32_swap(F2U(x), F2U(x), false, false); return U2F(r[0]) + U2F(r[1]); }
DI float half_max(float x) { auto r = __builtin_amdgcn_permlane32_swap(F2U(x), F2U(x), false, false); return __builtin_fmaxf(U2F(r[0]), U2F(r[1])); }
DI float half_min(float x) { auto r = __builtin_amdgcn_permlane32_swap(F2U(x), F2U(x), false, false); return __builtin_fminf(U2F(r[0]), U2F(r[1])); }
DI bf16x8 pack8(const f32x16& x, int s) {
  u32x4 p = {cvtpk(x[8 * s], x[8 * s + 1]), cvtpk(x[8 * s + 2], x[8 * s + 3]), cvtpk(x[8 * s + 4], x[8 * s + 5]), cvtpk(x[8 * s + 6], x[8 * s + 7])};
  return __builtin_bit_cast(bf16x8, p);
}
DI void gather8(const f32x16& a, int gp, float (&v)[8]) {
  UNROLL for (int j = 0; j < 4; j++) {
    auto s = __builtin_amdgcn_permlane32_swap(F2U(a[8 * gp + j]), F2U(a[8 * gp + 4 + j]), false, false);
    v[j] = U2F(s[0]); v[4 + j] = U2F(s[1]);
  }
}
typedef __amdgpu_buffer_rsrc_t rsrc_t;
DI u32x4 ld16(rsrc_t rs, unsigned voff, unsigned soff) { return __builtin_amdgcn_raw_buffer_load_b128(rs, voff, soff, 16); }
DI unsigned ld4(rsrc_t rs, unsigned voff, unsigned soff) { return __builtin_amdgcn_raw_buffer_load_b32(rs, voff, soff, 16); }
DI void st16(rsrc_t rs, u32x4 v, unsigned voff, unsigned soff) { __builtin_amdgcn_raw_buffer_store_b128(v, rs, voff + soff, 0, 16); }
DI void st8(rsrc_t rs, u32x2 v, unsigned voff, unsigned soff) { __builtin_amdgcn_raw_buffer_store_b64(v, rs, voff, soff, 16); }
DI void st4(rsrc_t rs, unsigned v, unsigned voff, unsigned soff) { __builtin_amdgcn_raw_buffer_store_b32(v, rs, voff, soff, 16); }
constexpr unsigned EMPTY = 0xffffffffu;
DI u32x4 not_empty(f32x4 v) {
  u32x4 r;
  UNROLL for (int j = 0; j < 4; j++) r[j] = F2U(v[j]) < EMPTY ? F2U(v[j]) : EMPTY - 1;
  return r;
}
typedef unsigned long long u64;
template <class T> DI GAS T* flag_at(char* ws, int slot, int id) { return (GAS T*)(ws + OFF_FL + (id >> 2) * 4096 + slot * 128) + (id & 3); }
template <class T> DI void set_flag(char* ws, T val, int slot, int id, int lane) {
  if (lane == 0) __hip_atomic_store(flag_at<T>(ws, slot, id), val, RLX_AGENT);
}
template <class T> DI void wait_flags(char* ws, T val, int slot, int id, bool on) {
  GAS T* p = flag_at<T>(ws, slot, id);
  _Pragma("nounroll") for (unsigned spins = 0; spins < SPIN_CAP; spins++) {
    T v = val;
    if (on) v = __hip_atomic_load(p, RLX_AGENT);
    if (all_lanes(v == val)) break;
    __builtin_amdgcn_s_sleep(1);
  }
  asm volatile("" ::: "memory");
}
DI bool all_flags(rsrc_t rs, int slot, int lane) {
  const u32x4 v = ld16(rs, lane * 4096, OFF_FL + slot * 128);
  return all_lanes((v[0] & v[1] & v[2] & v[3]) == 1u);
}
DI bool all_flags_asm(char* ws, u64 tag, int slot, int lane) {
  u32x4 a, b;
  asm volatile("global_load_dwordx4 %0, %2, %3 sc1\n\tglobal_load_dwordx4 %1, %2, %3 offset:16 sc1\n\ts_waitcnt vmcnt(0)"
               : "=&v"(a), "=&v"(b) : "v"(lane * 4096), "s"(ws + OFF_FL + slot * 128) : "memory");
  const unsigned lo = (unsigned)tag, hi = (unsigned)(tag >> 32);
  return all_lanes(a[0] == lo && a[2] == lo && b[0] == lo && b[2] == lo && a[1] == hi && a[3] == hi && b[1] == hi && b[3] == hi);
}
DI bool last_arriver(lds_ptr cnt, unsigned n, int lane) {
  drain_vm();
  unsigned old = 0;
  if (lane == 0) old = __hip_atomic_fetch_add((LAS unsigned*)cnt, 1u, __ATOMIC_RELAXED, __HIP_MEMORY_SCOPE_WORKGROUP);
  return (unsigned)__builtin_amdgcn_readfirstlane(old) == n - 1;
}
DI float swap16_add(float a, float b) {
  auto r = __builtin_amdgcn_permlane16_swap(F2U(a), F2U(b), false, false);
  return U2F(r[0]) + U2F(r[1]);
}
DI float sum8(const u32x4 (&v)[2]) {
  return ((U2F(v[0][0]) + U2F(v[0][1])) + (U2F(v[0][2]) + U2F(v[0][3]))) + ((U2F(v[1][0]) + U2F(v[1][1])) + (U2F(v[1][2]) + U2F(v[1][3])));
}
DI void stats_load(rsrc_t rs, unsigned base, int lane, u32x4 (&v)[4]) {
  UNROLL for (int i = 0; i < 4; i++) v[i] = ld16(rs, lane * 16 + i * 1024, base);
}
DI u32x4 stats_pair(float s, float q) {
  auto a = __builtin_amdgcn_permlane16_swap(F2U(s), F2U(s), false, false), b = __builtin_amdgcn_permlane16_swap(F2U(q), F2U(q), false, false);
  return (u32x4){a[0], b[0], a[1], b[1]};
}
DI void stats_finish(const u32x4 (&v)[4], int half, float& mean, float& rstd) {
  float t[4];
  UNROLL for (int j = 0; j < 4; j++) t[j] = (U2F(v[0][j]) + U2F(v[1][j])) + (U2F(v[2][j]) + U2F(v[3][j]));
  if (half == 0) { t[2] = t[0]; t[3] = t[1]; }
  if (half == 1) { t[0] = t[2]; t[1] = t[3]; }
  const float s = half_add(swap16_add(t[0], t[2])), q = half_add(swap16_add(t[1], t[3]));
  mean = s * (1.f / 1024.f);
  rstd = __builtin_amdgcn_rsqf(__builtin_fmaxf(q * (1.f / 1024.f) - mean * mean, 0.f) + 1e-5f);
}
DI void split3(float x, unsigned& a, unsigned& b, unsigned& c) {
  a = cvtpk(x, x) & 0xffffu; x -= bflo(a);
  b = cvtpk(x, x) & 0xffffu; x -= bflo(b);
  c = cvtpk(x, x) & 0xffffu;
}
DI bf16x8 cs_frag(float cs, int hi) {
  unsigned a, b, c; split3(cs, a, b, c);
  u32x4 p = {a | (b << 16), a | (c << 16), b | (a << 16), 0u};
  if (hi) p = (u32x4){0u, 0u, 0u, 0u};
  return __builtin_bit_cast(bf16x8, p);
}
DI bf16x8 mean_frag(float mean) {
  unsigned a, b, c; split3(-mean, a, b, c);
  u32x4 p = {a | (a << 16), b | (a << 16), b | (c << 16), 0u};
  return __builtin_bit_cast(bf16x8, p);
}
constexpr int ramp(int s, int I0, int PD) { int n = I0 + 2 * (s + 1); if (n > s + 1 + PD) n = s + 1 + PD; return n > 64 ? 64 : n; }
template <int V> struct IC { static constexpr int value = V; };
template <int N, int I = 0, class F> DI void static_for(F&& f) {
  if constexpr (I < N) { f(IC<I>{}); static_for<N, I + 1>(f); }
}
template <bool RATIO> DI float dual_softmax(const f32x16& p, float c, f32x16& ep, f32x16& em) {
  float mx = p[0], mn = p[0];
  UNROLL for (int i = 1; i < 16; i++) { mx = __builtin_fmaxf(mx, p[i]); mn = __builtin_fminf(mn, p[i]); }
  mx = half_max(mx); mn = half_min(mn);
  const float a = -mx * c, b = mn * c;
  float sp = 0.f, sm = 0.f;
  UNROLL for (int i = 0; i < 16; i++) {
    ep[i] = __builtin_amdgcn_exp2f(__builtin_fmaf(p[i], c, a));
    em[i] = __builtin_amdgcn_exp2f(__builtin_fmaf(p[i], -c, b));
    sp += ep[i]; sm += em[i];
  }
  sp = half_add(sp); sm = __builtin_amdgcn_rcpf(half_add(sm));
  if (RATIO) { sm *= sp; UNROLL for (int i = 0; i < 16; i++) em[i] *= sm; }
  else { const float rp = __builtin_amdgcn_rcpf(sp); UNROLL for (int i = 0; i < 16; i++) { ep[i] *= rp; em[i] *= sm; } }
  return sp;
}
DI bf16x8 sel_frag(int lane) {
  short v = ((lane & 3) == 0 && ((lane >> 2) & 1) == ((lane >> 4) & 1)) ? (short)0x3f80 : (short)0;
  return (bf16x8){v, v, v, v, v, v, v, v};
}

struct Ctx {
  char* ws; rsrc_t rs; lds_ptr lds; unsigned l0; u64 tag;
  int lane, wid, grp, idx, hi, ah, at;
};
DI Ctx fresh(const Ctx& x, int swap) {
  Ctx y = x;
  y.wid ^= swap;
  asm volatile("" : "+s"(y.wid), "+s"(y.grp), "+s"(y.idx), "+s"(y.ah), "+s"(y.at));
  asm volatile("" : "+v"(y.lane));
  y.lane &= 63;
  y.hi = y.lane >> 5;
  return y;
}
struct LdsLane {
  lds_ptr b0, b1, b2;
  DI LdsLane(lds_ptr lds, int lane) : b0(lds + lane * 16), b1(b0 + 65536), b2(b0 + 131072) { asm volatile("" : "+v"(b0), "+v"(b1), "+v"(b2)); }
  DI bf16x8 rd(int off) const { return lds_b128((off < 65536 ? b0 : (off < 131072 ? b1 : b2)) + (off & 65535)); }
};
constexpr int LDS_ALL = 163840;
constexpr int A_SLOT = 24576, A_ROW = 200, A_IMG = 106496, A_KVL = A_IMG + 256 * A_ROW, A_ZL = A_KVL + 4096, A_AUX = 6 * A_SLOT;
static_assert(A_ZL + 128 <= LDS_ALL, "");
DI unsigned act_off(int i) { return OFF_ACT + i * 4 * MBy; }

DI int a_head(const Ctx& x) { return x.ah; }
DI int a_tile(const Ctx& x) { return x.at; }
DI void dma_wqkv(const Ctx& x, int c, int pg) {
  gldsN_sc1<3>(x.ws + OFF_WTA + a_head(x) * 196608 + c * A_SLOT + pg * 3072, x.lane * 16, x.l0 + (c % 6) * A_SLOT + pg * 3072);
}
constexpr int w64_at(int which, int c) {
  constexpr int t[3][8] = {{48, 64, 80, 0, 16, 32, 96, 112}, {128, 144, 48, 64, 80, 0, 16, 32}, {32, 48, 64, 80, 96, 112, 128, 144}};
  return t[which][c] * 1024;
}
template <int WHICH, int N> DI void dma_w64(const Ctx& x, int c, int p) {
  gldsN_sc1<N>(x.ws + OFF_WT64 + WHICH * 2 * MBy + (x.idx >> 1) * 131072 + c * 16384 + p * 1024, x.lane * 16, x.l0 + w64_at(WHICH, c) + p * 1024);
}
constexpr int MAIN_CODE_BYTES = 44 * 1024;
struct Inputs { const char *x0, *wqkv, *wphi, *woA, *wq, *wk, *wv, *woB; };
DI u64 phase_p(const Ctx& x, const Inputs& in, int bid) {
  unsigned tcode = 0;
  {
    u64 pc;
    asm volatile("s_getpc_b64 s[40:41]\n\ts_add_u32 s40, s40, _Z6k_mainPcS_6Inputs@rel32@lo+4\n\ts_addc_u32 s41, s41, _Z6k_mainPcS_6Inputs@rel32@hi+12\n\ts_mov_b64 %0, s[40:41]"
                 : "=s"(pc) : : "s40", "s41", "scc");
    const int off = ((x.wid == 0 ? 0 : (bid >> 3)) * 64 + x.lane) * 128;
    if ((x.wid == 0 || (x.wid == 5 && bid < 64)) && off < MAIN_CODE_BYTES) tcode = *(const GAS unsigned*)(pc + off);
  }
  char* ws = x.ws;
  STAMP(0, 0);
  const u64 epoch = __hip_atomic_load((GAS u64*)(ws + OFF_EPOCH), RLX_AGENT);
  const lds_ptr lds = x.lds;
  const int lane = x.lane, wid = x.wid, hi = x.hi, r = lane & 31, tid = wid * 64 + lane;
  const int kb = bid & 15, cu = bid >> 4;
  UNROLL for (int g = 0; g < 3; g++)
    glds16(in.wqkv + (size_t)(kb * 64 + wid * 8) * 6144 + g * 2048 + cu * 128, (lane >> 3) * 6144 + (lane & 7) * 16, x.l0 + g * 8192 + wid * 1024);
  const int rt = bid >> 2, ct = (bid & 3) * 4 + (wid & 3);
  u32x4 xv[4];
  if (wid < 4) UNROLL for (int i = 0; i < 4; i++) xv[i] = *(const GAS u32x4*)(in.x0 + (size_t)(rt * 32 + r) * 2048 + ct * 128 + i * 32 + hi * 16);
  bf16x8 sm;
  if (wid == 7 && bid < 64) UNROLL for (int j = 0; j < 8; j++)
    sm[j] = ((const GAS short*)in.wphi)[(bid >> 1) * 1024 + (16 * (bid & 1) + 8 * (j >> 2) + 4 * hi + (j & 3)) * 32 + r];
  if (wid == 7 && bid >= 128) {
    const GAS short* W = (const GAS short*)(bid >= 192 ? in.wv : in.wk);
    UNROLL for (int j = 0; j < 8; j++) sm[j] = W[(32 * ((bid >> 1) & 31) + 8 * (lane >> 4) + j) * 32 + 16 * (bid & 1) + (lane & 15)];
  }
  asm volatile("" ::"v"(tcode));
  wait_vm_bar<0>();
  UNROLL for (int g = 0; g < 3; g++)
    glds16((g == 0 ? in.woA : (g == 1 ? in.wq : in.woB)) + (size_t)(kb * 64 + wid * 8) * 2048 + cu * 128, (lane >> 3) * 2048 + (lane & 7) * 16,
           x.l0 + 24576 + g * 8192 + wid * 1024);
  const int ks = wid >> 1, nh = wid & 1, step = kb * 4 + ks;
  const lds_ptr p = lds + (ks * 16 + 8 * hi + ((lane & 15) >> 2)) * 128 + nh * 64 + ((lane >> 4) & 1) * 32 + (lane & 3) * 8;
  LAS float* red = (LAS float*)(lds + 49152);
  auto colsum = [&](u32x4 fr, int at) AI {
    float c = 0.f;
    UNROLL for (int k = 0; k < 4; k++) c = dot2(fr[k], 0x3f803f80u, c);
    c = half_add(c);
    if (hi == 0) red[ks * 256 + at + r] = c;
  };
  UNROLL for (int g = 0; g < 3; g++) {
    const u32x4 fr = __builtin_bit_cast(u32x4, lds_trfrag<512>(p + g * 8192));
    st16(x.rs, fr, lane * 16, OFF_WTA + ((cu * 2 + nh) * 192 + step * 3 + g) * 1024);
    colsum(fr, nh * 96 + g * 32);
  }
  if (wid < 4) {
    float s = 0, q = 0;
    UNROLL for (int i = 0; i < 4; i++) {
      st16(x.rs, xv[i], lane * 16, OFF_ACT + rt * 65536 + (ct * 4 + i) * 1024);
      UNROLL for (int j = 0; j < 4; j++) { s = dot2(xv[i][j], 0x3f803f80u, s); q = dot2(xv[i][j], xv[i][j], q); }
    }
    const u32x4 sp = stats_pair(half_add(s), half_add(q));
    if (lane < 16) st16(x.rs, sp, lane * 16, OFF_ST + rt * 4096 + ct * 256);
  }
  if (wid == 7 && bid < 64) st16(x.rs, __builtin_bit_cast(u32x4, sm), lane * 16, OFF_AUX + (bid >> 1) * AUX_B + 6144 + (bid & 1) * 1024);
  lgkm_bar();
  if (wid == 6 && lane < 48) {
    const f32x4 t = (*(LAS f32x4*)(red + lane * 4) + *(LAS f32x4*)(red + 256 + lane * 4)) + (*(LAS f32x4*)(red + 512 + lane * 4) + *(LAS f32x4*)(red + 768 + lane * 4));
    st16(x.rs, __builtin_bit_cast(u32x4, t), (lane >= 24 ? AUX_B - 384 : 0) + lane * 16, OFF_AUX + cu * 2 * AUX_B + kb * 384);
  }
  asm volatile("s_waitcnt vmcnt(0) lgkmcnt(0)\n\ts_barrier" ::: "memory");
  STAMP(0, 11);
  const u64 tag = (((u64)(unsigned)__builtin_amdgcn_readfirstlane((unsigned)(epoch >> 32)) << 32) | (unsigned)__builtin_amdgcn_readfirstlane((unsigned)epoch)) *
                      6364136223846793005ull + 1442695040888963407ull;
  if (wid == 0) set_flag(ws, tag, 0, bid, lane);
  UNROLL for (int g = 0; g < 3; g++) {
    const u32x4 fr = __builtin_bit_cast(u32x4, lds_trfrag<512>(p + 24576 + g * 8192));
    st16(x.rs, fr, lane * 16, OFF_WT64 + g * 2 * MBy + (cu * 128 + step * 2 + nh) * 1024);
    if (g == 1) colsum(fr, nh * 32);
  }
  if (wid == 5 && lane < 24) {
    const int slot = (bid & 3) * 3 + (lane >> 3);
    if (slot != 0 && slot != 8) st16(x.rs, (u32x4){0u, 0u, 0u, 0u}, slot * 128 + (lane & 7) * 16, OFF_FL + (bid >> 2) * 4096);
  }
  const u32x4 e = {EMPTY, EMPTY, EMPTY, EMPTY};
  st16(x.rs, e, tid * 16, OFF_SLAB + (x.ah * 8 + x.at) * SLAB_B);
  if (tid < 16) st16(x.rs, e, tid * 16, OFF_SLAB + (x.ah * 8 + x.at) * SLAB_B + 8192);
  if (wid == 7 && bid >= 128) {
    st16(x.rs, __builtin_bit_cast(u32x4, sm), lane * 16, OFF_WKVF + (bid - 128) * 1024);
    float c = 0.f;
    UNROLL for (int k = 0; k < 4; k++) c = dot2(__builtin_bit_cast(u32x4, sm)[k], 0x3f803f80u, c);
    c = half_add(swap16_add(c, c));
    if (lane < 16) st4(x.rs, F2U(c), lane * 4, OFF_CSKV + ((bid >= 192) * 2 + (bid & 1)) * 2048 + ((bid >> 1) & 31) * 64);
  }
  lgkm_bar();
  if (wid == 6 && lane < 16) {
    u32x4 t;
    UNROLL for (int j = 0; j < 4; j++) t[j] = F2U((red[j * 16 + lane] + red[256 + j * 16 + lane]) + (red[512 + j * 16 + lane] + red[768 + j * 16 + lane]));
    st16(x.rs, t, lane * 16, OFF_CS + cu * 4096 + kb * 256);
  }
  if (wid == 0) wait_flags(ws, tag, 0, lane < 16 ? (x.ah >> 1) * 16 + lane : (lane < 48 ? x.at * 32 + lane - 16 : x.ah * 2 + (lane & 1)), lane < 50);
  asm volatile("s_waitcnt vmcnt(0) lgkmcnt(0)\n\ts_barrier" ::: "memory");
  if (wid == 0) set_flag(ws, tag, 8, bid, lane);
  return tag;
}

DI void phase_a(const Ctx& x, int L, u32x4 (&res)[4]) {
  char* ws = x.ws;
  const int lane = x.lane, wid = x.wid, hi = x.hi, h = a_head(x), tt = a_tile(x), r = lane & 31, rt = tt * 8 + wid;
  const lds_ptr lds = x.lds;
  constexpr int NSTEP = 64, PD = 16, I0 = 6;
  if (L) { STAMP(4 * L, 0); }
  auto aux = [&]() AI { gldsN_sc1<1>(ws + OFF_AUX + h * AUX_B + wid * 1024, lane * 16, x.l0 + A_AUX + wid * 1024); };
  float csv[3][8];
  bf16x8 wph[2], csf[3], mf;
  float rstd;
  if (L) {
    if (wid == 0) wait_flags(ws, 1u, 4 * L, tt * 32 + lane, lane < 32);
    UNROLL for (int c = 4; c < 6; c++) dma_wqkv(x, c, wid);
    aux();
    wait_vm_bar<6>();
  }

  STAMP(4 * L, 10);
  const unsigned abase = act_off(4 * L) + rt * 65536;
  auto aload = [&](int s) AI { return __builtin_bit_cast(bf16x8, ld16(x.rs, lane * 16 + (s & 3) * 1024, abase + (s >> 2) * 4096)); };
  bf16x8 a[PD];
  UNROLL for (int s = 0; s < I0; s++) a[s] = aload(s);
  f32x16 acc[3] = {{}, {}, {}};
  const int q4 = (lane & 15) >> 2;
  bf16x8 bf[2][3];
  const LdsLane ll(lds, lane);
  auto bread = [&](int s) AI { UNROLL for (int i = s < 48 ? 0 : 1; i < 3; i++) bf[s & 1][i] = ll.rd(((s % 48) * 3 + i) * 1024); };
  u32x4 stv[4];
  if (L == 0) {
    UNROLL for (int c = 2; c < 6; c++) dma_wqkv(x, c, wid);
    aux();
    wait_vm_bar<I0 + 12>();
  }
  STAMP(4 * L, 1); STAMPC(4 * L, 14);
  bread(0);
  static_for<NSTEP>([&](auto sc) AI {
    constexpr int s = decltype(sc)::value;
    if (s == 15) { wait_vm_bar<ramp(14, I0, PD) - I0>(); dma_wqkv(x, 6, wid); }
    if (s == 25) {
      if (L == 0 && wid == 0)
        _Pragma("nounroll") for (unsigned spins = 0; spins < SPIN_CAP && !all_flags_asm(ws, x.tag, 8, lane); spins++) {}
      bar_only(); dma_wqkv(x, 7, wid); dma_w64<0, 2>(x, 0, wid * 2); }
    if (s == 47) {
      asm volatile("s_waitcnt vmcnt(22) lgkmcnt(0)\n\ts_barrier" ::: "memory");
      dma_w64<0, 2>(x, 1, wid * 2); dma_w64<0, 2>(x, 2, wid * 2);
    }
    if (s == 28) stats_load(x.rs, OFF_ST + 2 * L * 262144 + rt * 4096, lane, stv);
    if (s == 42) {
      UNROLL for (int i = 0; i < 3; i++) UNROLL for (int k = 0; k < 8; k++) csv[i][k] = *(LAS float*)(lds + A_AUX + ((hi * 8 + k) * 96 + i * 32 + r) * 4);
      UNROLL for (int i = 0; i < 2; i++) wph[i] = ll.rd(A_AUX + 6144 + i * 1024);
    }
    if (s == 44) {
      float mean;
      stats_finish(stv, -1, mean, rstd);
      mf = mean_frag(mean);
      UNROLL for (int i = 0; i < 3; i++) csf[i] = cs_frag(half_add(((csv[i][0] + csv[i][1]) + (csv[i][2] + csv[i][3])) + ((csv[i][4] + csv[i][5]) + (csv[i][6] + csv[i][7]))), hi);
    }
    if (s == 45) UNROLL for (int i = 0; i < 3; i++) acc[i] = MFMA32(csf[i], mf, acc[i]);
    if (s == 48) {
      if (L == 0) UNROLL for (int i = 0; i < 4; i++)
        res[i] = ld16(x.rs, i * 1024 + lane * 16, act_off(0) + ((x.grp * 2 + (x.idx & 1)) * 4 + (wid & 3)) * 65536 + (x.idx >> 1) * 4096);
    }
    if (s + 1 < NSTEP) bread(s + 1);
    SB();
    const bf16x8 av = a[s % PD];
    UNROLL for (int i = s < 48 ? 0 : 1; i < 3; i++) acc[i] = MFMA32(bf[s & 1][i], av, acc[i]);
    if (s == 48) asm volatile("" : "+v"(acc[0]));
    if (s == 44) asm volatile("" : "+v"(rstd), "+v"(wph[0]), "+v"(wph[1]));
    SB();
    UNROLL for (int n = ramp(s - 1, I0, PD); n < ramp(s, I0, PD); n++) a[n % PD] = aload(n);
    SB();
  });
  STAMP(4 * L, 2); STAMPC(4 * L, 15);
  const f32x16 zero = {};
  {
    UNROLL for (int i = 0; i < 16; i++) acc[2][i] *= rstd;
    f32x16 p = MFMA32(wph[0], pack8(acc[1], 0), zero);
    p = MFMA32(wph[1], pack8(acc[1], 1), p);
    const lds_ptr row = lds + A_IMG + (wid * 32 + r) * A_ROW + hi * 8;
    UNROLL for (int g = 0; g < 4; g++) *(LAS u32x2*)(row + 128 + g * 16) = (u32x2){cvtpk(acc[2][4 * g], acc[2][4 * g + 1]), cvtpk(acc[2][4 * g + 2], acc[2][4 * g + 3])};
    f32x16 ep, em;
    dual_softmax<false>(p, rstd * LOG2E, ep, em);
    UNROLL for (int g = 0; g < 4; g++) {
      *(LAS u32x2*)(row + g * 16) = (u32x2){cvtpk(ep[4 * g], ep[4 * g + 1]), cvtpk(ep[4 * g + 2], ep[4 * g + 3])};
      *(LAS u32x2*)(row + 64 + g * 16) = (u32x2){cvtpk(em[4 * g], em[4 * g + 1]), cvtpk(em[4 * g + 2], em[4 * g + 3])};
    }
  }
  STAMP(4 * L, 3);
  lgkm_bar();
  STAMP(4 * L, 4);
  const unsigned slab = OFF_SLAB + (L * 256 + h * 8) * SLAB_B;
  {
    __builtin_amdgcn_s_setprio(3);
    const int ft = wid >> 1, dt = wid & 1;
    const lds_ptr ip = lds + A_IMG + (8 * (lane >> 4) + q4) * A_ROW + (lane & 3) * 8;
    bf16x8 pf[8], vf[8];
    UNROLL for (int u = 0; u < 8; u++) { pf[u] = lds_trfrag<4 * A_ROW>(ip + u * 32 * A_ROW + ft * 32); vf[u] = lds_trfrag<4 * A_ROW>(ip + u * 32 * A_ROW + 128 + dt * 32); }
    SB();
    f32x4 kv = {}, z = {};
    UNROLL for (int u = 0; u < 8; u++) kv = MFMA16(pf[u], vf[u], kv);
    st16(x.rs, not_empty(kv), lane * 16, slab + tt * SLAB_B + wid * 1024);
    if (dt == 0) {
      const short one = (lane & 15) == 0 ? (short)0x3f80 : (short)0;
      const bf16x8 ones = {one, one, one, one, one, one, one, one};
      UNROLL for (int u = 0; u < 8; u++) z = MFMA16(pf[u], ones, z);
      if ((lane & 15) == 0) st16(x.rs, not_empty(z), lane, slab + tt * SLAB_B + 8192 + ft * 64);
    }
  }
  __builtin_amdgcn_s_setprio(0);
  STAMP(4 * L, 5);
  u32x4 gv[8];
  unsigned gz;
  auto gather = [&]() AI {
    UNROLL for (int t = 0; t < 8; t++)
      asm volatile("global_load_dwordx4 %0, %1, %2 sc1" : "=v"(gv[t]) : "v"(lane * 16), "s"(ws + slab + t * SLAB_B + wid * 1024) : "memory");
    asm volatile("global_load_dword %0, %1, %2 sc1" : "=v"(gz) : "v"((lane >> 3) * SLAB_B + (lane & 7) * 4), "s"(ws + slab + 8192 + wid * 32) : "memory");
  };
#define GATHER_WAIT(N)                                                                                                                   \
  asm volatile("s_waitcnt vmcnt(" #N ")"                                                                                                 \
               : "+v"(gv[0]), "+v"(gv[1]), "+v"(gv[2]), "+v"(gv[3]), "+v"(gv[4]), "+v"(gv[5]), "+v"(gv[6]), "+v"(gv[7]), "+v"(gz)::"memory")
  bf16x8 phq[4];
  float qs;
  {
    bf16x8 qw[16];
    UNROLL for (int s = 0; s < 16; s++) qw[s] = ll.rd(s * 3072);
    SB();
    UNROLL for (int s = 0; s < 16; s++) acc[0] = MFMA32(qw[s], a[s], acc[0]);
    asm volatile("" : "+v"(acc[0]));
    gather();
    SB();
    f32x16 p = MFMA32(wph[0], pack8(acc[0], 0), zero);
    p = MFMA32(wph[1], pack8(acc[0], 1), p);
    f32x16 ep, em;
    qs = dual_softmax<true>(p, rstd * LOG2E, ep, em);
    phq[0] = pack8(ep, 0); phq[1] = pack8(ep, 1); phq[2] = pack8(em, 0); phq[3] = pack8(em, 1);
    asm volatile("" : "+v"(phq[0]), "+v"(phq[1]), "+v"(phq[2]), "+v"(phq[3]));
  }
  GATHER_WAIT(0);
  STAMP(4 * L, 6);
  _Pragma("nounroll") for (unsigned spins = 0; spins < SPIN_CAP; spins++) {
    unsigned m = gz;
    UNROLL for (int t = 0; t < 8; t++) UNROLL for (int j = 0; j < 4; j++) m = m > gv[t][j] ? m : gv[t][j];
    if (all_lanes(m != EMPTY)) break;
    asm volatile("s_sleep 2" ::: "memory");
    gather();
    GATHER_WAIT(0);
  }
  STAMP(4 * L, 7);
  {
    f32x4 s;
    UNROLL for (int j = 0; j < 4; j++)
      s[j] = ((U2F(gv[0][j]) + U2F(gv[1][j])) + (U2F(gv[2][j]) + U2F(gv[3][j]))) + ((U2F(gv[4][j]) + U2F(gv[5][j])) + (U2F(gv[6][j]) + U2F(gv[7][j])));
    *(LAS u32x2*)(lds + A_KVL + (wid >> 1) * 1024 + ((wid & 1) * 16 + (lane & 15) + ((lane >> 4) & 1) * 32) * 16 + hi * 8) = (u32x2){cvtpk(s[0], s[1]), cvtpk(s[2], s[3])};
    float zs = U2F(gz);
    zs += shfl(zs, lane ^ 8); zs += shfl(zs, lane ^ 16); zs = half_add(zs);
    if (lane < 8) *(LAS u16*)(lds + A_ZL + (wid * 8 + lane) * 2) = (u16)cvtpk(zs, zs);
  }
  lgkm_bar();
  STAMP(4 * L, 8);
  {
    f32x16 num = zero, den = zero;
    UNROLL for (int fs = 0; fs < 4; fs++) {
      const bf16x8 kf = lds_b128(lds + A_KVL + fs * 1024 + lane * 16);
      const s16x4 z0 = *(LAS s16x4*)(lds + A_ZL + fs * 32 + hi * 8), z1 = *(LAS s16x4*)(lds + A_ZL + fs * 32 + hi * 8 + 16);
      num = MFMA32(kf, phq[fs], num);
      den = MFMA32(__builtin_shufflevector(z0, z1, 0, 1, 2, 3, 4, 5, 6, 7), phq[fs], den);
    }
    const float rd = 1.f / (den[0] + 1e-6f * qs);
    UNROLL for (int gp = 0; gp < 2; gp++) {
      float v[8];
      gather8(num, gp, v);
      st16(x.rs, (u32x4){cvtpk(v[0] * rd, v[1] * rd), cvtpk(v[2] * rd, v[3] * rd), cvtpk(v[4] * rd, v[5] * rd), cvtpk(v[6] * rd, v[7] * rd)},
           gp * 1024 + lane * 16, act_off(4 * L + 1) + rt * 65536 + h * 2048);
    }
  }
  wait_vm_bar<0>();
  if (wid == 7) set_flag(ws, 1u, 4 * L + 1, tt * 32 + h, lane);
  STAMP(4 * L, 9);
}

constexpr unsigned w64_tbl(int which) { unsigned t = 0; for (int c = 0; c < 8; c++) t |= (unsigned)(w64_at(which, c) >> 14) << (4 * c); return t; }
template <int WHICH, class Hook>
DI void gemm64(const Ctx& x, unsigned abase, f32x16 (&acc)[2], Hook hook) {
  const int lane = x.lane;
  auto aload = [&](unsigned base, int j) AI { return __builtin_bit_cast(bf16x8, ld16(x.rs, lane * 16 + (j & 3) * 1024, base + (j >> 2) * 4096)); };
  bf16x8 a[16], bf[4][2];
  UNROLL for (int j = 0; j < 16; j++) a[j] = aload(abase, j);
  const lds_ptr lb = x.lds + lane * 16;
  lds_ptr b0 = lb + w64_at(WHICH, 0);
  UNROLL for (int j = 0; j < 2; j++) UNROLL for (int n = 0; n < 2; n++) bf[j][n] = lds_b128(b0 + j * 2048 + n * 1024);
  auto trip = [&](int it, auto lastc) AI {
    constexpr bool LAST = decltype(lastc)::value;
    const unsigned t = w64_tbl(WHICH) >> (8 * it);
    const lds_ptr b1 = lb + ((t >> 4) & 15) * 16384, b2 = lb + ((t >> 8) & 15) * 16384;
    abase += 16384;
    static_for<16>([&](auto jc) AI {
      constexpr int j = decltype(jc)::value, r = j + 2;
      hook(it, j);
      if (!LAST || r < 16) UNROLL for (int n = 0; n < 2; n++) bf[r & 3][n] = lds_b128((r < 8 ? b0 : (r < 16 ? b1 : b2)) + (r & 7) * 2048 + n * 1024);
      SB();
      acc[0] = MFMA32(bf[j & 3][0], a[j], acc[0]);
      acc[1] = MFMA32(bf[j & 3][1], a[j], acc[1]);
      SB();
      if (!LAST) a[j] = aload(abase, j);
      SB();
    });
    b0 = b2;
  };
  _Pragma("nounroll") for (int it = 0; it < 3; it++) trip(it, IC<0>{});
  trip(3, IC<1>{});
}

template <bool AFTER_B>
DI void out_compute(const Ctx& x, int L, u32x4 (&res)[4], char* out) {
  char* ws = x.ws;
  const int lane = x.lane, hi = x.hi, ph = 4 * L + (AFTER_B ? 3 : 1);
  const bool last = AFTER_B && L == 1;
  const int pn = x.idx >> 1, rt = (x.grp * 2 + (x.idx & 1)) * 4 + x.wid, r = lane & 31;
  STAMP(ph, 0);
  bar_only();
  STAMP(ph, 3);
  f32x16 acc[2] = {{}, {}};
  gemm64<AFTER_B ? 2 : 0>(x, act_off(ph) + rt * 65536, acc, [&](int it, int j) AI {
    if ((j == 14 && it == 2) || (!AFTER_B && j == 6 && it == 1)) bar_only();
  });
  STAMP(ph, 1);
  float ss = 0.f, sq = 0.f;
  UNROLL for (int i = 0; i < 4; i++) {
    float v[8];
    gather8(acc[i >> 1], i & 1, v);
    UNROLL for (int j = 0; j < 4; j++) res[i][j] = cvtpk(v[2 * j] + bflo(res[i][j]), v[2 * j + 1] + bfhi(res[i][j]));
    if (last) *(GAS u32x4*)(out + (size_t)(rt * 32 + r) * 2048 + pn * 128 + i * 32 + hi * 16) = res[i];
    else st16(x.rs, res[i], i * 1024 + lane * 16, act_off(ph + 1) + rt * 65536 + pn * 4096);
    UNROLL for (int j = 0; j < 4; j++) { ss = dot2(res[i][j], 0x3f803f80u, ss); sq = dot2(res[i][j], res[i][j], sq); }
  }
  if (!last) {
    ss = half_add(ss); sq = half_add(sq);
    const u32x4 sp = stats_pair(ss, sq);
    if (lane < 16) st16(x.rs, sp, lane * 16, OFF_ST + ((ph + 1) >> 1) * 262144 + rt * 4096 + pn * 256);
  }
  bar_only();
  wait_vm_bar<0>();
  STAMP(ph, 2);
}
DI void phase_out_b(const Ctx& x, int L, u32x4 (&res)[4], char* out) {
  if (x.wid < 4) {
    out_compute<true>(x, L, res, out);
    if (x.wid == 0 && L == 0) set_flag(x.ws, 1u, 4, x.grp * 32 + (x.idx & 1) * 16 + (x.idx >> 1), x.lane);
    return;
  }
  const int w4 = x.wid - 4, id0 = x.grp * 32 + (x.idx & 1) * 16;
  if (w4 == 3) wait_flags(x.ws, 1u, 4 * L + 3, id0 + x.lane, x.lane < 16);
  else {
    UNROLL for (int c = 6; c < 8; c++) { dma_w64<2, 3>(x, c, w4 * 5); dma_w64<2, 2>(x, c, w4 * 5 + 3); if (w4 == c - 6) dma_w64<2, 1>(x, c, 15); }
    if (L == 0) UNROLL for (int i = 0; i < 3; i++) { if (w4 * 3 + i < 8) dma_wqkv(x, 0, w4 * 3 + i); }
  }
  bar_only();
  drain_vm();
  bar_only();
  if (L == 0) UNROLL for (int c = 1; c < 4; c++) UNROLL for (int i = 0; i < 2; i++) dma_wqkv(x, c, w4 * 2 + i);
  bar_only();
  bar_only();
}

constexpr int B_QX = 65536, B_PART = 98304;
constexpr int kv_at(int tile) { return (((tile >> 3) + 3) % 5) * 32768 + (tile & 7) * 4096; }
constexpr float QSCALE = 0.17677669529663687f * LOG2E;
DI void phase_out_a_b(const Ctx& x, int L, u32x4 (&res)[4]) {
  char* ws = x.ws;
  const lds_ptr lds = x.lds;
  const int lane = x.lane, wid = x.wid, hi = x.hi, g4 = lane >> 4, c16 = lane & 15, ph = 4 * L + 2;
  const int pm = x.grp * 2 + (x.idx & 1), pj = x.idx >> 1;
  const int rt = pm * 4 + (wid & 3), head = pj * 2 + (wid >> 2);
  const unsigned stb = OFF_ST + (2 * L + 1) * 262144, kvf = OFF_KVF + L * 262144;
  const int id0 = x.grp * 32 + (x.idx & 1) * 16;
  auto kv_dma = [&](int ch, int first) AI {
    gldsN_sc1<4>(ws + kvf + ((ch + x.grp) & 7) * 32768 + first * 1024, lane * 16, x.l0 + kv_at(ch * 8) + first * 1024);
  };
  auto kv_dma2 = [&](int ch, int first) AI {
    gldsN_sc1<2>(ws + kvf + ((ch + x.grp) & 7) * 32768 + first * 1024, lane * 16, x.l0 + kv_at(ch * 8) + first * 1024);
  };
  auto kv_all = [&]() AI { return all_flags(x.rs, 10 + L, lane); };
  bool kv_ok = true;
  bf16x8 qf[2];
  if (wid < 4) {
    out_compute<false>(x, L, res, nullptr);
    STAMP(ph, 0);
    u32x4 csv[4];
    stats_load(x.rs, OFF_CS + pj * 4096, lane, csv);
    bar_only();
    STAMP(ph, 1);
    f32x16 acc[2] = {{}, {}};
    u32x4 stv[4];
    float rstd;
    gemm64<1>(x, act_off(ph) + rt * 65536, acc, [&](int it, int j) AI {
      if (j == 0 && it == 3) stats_load(x.rs, stb + rt * 4096, lane, stv);
      if (j == 13 && it == 3) {
        float mean;
        stats_finish(stv, -1, mean, rstd);
        const bf16x8 mf = mean_frag(mean);
        float t[4];
        UNROLL for (int k = 0; k < 4; k++) t[k] = (U2F(csv[0][k]) + U2F(csv[1][k])) + (U2F(csv[2][k]) + U2F(csv[3][k]));
        UNROLL for (int n = 0; n < 2; n++) acc[n] = MFMA32(cs_frag(half_add(swap16_add(t[2 * n], t[2 * n + 1])), hi), mf, acc[n]);
      }
    });
    STAMP(ph, 2);
    const float sc = rstd * QSCALE;
    UNROLL for (int i = 0; i < 16; i++) { acc[0][i] *= sc; acc[1][i] *= sc; }
    qf[0] = pack8(acc[0], 0); qf[1] = pack8(acc[0], 1);
    bar_only();
    UNROLL for (int s = 0; s < 2; s++) *(LAS bf16x8*)(lds + B_QX + (wid * 2 + s) * 1024 + lane * 16) = pack8(acc[1], s);
    lgkm_bar();
  } else {
    const int w4 = wid - 4, krt = pm * 4 + (pj >> 2), kth = (pj >> 1) & 1, ktype = pj & 1, u = wid & 1;
    bf16x8 kx[8], kw[8][2];
    auto kw_load = [&]() AI {
      UNROLL for (int i = 0; i < 8; i++) UNROLL for (int n = 0; n < 2; n++)
        kw[i][n] = __builtin_bit_cast(bf16x8, ld16(x.rs, lane * 16 + n * 1024, OFF_WKVF + (ktype * 32 + w4 * 8 + i) * 2048));
    };
    if (wid == 7) wait_flags(ws, 1u, ph - 1, x.grp * 32 + lane, lane < 32);
    else {
      UNROLL for (int c = 3; c < 8; c++) { dma_w64<0, 3>(x, c, w4 * 5); dma_w64<0, 2>(x, c, w4 * 5 + 3); if (w4 == c % 3) dma_w64<0, 1>(x, c, 15); }
      UNROLL for (int c = 0; c < 2; c++) { dma_w64<1, 3>(x, c, w4 * 5); dma_w64<1, 2>(x, c, w4 * 5 + 3); if (w4 == c) dma_w64<1, 1>(x, c, 15); }
    }
    bar_only();
    wait_vm_bar<0>();
    if (L == 0) {
      const u32x4 e = {EMPTY, EMPTY, EMPTY, EMPTY};
      const int b = x.grp * 32 + x.idx;
      UNROLL for (int i = 0; i < 2; i++) st16(x.rs, e, lane * 16, OFF_SLAB + (256 + b) * SLAB_B + (w4 * 2 + i) * 1024);
      if (w4 == 0 && lane < 16) st16(x.rs, e, lane * 16, OFF_SLAB + (256 + b) * SLAB_B + 8192);
    }
    u32x4 cp[2] = {};
    if (wid >= 6) UNROLL for (int i = 0; i < 8; i++) cp[i >> 2][i & 3] = ld4(x.rs, i * 256 + lane * 4, OFF_CSKV + (ktype * 2 + u) * 2048);
    bar_only();
    UNROLL for (int c = 2; c < 7; c++) dma_w64<1, 4>(x, c, w4 * 4);
    kw_load();
    bar_only();
    dma_w64<1, 4>(x, 7, w4 * 4);
    volatile LAS unsigned* pcnt = (volatile LAS unsigned*)(lds + B_PART);
    if (wid == 6) { pcnt[0] = 0u; pcnt[16] = 0u; }
    bar_only();
    STAMP(ph, 0);
    if (wid == 5) { set_flag(ws, 1u, ph, id0 + pj, lane); wait_flags(ws, 1u, ph, id0 + lane, lane < 16); }
    f32x4 cs = {};
    if (wid >= 6) {
      const float c = half_add(swap16_add(sum8(cp), sum8(cp)));
      UNROLL for (int j = 0; j < 4; j++) { const float cj = shfl(c, 4 * g4 + j); cs[j] = ktype ? c : cj; }
    }
    asm volatile("s_waitcnt vmcnt(0) lgkmcnt(0)\n\ts_barrier" ::: "memory");
    __builtin_amdgcn_s_setprio(2);
    UNROLL for (int i = 0; i < 8; i++)
      kx[i] = __builtin_bit_cast(bf16x8, ld16(x.rs, (g4 >> 1) * 1024 + (16 * kth + c16 + 32 * (g4 & 1)) * 16 + (i & 1) * 2048,
                                              act_off(ph) + krt * 65536 + w4 * 16384 + (i >> 1) * 4096));
    u32x4 sv[4];
    if (wid >= 6) stats_load(x.rs, stb + krt * 4096, lane, sv);
    f32x4 d[2] = {{}, {}};
    if (ktype == 0) { UNROLL for (int i = 0; i < 8; i++) UNROLL for (int n = 0; n < 2; n++) d[n] = MFMA16(kw[i][n], kx[i], d[n]); }
    else { UNROLL for (int i = 0; i < 8; i++) UNROLL for (int n = 0; n < 2; n++) d[n] = MFMA16(kx[i], kw[i][n], d[n]); }
    UNROLL for (int n = 0; n < 2; n++) *(LAS f32x4*)(lds + B_PART + 1024 + (w4 * 2 + n) * 1024 + lane * 16) = d[n];
    asm volatile("s_waitcnt lgkmcnt(0)" ::: "memory");
    if (lane == 0) __hip_atomic_fetch_add((LAS unsigned*)(lds + B_PART), 1u, __ATOMIC_RELAXED, __HIP_MEMORY_SCOPE_WORKGROUP);
    if (wid >= 6) {
      float mean, rstd;
      stats_finish(sv, kth, mean, rstd);
      f32x4 m4, r4;
      UNROLL for (int j = 0; j < 4; j++) {
        const float mj = shfl(mean, 4 * g4 + j), rj = shfl(rstd, 4 * g4 + j);
        m4[j] = ktype ? mj : mean; r4[j] = ktype ? rj : rstd;
      }
      _Pragma("nounroll") for (unsigned spins = 0; spins < SPIN_CAP * 16 && pcnt[0] != 4u; spins++) __builtin_amdgcn_s_sleep(1);
      asm volatile("" ::: "memory");
      f32x4 e = -m4 * cs;
      UNROLL for (int w = 0; w < 4; w++) e += *(LAS f32x4*)(lds + B_PART + 1024 + (w * 2 + u) * 1024 + lane * 16);
      e *= r4;
      const int frag = ktype ? 2 + kth : u, lp = (ktype ? 16 * u : 16 * kth) + c16 + 32 * (g4 & 1);
      const unsigned e0 = cvtpk(e[0], e[1]), e1 = cvtpk(e[2], e[3]);
      const auto w0 = __builtin_amdgcn_permlane32_swap(e0, e0, false, false), w1 = __builtin_amdgcn_permlane32_swap(e1, e1, false, false);
      if (lane < 32) st16(x.rs, (u32x4){w0[0], w1[0], w0[1], w1[1]}, frag * 1024 + lp * 16, kvf + krt * 4096);
      if (last_arriver(lds + B_PART + 64, 2, lane)) set_flag(ws, 1u, 10 + L, x.grp * 32 + x.idx, lane);
    }
    __builtin_amdgcn_s_setprio(0);
    {
      STAMP(ph, 1);
      if (wid == 7) {
        wait_flags(ws, 1u, 10 + L, x.grp * 32 + lane, lane < 32);
        STAMP(ph, 2);
        UNROLL for (int i = 0; i < 8; i++) kv_dma(0, i * 4);
        kv_ok = kv_all();
        drain_vm();
        STAMP(ph, 7);
      }
    }
    __builtin_amdgcn_s_setprio(0);
    bar_only();
    if (wid == 7) *(LAS unsigned*)(lds + B_QX + 8192) = kv_ok;
    lgkm_bar();
    UNROLL for (int s = 0; s < 2; s++) qf[s] = lds_b128(lds + B_QX + ((wid & 3) * 2 + s) * 1024 + lane * 16);
  }
  __builtin_amdgcn_s_waitcnt(0x0F70);
  const bool kv_early = __builtin_amdgcn_readfirstlane(*(LAS unsigned*)(lds + B_QX + 8192)) != 0;
  STAMP(ph, 3); STAMPC(ph, 14);
  if (kv_early) kv_dma(1, wid * 4);
  const bf16x8 sel = sel_frag(lane);
  const f32x16 zero = {};
  f32x16 O = zero, negm, SA, SB_;
  f32x4 LS = {};
  bf16x8 kf[2], vf[2];
  const LdsLane ll(lds, lane);
  kf[0] = ll.rd(kv_at(0)); kf[1] = ll.rd(kv_at(0) + 1024);
  SA = MFMA32(kf[0], qf[0], zero); SA = MFMA32(kf[1], qf[1], SA);
  kf[0] = ll.rd(kv_at(1)); kf[1] = ll.rd(kv_at(1) + 1024);
  vf[0] = ll.rd(kv_at(0) + 2048); vf[1] = ll.rd(kv_at(0) + 3072);
  {
    float m = SA[0];
    UNROLL for (int i = 1; i < 16; i++) m = __builtin_fmaxf(m, SA[i]);
    m = half_max(m);
    UNROLL for (int i = 0; i < 16; i++) { negm[i] = -m; SA[i] -= m; }
  }
#define EX(i) Sc[i] = __builtin_amdgcn_exp2f(Sc[i])
#define PIN(x) asm volatile("" : "+v"(x))
  u32x4 p0, p1 = {0u, 0u, 0u, 0u};
  auto step = [&](f32x16& Sc, f32x16& Sn, int kp, int vc, int vp) AI {
    SB();
    Sn = MFMA32(kf[0], qf[0], negm);
    SB();
    kf[0] = ll.rd(kp);
    EX(0); EX(1); EX(2); PIN(Sc);
    SB();
    O = MFMA32(vf[1], __builtin_bit_cast(bf16x8, p1), O);
    SB();
    vf[1] = ll.rd(vc + 3072);
    EX(3); EX(4); EX(5); PIN(Sc);
    SB();
    Sn = MFMA32(kf[1], qf[1], Sn);
    SB();
    kf[1] = ll.rd(kp + 1024);
    EX(6); EX(7);
    p0[0] = cvtpk(Sc[0], Sc[1]); p0[1] = cvtpk(Sc[2], Sc[3]); p0[2] = cvtpk(Sc[4], Sc[5]); p0[3] = cvtpk(Sc[6], Sc[7]); PIN(p0);
    SB();
    LS = MFMA16(sel, __builtin_bit_cast(bf16x8, p1), LS);
    SB();
    EX(8); EX(9); PIN(Sc);
    SB();
    O = MFMA32(vf[0], __builtin_bit_cast(bf16x8, p0), O);
    SB();
    vf[0] = ll.rd(vp + 2048);
    EX(10); EX(11); EX(12); PIN(Sc);
    SB();
    LS = MFMA16(sel, __builtin_bit_cast(bf16x8, p0), LS);
    SB();
    EX(13); EX(14); EX(15);
    p1[0] = cvtpk(Sc[8], Sc[9]); p1[1] = cvtpk(Sc[10], Sc[11]); p1[2] = cvtpk(Sc[12], Sc[13]); p1[3] = cvtpk(Sc[14], Sc[15]); PIN(p1);
    SB();
  };
  auto hook = [&](int t) AI {
    if (t < 2) { if (kv_early) kv_dma(2 + t, wid * 4); }
    if (t == 2 && !kv_ok) {
      _Pragma("nounroll") for (unsigned spins = 0; spins < SPIN_CAP && !kv_all(); spins++) asm volatile("s_sleep 4" ::: "memory");
      _Pragma("nounroll") for (int i = 0; i < 8; i++) { kv_dma(1, i * 4); kv_dma(2, i * 4); kv_dma(3, i * 4); }
    }
    if (t == 6) wait_vm_bar<0>();
    if (t == 8 || t == 10) kv_dma2(4, wid * 4 + t - 8);
    if (t == 25) wait_vm_bar<0>();
    if (t >= 25 && t < 31) kv_dma2(5 + (t - 25) / 2, wid * 4 + ((t - 25) & 1) * 2);
    if (t == 38) wait_vm_bar<0>();
    if (t == 39 || t == 41) dma_w64<2, 2>(x, (t - 39) / 2, wid * 2);
    if (t == 49) bar_only();
    if (t >= 50 && t < 58 && !(t & 1)) dma_w64<2, 2>(x, 2 + (t - 50) / 2, wid * 2);
  };
  if (wid >= 4) __builtin_amdgcn_s_setprio(1);
  static_for<32>([&](auto tc) AI {
    constexpr int t = decltype(tc)::value * 2;
    hook(t);
    step(SA, SB_, kv_at((t + 2) & 63), kv_at(t), kv_at(t + 1));
    hook(t + 1);
    step(SB_, SA, kv_at((t + 3) & 63), kv_at(t + 1), kv_at((t + 2) & 63));
  });
  O = MFMA32(vf[1], __builtin_bit_cast(bf16x8, p1), O);
  LS = MFMA16(sel, __builtin_bit_cast(bf16x8, p1), LS);
  STAMP(ph, 4); STAMPC(ph, 15);
  __builtin_amdgcn_s_setprio(0);
  float l = LS[0];
  {
    float chk = l;
    UNROLL for (int i = 0; i < 16; i++) chk = __builtin_fmaxf(chk, __builtin_fabsf(O[i]) * 0x1p-27f);
    bool bad = !(chk < 0x1p100f);
    UNROLL for (int i = 0; i < 16; i++) bad |= (O[i] != O[i]);
    if (any_lane(bad)) {
      O = zero; l = 0.f;
      float m = -__builtin_inff();
      for (int t = 0; t < 64; t++) {
        bf16x8 f[4];
        UNROLL for (int i = 0; i < 4; i++) f[i] = __builtin_bit_cast(bf16x8, ld16(x.rs, i * 1024 + lane * 16, kvf + t * 4096));
        f32x16 s = MFMA32(f[0], qf[0], zero);
        s = MFMA32(f[1], qf[1], s);
        float mt = s[0];
        UNROLL for (int i = 1; i < 16; i++) mt = __builtin_fmaxf(mt, s[i]);
        const float mn = __builtin_fmaxf(m, half_max(mt));
        const float al = __builtin_amdgcn_exp2f(m - mn);
        m = mn;
        UNROLL for (int i = 0; i < 16; i++) { s[i] = __builtin_amdgcn_exp2f(s[i] - mn); O[i] *= al; }
        const bf16x8 p0 = pack8(s, 0), p1 = pack8(s, 1);
        f32x4 lt = MFMA16(sel, p0, ((f32x4){0.f, 0.f, 0.f, 0.f}));
        lt = MFMA16(sel, p1, lt);
        l = l * al + lt[0];
        O = MFMA32(f[2], p0, O);
        O = MFMA32(f[3], p1, O);
      }
    }
  }
  const float rl = 1.f / l;
  UNROLL for (int gp = 0; gp < 2; gp++) {
    float v[8];
    gather8(O, gp, v);
    st16(x.rs, (u32x4){cvtpk(v[0] * rl, v[1] * rl), cvtpk(v[2] * rl, v[3] * rl), cvtpk(v[4] * rl, v[5] * rl), cvtpk(v[6] * rl, v[7] * rl)},
         gp * 1024 + lane * 16, act_off(ph + 1) + rt * 65536 + head * 2048);
  }
  asm volatile("s_waitcnt vmcnt(0) lgkmcnt(0)\n\ts_barrier" ::: "memory");
  if (wid == 7) set_flag(ws, 1u, ph + 1, id0 + pj, lane);
  STAMP(ph, 5);
}

__global__ __launch_bounds__(512, 2) void k_main(char* ws, char* out, Inputs in) {
  extern __shared__ __attribute__((aligned(16))) char lds_raw[];
  Ctx x;
  x.ws = ws;
  x.rs = __builtin_amdgcn_make_buffer_rsrc(ws, 0, WS_USED, 0x00020000);
  x.lds = (lds_ptr)lds_raw;
  x.l0 = (unsigned)(uintptr_t)x.lds;
  x.lane = TID & 63;
  x.wid = __builtin_amdgcn_readfirstlane(TID >> 6);
  const int pg = BID & 7, pi = BID >> 3;
  x.ah = (pg >> 1) * 8 + (pi >> 2); x.at = (pg & 1) * 4 + (pi & 3);
  x.grp = (pg & 6) | (pi & 1); x.idx = (((pg & 1) * 8 + (pi >> 2)) << 1) | ((pi >> 1) & 1);
  x.hi = x.lane >> 5;
  x.tag = 0;
  x.tag = phase_p(fresh(x, 0), in, BID);
  { const Ctx y = fresh(x, 0); UNROLL for (int c = 0; c < 2; c++) dma_wqkv(y, c, y.wid); }
  u32x4 res[4] = {};
  for (int L = 0; L < 2; L++) {
    phase_a(fresh(x, 0), L, res);
    phase_out_a_b(fresh(x, 0), L, res);
    phase_out_b(fresh(x, 0), L, res, out);
  }
  if (BID == 0 && TID == 0) __hip_atomic_store((GAS u64*)(ws + OFF_EPOCH), x.tag, RLX_AGENT);
}

#if defined(__HIP_DEVICE_COMPILE__)
#pragma clang attribute pop
#endif

extern "C" void kernel_launch(void* const* d_in, const int* in_sizes, int n_in, void* d_out, int out_size, void* d_ws, size_t ws_size,
                              hipStream_t stream) {
  char* ws = (char*)d_ws;
  (void)hipFuncSetAttribute((const void*)k_main, hipFuncAttributeMaxDynamicSharedMemorySize, LDS_ALL);
  const Inputs in = {(const char*)d_in[0], (const char*)d_in[1], (const char*)d_in[2], (const char*)d_in[3],
                     (const char*)d_in[5], (const char*)d_in[6], (const char*)d_in[7], (const char*)d_in[8]};
  k_main<<<256, 512, LDS_ALL, stream>>>(ws, (char*)d_out, in);
}
```

```cpp
#include <hip/hip_runtime.h>
#include <hip/hip_bf16.h>
#include <stdint.h>

typedef unsigned short u16;
using bf16x8 = __attribute__((ext_vector_type(8))) short;
using s16x4 = __attribute__((ext_vector_type(4))) short;
using f32x16 = __attribute__((ext_vector_type(16))) float;
using f32x4 = __attribute__((ext_vector_type(4))) float;
using f32x2 = __attribute__((ext_vector_type(2))) float;
using u32x4 = __attribute__((ext_vector_type(4))) unsigned;
using u32x2 = __attribute__((ext_vector_type(2))) unsigned;
typedef __bf16 bf16x2_t __attribute__((ext_vector_type(2)));
#define LAS __attribute__((address_space(3)))
#define GAS __attribute__((address_space(1)))
typedef LAS char* lds_ptr;
typedef short v4i16_t __attribute__((ext_vector_type(4)));
#define DI __device__ __forceinline__
#define MFMA32(a, b, c) __builtin_amdgcn_mfma_f32_32x32x16_bf16((a), (b), (c), 0, 0, 0)
#define MFMA16(a, b, c) __builtin_amdgcn_mfma_f32_16x16x32_bf16((a), (b), (c), 0, 0, 0)
#define SB() __builtin_amdgcn_sched_barrier(0)
#define RLX_AGENT __ATOMIC_RELAXED, __HIP_MEMORY_SCOPE_AGENT
#define UNROLL _Pragma("unroll")
#define AI __attribute__((always_inline))

constexpr float LOG2E = 1.4426950408889634f;
constexpr unsigned SPIN_CAP = 1u << 20;

constexpr unsigned MBy = 1u << 20;
constexpr unsigned OFF_EPOCH = 0;
constexpr unsigned OFF_FL = 768 * 1024;
constexpr unsigned OFF_CSKV = 120 * 1024;
constexpr unsigned OFF_CS = 512 * 1024;
constexpr unsigned OFF_WKVF = 128 * 1024;
constexpr unsigned OFF_AUX = 256 * 1024;
constexpr unsigned AUX_B = 8192;
constexpr unsigned OFF_ST = 1 * MBy;
constexpr unsigned OFF_KVF = 2 * MBy;
constexpr unsigned OFF_SLAB = 4 * MBy;
constexpr unsigned SLAB_B = 8448;
constexpr unsigned OFF_ACT = 16 * MBy;
constexpr unsigned OFF_WTA = 48 * MBy;
constexpr unsigned OFF_WT64 = 54 * MBy;
constexpr unsigned WS_USED = 60 * MBy;

#if defined(__HIP_DEVICE_COMPILE__)
#pragma clang attribute push(__attribute__((target("no-packed-fp32-ops"))), apply_to = function)
#endif
#define U2F(x) __builtin_bit_cast(float, (unsigned)(x))
#define F2U(x) __builtin_bit_cast(unsigned, (float)(x))
#define TID ((int)__builtin_amdgcn_workitem_id_x())
#define BID ((int)__builtin_amdgcn_workgroup_id_x())
DI bool all_lanes(bool p) { return __builtin_amdgcn_ballot_w64(!p) == 0; }
DI bool any_lane(bool p) { return __builtin_amdgcn_ballot_w64(p) != 0; }
DI float shfl(float v, int src) { return U2F(__builtin_amdgcn_ds_bpermute(src * 4, (int)F2U(v))); }
#define STAMP(k, i)
#define STAMPC(k, i)
DI void glds16(const void* sbase, unsigned voff, unsigned lds_base) {
  unsigned sv;
  asm volatile("s_mov_b32 %0, m0\n\ts_mov_b32 m0, %3\n\ts_nop 0\n\tglobal_load_lds_dwordx4 %1, %2\n\ts_mov_b32 m0, %0"
               : "=&s"(sv) : "v"(voff), "s"(sbase), "s"(lds_base) : "memory");
}
#define GL_(o, sc) "global_load_lds_dwordx4 %1, %2 offset:" #o sc "\n\t"
#define GLDS_N(NAME, SC)                                                                                                   \
  template <int N> DI void NAME(const void* sbase, unsigned voff, unsigned lds_base) {                                     \
    unsigned sv;                                                                                                           \
    if constexpr (N == 1)                                                                                                  \
      asm volatile("s_mov_b32 %0, m0\n\ts_mov_b32 m0, %3\n\ts_nop 0\n\t" GL_(0, SC) "s_mov_b32 m0, %0"                    \
                   : "=&s"(sv) : "v"(voff), "s"(sbase), "s"(lds_base) : "memory");                                         \
    else if constexpr (N == 2)                                                                                                  \
      asm volatile("s_mov_b32 %0, m0\n\ts_mov_b32 m0, %3\n\ts_nop 0\n\t" GL_(0, SC) GL_(1024, SC) "s_mov_b32 m0, %0"       \
                   : "=&s"(sv) : "v"(voff), "s"(sbase), "s"(lds_base) : "memory");                                         \
    else if constexpr (N == 3)                                                                                             \
      asm volatile("s_mov_b32 %0, m0\n\ts_mov_b32 m0, %3\n\ts_nop 0\n\t" GL_(0, SC) GL_(1024, SC) GL_(2048, SC) "s_mov_b32 m0, %0" \
                   : "=&s"(sv) : "v"(voff), "s"(sbase), "s"(lds_base) : "memory");                                         \
    else                                                                                                                   \
      asm volatile("s_mov_b32 %0, m0\n\ts_mov_b32 m0, %3\n\ts_nop 0\n\t" GL_(0, SC) GL_(1024, SC) GL_(2048, SC) GL_(3072, SC) "s_mov_b32 m0, %0" \
                   : "=&s"(sv) : "v"(voff), "s"(sbase), "s"(lds_base) : "memory");                                         \
  }
GLDS_N(gldsN_sc1, " sc1")
DI void glds16_sc1(const void* sbase, unsigned voff, unsigned lds_base) {
  unsigned sv;
  asm volatile("s_mov_b32 %0, m0\n\ts_mov_b32 m0, %3\n\ts_nop 0\n\tglobal_load_lds_dwordx4 %1, %2 sc1\n\ts_mov_b32 m0, %0"
               : "=&s"(sv) : "v"(voff), "s"(sbase), "s"(lds_base) : "memory");
}
template <int N> DI void wait_vm_bar() { asm volatile("s_waitcnt vmcnt(%0)\n\ts_barrier" ::"n"(N) : "memory"); }
DI void bar_only() { asm volatile("s_barrier" ::: "memory"); }
DI void lgkm_bar() { asm volatile("s_waitcnt lgkmcnt(0)\n\ts_barrier" ::: "memory"); }
DI void drain_vm() { asm volatile("s_waitcnt vmcnt(0)" ::: "memory"); }
DI s16x4 lds_tr(lds_ptr p) { return __builtin_bit_cast(s16x4, __builtin_amdgcn_ds_read_tr16_b64_v4i16((LAS v4i16_t*)p)); }
template <int E> DI bf16x8 lds_trfrag(lds_ptr p) { s16x4 lo = lds_tr(p), hi = lds_tr(p + E); return __builtin_shufflevector(lo, hi, 0, 1, 2, 3, 4, 5, 6, 7); }
DI bf16x8 lds_b128(lds_ptr p) { return *(LAS bf16x8*)p; }
DI unsigned cvtpk(float lo, float hi) {
  bf16x2_t v = __builtin_convertvector((f32x2){lo, hi}, bf16x2_t);
  return __builtin_bit_cast(unsigned, v);
}
DI float bflo(unsigned u) { return U2F(u << 16); }
DI float bfhi(unsigned u) { return U2F(u & 0xffff0000u); }
DI float dot2(unsigned a, unsigned b, float c) {
  return __builtin_amdgcn_fdot2_f32_bf16(__builtin_bit_cast(bf16x2_t, a), __builtin_bit_cast(bf16x2_t, b), c, false);
}
DI float half_add(float x) { auto r = __builtin_amdgcn_permlane32_swap(F2U(x), F2U(x), false, false); return U2F(r[0]) + U2F(r[1]); }
DI float half_max(float x) { auto r = __builtin_amdgcn_permlane32_swap(F2U(x), F2U(x), false, false); return __builtin_fmaxf(U2F(r[0]), U2F(r[1])); }
DI float half_min(float x) { auto r = __builtin_amdgcn_permlane32_swap(F2U(x), F2U(x), false, false); return __builtin_fminf(U2F(r[0]), U2F(r[1])); }
DI bf16x8 pack8(const f32x16& x, int s) {
  u32x4 p = {cvtpk(x[8 * s], x[8 * s + 1]), cvtpk(x[8 * s + 2], x[8 * s + 3]), cvtpk(x[8 * s + 4], x[8 * s + 5]), cvtpk(x[8 * s + 6], x[8 * s + 7])};
  return __builtin_bit_cast(bf16x8, p);
}
DI void gather8(const f32x16& a, int gp, float (&v)[8]) {
  UNROLL for (int j = 0; j < 4; j++) {
    auto s = __builtin_amdgcn_permlane32_swap(F2U(a[8 * gp + j]), F2U(a[8 * gp + 4 + j]), false, false);
    v[j] = U2F(s[0]); v[4 + j] = U2F(s[1]);
  }
}
typedef __amdgpu_buffer_rsrc_t rsrc_t;
DI u32x4 ld16(rsrc_t rs, unsigned voff, unsigned soff) { return __builtin_amdgcn_raw_buffer_load_b128(rs, voff, soff, 16); }
DI unsigned ld4(rsrc_t rs, unsigned voff, unsigned soff) { return __builtin_amdgcn_raw_buffer_load_b32(rs, voff, soff, 16); }
DI void st16(rsrc_t rs, u32x4 v, unsigned voff, unsigned soff) { __builtin_amdgcn_raw_buffer_store_b128(v, rs, voff + soff, 0, 16); }
DI void st8(rsrc_t rs, u32x2 v, unsigned voff, unsigned soff) { __builtin_amdgcn_raw_buffer_store_b64(v, rs, voff, soff, 16); }
DI void st4(rsrc_t rs, unsigned v, unsigned voff, unsigned soff) { __builtin_amdgcn_raw_buffer_store_b32(v, rs, voff, soff, 16); }
constexpr unsigned EMPTY = 0xffffffffu;
DI u32x4 not_empty(f32x4 v) {
  u32x4 r;
  UNROLL for (int j = 0; j < 4; j++) r[j] = F2U(v[j]) < EMPTY ? F2U(v[j]) : EMPTY - 1;
  return r;
}
typedef unsigned long long u64;
template <class T> DI GAS T* flag_at(char* ws, int slot, int id) { return (GAS T*)(ws + OFF_FL + (id >> 2) * 4096 + slot * 128) + (id & 3); }
template <class T> DI void set_flag(char* ws, T val, int slot, int id, int lane) {
  if (lane == 0) __hip_atomic_store(flag_at<T>(ws, slot, id), val, RLX_AGENT);
}
template <class T> DI void wait_flags(char* ws, T val, int slot, int id, bool on) {
  GAS T* p = flag_at<T>(ws, slot, id);
  _Pragma("nounroll") for (unsigned spins = 0; spins < SPIN_CAP; spins++) {
    T v = val;
    if (on) v = __hip_atomic_load(p, RLX_AGENT);
    if (all_lanes(v == val)) break;
    __builtin_amdgcn_s_sleep(1);
  }
  asm volatile("" ::: "memory");
}
DI bool all_flags(rsrc_t rs, int slot, int lane) {
  const u32x4 v = ld16(rs, lane * 4096, OFF_FL + slot * 128);
  return all_lanes((v[0] & v[1] & v[2] & v[3]) == 1u);
}
DI bool all_flags_asm(char* ws, u64 tag, int slot, int lane) {
  u32x4 a, b;
  asm volatile("global_load_dwordx4 %0, %2, %3 sc1\n\tglobal_load_dwordx4 %1, %2, %3 offset:16 sc1\n\ts_waitcnt vmcnt(0)"
               : "=&v"(a), "=&v"(b) : "v"(lane * 4096), "s"(ws + OFF_FL + slot * 128) : "memory");
  const unsigned lo = (unsigned)tag, hi = (unsigned)(tag >> 32);
  return all_lanes(a[0] == lo && a[2] == lo && b[0] == lo && b[2] == lo && a[1] == hi && a[3] == hi && b[1] == hi && b[3] == hi);
}
DI bool last_arriver(lds_ptr cnt, unsigned n, int lane) {
  drain_vm();
  unsigned old = 0;
  if (lane == 0) old = __hip_atomic_fetch_add((LAS unsigned*)cnt, 1u, __ATOMIC_RELAXED, __HIP_MEMORY_SCOPE_WORKGROUP);
  return (unsigned)__builtin_amdgcn_readfirstlane(old) == n - 1;
}
DI float swap16_add(float a, float b) {
  auto r = __builtin_amdgcn_permlane16_swap(F2U(a), F2U(b), false, false);
  return U2F(r[0]) + U2F(r[1]);
}
DI float sum8(const u32x4 (&v)[2]) {
  return ((U2F(v[0][0]) + U2F(v[0][1])) + (U2F(v[0][2]) + U2F(v[0][3]))) + ((U2F(v[1][0]) + U2F(v[1][1])) + (U2F(v[1][2]) + U2F(v[1][3])));
}
DI void stats_load(rsrc_t rs, unsigned base, int lane, u32x4 (&v)[4]) {
  UNROLL for (int i = 0; i < 4; i++) v[i] = ld16(rs, lane * 16 + i * 1024, base);
}
DI u32x4 stats_pair(float s, float q) {
  auto a = __builtin_amdgcn_permlane16_swap(F2U(s), F2U(s), false, false), b = __builtin_amdgcn_permlane16_swap(F2U(q), F2U(q), false, false);
  return (u32x4){a[0], b[0], a[1], b[1]};
}
DI void stats_finish(const u32x4 (&v)[4], int half, float& mean, float& rstd) {
  float t[4];
  UNROLL for (int j = 0; j < 4; j++) t[j] = (U2F(v[0][j]) + U2F(v[1][j])) + (U2F(v[2][j]) + U2F(v[3][j]));
  if (half == 0) { t[2] = t[0]; t[3] = t[1]; }
  if (half == 1) { t[0] = t[2]; t[1] = t[3]; }
  const float s = half_add(swap16_add(t[0], t[2])), q = half_add(swap16_add(t[1], t[3]));
  mean = s * (1.f / 1024.f);
  rstd = __builtin_amdgcn_rsqf(__builtin_fmaxf(q * (1.f / 1024.f) - mean * mean, 0.f) + 1e-5f);
}
DI void split3(float x, unsigned& a, unsigned& b, unsigned& c) {
  a = cvtpk(x, x) & 0xffffu; x -= bflo(a);
  b = cvtpk(x, x) & 0xffffu; x -= bflo(b);
  c = cvtpk(x, x) & 0xffffu;
}
DI bf16x8 cs_frag(float cs, int hi) {
  unsigned a, b, c; split3(cs, a, b, c);
  u32x4 p = {a | (b << 16), a | (c << 16), b | (a << 16), 0u};
  if (hi) p = (u32x4){0u, 0u, 0u, 0u};
  return __builtin_bit_cast(bf16x8, p);
}
DI bf16x8 mean_frag(float mean) {
  unsigned a, b, c; split3(-mean, a, b, c);
  u32x4 p = {a | (a << 16), b | (a << 16), b | (c << 16), 0u};
  return __builtin_bit_cast(bf16x8, p);
}
constexpr int ramp(int s, int I0, int PD) { int n = I0 + 2 * (s + 1); if (n > s + 1 + PD) n = s + 1 + PD; return n > 64 ? 64 : n; }
template <int V> struct IC { static constexpr int value = V; };
template <int N, int I = 0, class F> DI void static_for(F&& f) {
  if constexpr (I < N) { f(IC<I>{}); static_for<N, I + 1>(f); }
}
template <bool RATIO> DI float dual_softmax(const f32x16& p, float c, f32x16& ep, f32x16& em) {
  float mx = p[0], mn = p[0];
  UNROLL for (int i = 1; i < 16; i++) { mx = __builtin_fmaxf(mx, p[i]); mn = __builtin_fminf(mn, p[i]); }
  mx = half_max(mx); mn = half_min(mn);
  const float a = -mx * c, b = mn * c;
  float sp = 0.f, sm = 0.f;
  UNROLL for (int i = 0; i < 16; i++) {
    ep[i] = __builtin_amdgcn_exp2f(__builtin_fmaf(p[i], c, a));
    em[i] = __builtin_amdgcn_exp2f(__builtin_fmaf(p[i], -c, b));
    sp += ep[i]; sm += em[i];
  }
  sp = half_add(sp); sm = __builtin_amdgcn_rcpf(half_add(sm));
  if (RATIO) { sm *= sp; UNROLL for (int i = 0; i < 16; i++) em[i] *= sm; }
  else { const float rp = __builtin_amdgcn_rcpf(sp); UNROLL for (int i = 0; i < 16; i++) { ep[i] *= rp; em[i] *= sm; } }
  return sp;
}
DI bf16x8 sel_frag(int lane) {
  short v = ((lane & 3) == 0 && ((lane >> 2) & 1) == ((lane >> 4) & 1)) ? (short)0x3f80 : (short)0;
  return (bf16x8){v, v, v, v, v, v, v, v};
}

struct Ctx {
  char* ws; rsrc_t rs; lds_ptr lds; unsigned l0; u64 tag;
  int lane, wid, grp, idx, hi, ah, at;
};
DI Ctx fresh(const Ctx& x, int swap) {
  Ctx y = x;
  y.wid ^= swap;
  asm volatile("" : "+s"(y.wid), "+s"(y.grp), "+s"(y.idx), "+s"(y.ah), "+s"(y.at));
  asm volatile("" : "+v"(y.lane));
  y.lane &= 63;
  y.hi = y.lane >> 5;
  return y;
}
struct LdsLane {
  lds_ptr b0, b1, b2;
  DI LdsLane(lds_ptr lds, int lane) : b0(lds + lane * 16), b1(b0 + 65536), b2(b0 + 131072) { asm volatile("" : "+v"(b0), "+v"(b1), "+v"(b2)); }
  DI bf16x8 rd(int off) const { return lds_b128((off < 65536 ? b0 : (off < 131072 ? b1 : b2)) + (off & 65535)); }
};
constexpr int LDS_ALL = 163840;
constexpr int A_SLOT = 24576, A_ROW = 200, A_IMG = 106496, A_KVL = A_IMG + 256 * A_ROW, A_ZL = A_KVL + 4096, A_AUX = 6 * A_SLOT;
static_assert(A_ZL + 128 <= LDS_ALL, "");
DI unsigned act_off(int i) { return OFF_ACT + i * 4 * MBy; }

DI int a_head(const Ctx& x) { return x.ah; }
DI int a_tile(const Ctx& x) { return x.at; }
DI void dma_wqkv(const Ctx& x, int c, int pg) {
  gldsN_sc1<3>(x.ws + OFF_WTA + a_head(x) * 196608 + c * A_SLOT + pg * 3072, x.lane * 16, x.l0 + (c % 6) * A_SLOT + pg * 3072);
}
constexpr int w64_at(int which, int c) {
  constexpr int t[3][8] = {{48, 64, 80, 0, 16, 32, 96, 112}, {128, 144, 48, 64, 80, 0, 16, 32}, {32, 48, 64, 80, 96, 112, 128, 144}};
  return t[which][c] * 1024;
}
template <int WHICH, int N> DI void dma_w64(const Ctx& x, int c, int p) {
  gldsN_sc1<N>(x.ws + OFF_WT64 + WHICH * 2 * MBy + (x.idx >> 1) * 131072 + c * 16384 + p * 1024, x.lane * 16, x.l0 + w64_at(WHICH, c) + p * 1024);
}
struct Inputs { const char *x0, *wqkv, *wphi, *woA, *wq, *wk, *wv, *woB; };
DI u64 phase_p(const Ctx& x, const Inputs& in, int bid) {
  char* ws = x.ws;
  STAMP(0, 0);
  const u64 epoch = __hip_atomic_load((GAS u64*)(ws + OFF_EPOCH), RLX_AGENT);
  const lds_ptr lds = x.lds;
  const int lane = x.lane, wid = x.wid, hi = x.hi, r = lane & 31, tid = wid * 64 + lane;
  const int kb = bid & 15, cu = bid >> 4;
  UNROLL for (int g = 0; g < 3; g++)
    glds16(in.wqkv + (size_t)(kb * 64 + wid * 8) * 6144 + g * 2048 + cu * 128, (lane >> 3) * 6144 + (lane & 7) * 16, x.l0 + g * 8192 + wid * 1024);
  const int rt = bid >> 2, ct = (bid & 3) * 4 + (wid & 3);
  u32x4 xv[4];
  if (wid < 4) UNROLL for (int i = 0; i < 4; i++) xv[i] = *(const GAS u32x4*)(in.x0 + (size_t)(rt * 32 + r) * 2048 + ct * 128 + i * 32 + hi * 16);
  bf16x8 sm;
  if (wid == 7 && bid < 64) UNROLL for (int j = 0; j < 8; j++)
    sm[j] = ((const GAS short*)in.wphi)[(bid >> 1) * 1024 + (16 * (bid & 1) + 8 * (j >> 2) + 4 * hi + (j & 3)) * 32 + r];
  if (wid == 7 && bid >= 128) {
    const GAS short* W = (const GAS short*)(bid >= 192 ? in.wv : in.wk);
    UNROLL for (int j = 0; j < 8; j++) sm[j] = W[(32 * ((bid >> 1) & 31) + 8 * (lane >> 4) + j) * 32 + 16 * (bid & 1) + (lane & 15)];
  }
  wait_vm_bar<0>();
  UNROLL for (int g = 0; g < 3; g++)
    glds16((g == 0 ? in.woA : (g == 1 ? in.wq : in.woB)) + (size_t)(kb * 64 + wid * 8) * 2048 + cu * 128, (lane >> 3) * 2048 + (lane & 7) * 16,
           x.l0 + 24576 + g * 8192 + wid * 1024);
  const int ks = wid >> 1, nh = wid & 1, step = kb * 4 + ks;
  const lds_ptr p = lds + (ks * 16 + 8 * hi + ((lane & 15) >> 2)) * 128 + nh * 64 + ((lane >> 4) & 1) * 32 + (lane & 3) * 8;
  LAS float* red = (LAS float*)(lds + 49152);
  auto colsum = [&](u32x4 fr, int at) AI {
    float c = 0.f;
    UNROLL for (int k = 0; k < 4; k++) c = dot2(fr[k], 0x3f803f80u, c);
    c = half_add(c);
    if (hi == 0) red[ks * 256 + at + r] = c;
  };
  UNROLL for (int g = 0; g < 3; g++) {
    const u32x4 fr = __builtin_bit_cast(u32x4, lds_trfrag<512>(p + g * 8192));
    st16(x.rs, fr, lane * 16, OFF_WTA + ((cu * 2 + nh) * 192 + step * 3 + g) * 1024);
    colsum(fr, nh * 96 + g * 32);
  }
  if (wid < 4) {
    float s = 0, q = 0;
    UNROLL for (int i = 0; i < 4; i++) {
      st16(x.rs, xv[i], lane * 16, OFF_ACT + rt * 65536 + (ct * 4 + i) * 1024);
      UNROLL for (int j = 0; j < 4; j++) { s = dot2(xv[i][j], 0x3f803f80u, s); q = dot2(xv[i][j], xv[i][j], q); }
    }
    const u32x4 sp = stats_pair(half_add(s), half_add(q));
    if (lane < 16) st16(x.rs, sp, lane * 16, OFF_ST + rt * 4096 + ct * 256);
  }
  if (wid == 7 && bid < 64) st16(x.rs, __builtin_bit_cast(u32x4, sm), lane * 16, OFF_AUX + (bid >> 1) * AUX_B + 6144 + (bid & 1) * 1024);
  lgkm_bar();
  if (wid == 6 && lane < 48) {
    const f32x4 t = (*(LAS f32x4*)(red + lane * 4) + *(LAS f32x4*)(red + 256 + lane * 4)) + (*(LAS f32x4*)(red + 512 + lane * 4) + *(LAS f32x4*)(red + 768 + lane * 4));
    st16(x.rs, __builtin_bit_cast(u32x4, t), (lane >= 24 ? AUX_B - 384 : 0) + lane * 16, OFF_AUX + cu * 2 * AUX_B + kb * 384);
  }
  asm volatile("s_waitcnt vmcnt(0) lgkmcnt(0)\n\ts_barrier" ::: "memory");
  STAMP(0, 11);
  const u64 tag = (((u64)(unsigned)__builtin_amdgcn_readfirstlane((unsigned)(epoch >> 32)) << 32) | (unsigned)__builtin_amdgcn_readfirstlane((unsigned)epoch)) *
                      6364136223846793005ull + 1442695040888963407ull;
  if (wid == 0) set_flag(ws, tag, 0, bid, lane);
  UNROLL for (int g = 0; g < 3; g++) {
    const u32x4 fr = __builtin_bit_cast(u32x4, lds_trfrag<512>(p + 24576 + g * 8192));
    st16(x.rs, fr, lane * 16, OFF_WT64 + g * 2 * MBy + (cu * 128 + step * 2 + nh) * 1024);
    if (g == 1) colsum(fr, nh * 32);
  }
  if (wid == 5 && lane < 24) {
    const int slot = (bid & 3) * 3 + (lane >> 3);
    if (slot != 0 && slot != 8) st16(x.rs, (u32x4){0u, 0u, 0u, 0u}, slot * 128 + (lane & 7) * 16, OFF_FL + (bid >> 2) * 4096);
  }
  const u32x4 e = {EMPTY, EMPTY, EMPTY, EMPTY};
  st16(x.rs, e, tid * 16, OFF_SLAB + (x.ah * 8 + x.at) * SLAB_B);
  if (tid < 16) st16(x.rs, e, tid * 16, OFF_SLAB + (x.ah * 8 + x.at) * SLAB_B + 8192);
  if (wid == 7 && bid >= 128) {
    st16(x.rs, __builtin_bit_cast(u32x4, sm), lane * 16, OFF_WKVF + (bid - 128) * 1024);
    float c = 0.f;
    UNROLL for (int k = 0; k < 4; k++) c = dot2(__builtin_bit_cast(u32x4, sm)[k], 0x3f803f80u, c);
    c = half_add(swap16_add(c, c));
    if (lane < 16) st4(x.rs, F2U(c), lane * 4, OFF_CSKV + ((bid >= 192) * 2 + (bid & 1)) * 2048 + ((bid >> 1) & 31) * 64);
  }
  lgkm_bar();
  if (wid == 6 && lane < 16) {
    u32x4 t;
    UNROLL for (int j = 0; j < 4; j++) t[j] = F2U((red[j * 16 + lane] + red[256 + j * 16 + lane]) + (red[512 + j * 16 + lane] + red[768 + j * 16 + lane]));
    st16(x.rs, t, lane * 16, OFF_CS + cu * 4096 + kb * 256);
  }
  if (wid == 0) wait_flags(ws, tag, 0, lane < 16 ? (x.ah >> 1) * 16 + lane : (lane < 48 ? x.at * 32 + lane - 16 : x.ah * 2 + (lane & 1)), lane < 50);
  asm volatile("s_waitcnt vmcnt(0) lgkmcnt(0)\n\ts_barrier" ::: "memory");
  if (wid == 0) set_flag(ws, tag, 8, bid, lane);
  return tag;
}

DI void phase_a(const Ctx& x, int L, u32x4 (&res)[4]) {
  char* ws = x.ws;
  const int lane = x.lane, wid = x.wid, hi = x.hi, h = a_head(x), tt = a_tile(x), r = lane & 31, rt = tt * 8 + wid;
  const lds_ptr lds = x.lds;
  constexpr int NSTEP = 64, PD = 16, I0 = 6;
  if (L) { STAMP(4 * L, 0); }
  auto aux = [&]() AI { gldsN_sc1<1>(ws + OFF_AUX + h * AUX_B + wid * 1024, lane * 16, x.l0 + A_AUX + wid * 1024); };
  float csv[3][8];
  bf16x8 wph[2], csf[3], mf;
  float rstd;
  if (L) {
    if (wid == 0) wait_flags(ws, 1u, 4 * L, tt * 32 + lane, lane < 32);
    UNROLL for (int c = 4; c < 6; c++) dma_wqkv(x, c, wid);
    aux();
    wait_vm_bar<6>();
  }

  STAMP(4 * L, 10);
  const unsigned abase = act_off(4 * L) + rt * 65536;
  auto aload = [&](int s) AI { return __builtin_bit_cast(bf16x8, ld16(x.rs, lane * 16 + (s & 3) * 1024, abase + (s >> 2) * 4096)); };
  bf16x8 a[PD];
  UNROLL for (int s = 0; s < I0; s++) a[s] = aload(s);
  f32x16 acc[3] = {{}, {}, {}};
  const int q4 = (lane & 15) >> 2;
  bf16x8 bf[2][3];
  const LdsLane ll(lds, lane);
  auto bread = [&](int s) AI { UNROLL for (int i = s < 48 ? 0 : 1; i < 3; i++) bf[s & 1][i] = ll.rd(((s % 48) * 3 + i) * 1024); };
  u32x4 stv[4];
  if (L == 0) {
    UNROLL for (int c = 2; c < 6; c++) dma_wqkv(x, c, wid);
    aux();
    wait_vm_bar<I0 + 12>();
  }
  STAMP(4 * L, 1); STAMPC(4 * L, 14);
  bread(0);
  static_for<NSTEP>([&](auto sc) AI {
    constexpr int s = decltype(sc)::value;
    if (s == 15) { wait_vm_bar<ramp(14, I0, PD) - I0>(); dma_wqkv(x, 6, wid); }
    if (s == 25) {
      if (L == 0 && wid == 0)
        _Pragma("nounroll") for (unsigned spins = 0; spins < SPIN_CAP && !all_flags_asm(ws, x.tag, 8, lane); spins++) {}
      bar_only(); dma_wqkv(x, 7, wid); dma_w64<0, 2>(x, 0, wid * 2); }
    if (s == 47) {
      asm volatile("s_waitcnt vmcnt(22) lgkmcnt(0)\n\ts_barrier" ::: "memory");
      dma_w64<0, 2>(x, 1, wid * 2); dma_w64<0, 2>(x, 2, wid * 2);
    }
    if (s == 28) stats_load(x.rs, OFF_ST + 2 * L * 262144 + rt * 4096, lane, stv);
    if (s == 42) {
      UNROLL for (int i = 0; i < 3; i++) UNROLL for (int k = 0; k < 8; k++) csv[i][k] = *(LAS float*)(lds + A_AUX + ((hi * 8 + k) * 96 + i * 32 + r) * 4);
      UNROLL for (int i = 0; i < 2; i++) wph[i] = ll.rd(A_AUX + 6144 + i * 1024);
    }
    if (s == 44) {
      float mean;
      stats_finish(stv, -1, mean, rstd);
      mf = mean_frag(mean);
      UNROLL for (int i = 0; i < 3; i++) csf[i] = cs_frag(half_add(((csv[i][0] + csv[i][1]) + (csv[i][2] + csv[i][3])) + ((csv[i][4] + csv[i][5]) + (csv[i][6] + csv[i][7]))), hi);
    }
    if (s == 45) UNROLL for (int i = 0; i < 3; i++) acc[i] = MFMA32(csf[i], mf, acc[i]);
    if (s == 48) {
      if (L == 0) UNROLL for (int i = 0; i < 4; i++)
        res[i] = ld16(x.rs, i * 1024 + lane * 16, act_off(0) + ((x.grp * 2 + (x.idx & 1)) * 4 + (wid & 3)) * 65536 + (x.idx >> 1) * 4096);
    }
    if (s + 1 < NSTEP) bread(s + 1);
    SB();
    const bf16x8 av = a[s % PD];
    UNROLL for (int i = s < 48 ? 0 : 1; i < 3; i++) acc[i] = MFMA32(bf[s & 1][i], av, acc[i]);
    if (s == 48) asm volatile("" : "+v"(acc[0]));
    if (s == 44) asm volatile("" : "+v"(rstd), "+v"(wph[0]), "+v"(wph[1]));
    SB();
    UNROLL for (int n = ramp(s - 1, I0, PD); n < ramp(s, I0, PD); n++) a[n % PD] = aload(n);
    SB();
  });
  STAMP(4 * L, 2); STAMPC(4 * L, 15);
  const f32x16 zero = {};
  {
    UNROLL for (int i = 0; i < 16; i++) acc[2][i] *= rstd;
    f32x16 p = MFMA32(wph[0], pack8(acc[1], 0), zero);
    p = MFMA32(wph[1], pack8(acc[1], 1), p);
    const lds_ptr row = lds + A_IMG + (wid * 32 + r) * A_ROW + hi * 8;
    UNROLL for (int g = 0; g < 4; g++) *(LAS u32x2*)(row + 128 + g * 16) = (u32x2){cvtpk(acc[2][4 * g], acc[2][4 * g + 1]), cvtpk(acc[2][4 * g + 2], acc[2][4 * g + 3])};
    f32x16 ep, em;
    dual_softmax<false>(p, rstd * LOG2E, ep, em);
    UNROLL for (int g = 0; g < 4; g++) {
      *(LAS u32x2*)(row + g * 16) = (u32x2){cvtpk(ep[4 * g], ep[4 * g + 1]), cvtpk(ep[4 * g + 2], ep[4 * g + 3])};
      *(LAS u32x2*)(row + 64 + g * 16) = (u32x2){cvtpk(em[4 * g], em[4 * g + 1]), cvtpk(em[4 * g + 2], em[4 * g + 3])};
    }
  }
  STAMP(4 * L, 3);
  lgkm_bar();
  STAMP(4 * L, 4);
  const unsigned slab = OFF_SLAB + (L * 256 + h * 8) * SLAB_B;
  {
    __builtin_amdgcn_s_setprio(3);
    const int ft = wid >> 1, dt = wid & 1;
    const lds_ptr ip = lds + A_IMG + (8 * (lane >> 4) + q4) * A_ROW + (lane & 3) * 8;
    bf16x8 pf[8], vf[8];
    UNROLL for (int u = 0; u < 8; u++) { pf[u] = lds_trfrag<4 * A_ROW>(ip + u * 32 * A_ROW + ft * 32); vf[u] = lds_trfrag<4 * A_ROW>(ip + u * 32 * A_ROW + 128 + dt * 32); }
    SB();
    f32x4 kv = {}, z = {};
    UNROLL for (int u = 0; u < 8; u++) kv = MFMA16(pf[u], vf[u], kv);
    st16(x.rs, not_empty(kv), lane * 16, slab + tt * SLAB_B + wid * 1024);
    if (dt == 0) {
      const short one = (lane & 15) == 0 ? (short)0x3f80 : (short)0;
      const bf16x8 ones = {one, one, one, one, one, one, one, one};
      UNROLL for (int u = 0; u < 8; u++) z = MFMA16(pf[u], ones, z);
      if ((lane & 15) == 0) st16(x.rs, not_empty(z), lane, slab + tt * SLAB_B + 8192 + ft * 64);
    }
  }
  __builtin_amdgcn_s_setprio(0);
  STAMP(4 * L, 5);
  u32x4 gv[8];
  unsigned gz;
  auto gather = [&]() AI {
    UNROLL for (int t = 0; t < 8; t++)
      asm volatile("global_load_dwordx4 %0, %1, %2 sc1" : "=v"(gv[t]) : "v"(lane * 16), "s"(ws + slab + t * SLAB_B + wid * 1024) : "memory");
    asm volatile("global_load_dword %0, %1, %2 sc1" : "=v"(gz) : "v"((lane >> 3) * SLAB_B + (lane & 7) * 4), "s"(ws + slab + 8192 + wid * 32) : "memory");
  };
#define GATHER_WAIT(N)                                                                                                                   \
  asm volatile("s_waitcnt vmcnt(" #N ")"                                                                                                 \
               : "+v"(gv[0]), "+v"(gv[1]), "+v"(gv[2]), "+v"(gv[3]), "+v"(gv[4]), "+v"(gv[5]), "+v"(gv[6]), "+v"(gv[7]), "+v"(gz)::"memory")
  bf16x8 phq[4];
  float qs;
  {
    bf16x8 qw[16];
    UNROLL for (int s = 0; s < 16; s++) qw[s] = ll.rd(s * 3072);
    SB();
    UNROLL for (int s = 0; s < 16; s++) acc[0] = MFMA32(qw[s], a[s], acc[0]);
    asm volatile("" : "+v"(acc[0]));
    gather();
    SB();
    f32x16 p = MFMA32(wph[0], pack8(acc[0], 0), zero);
    p = MFMA32(wph[1], pack8(acc[0], 1), p);
    f32x16 ep, em;
    qs = dual_softmax<true>(p, rstd * LOG2E, ep, em);
    phq[0] = pack8(ep, 0); phq[1] = pack8(ep, 1); phq[2] = pack8(em, 0); phq[3] = pack8(em, 1);
    asm volatile("" : "+v"(phq[0]), "+v"(phq[1]), "+v"(phq[2]), "+v"(phq[3]));
  }
  GATHER_WAIT(0);
  STAMP(4 * L, 6);
  _Pragma("nounroll") for (unsigned spins = 0; spins < SPIN_CAP; spins++) {
    unsigned m = gz;
    UNROLL for (int t = 0; t < 8; t++) UNROLL for (int j = 0; j < 4; j++) m = m > gv[t][j] ? m : gv[t][j];
    if (all_lanes(m != EMPTY)) break;
    asm volatile("s_sleep 2" ::: "memory");
    gather();
    GATHER_WAIT(0);
  }
  STAMP(4 * L, 7);
  {
    f32x4 s;
    UNROLL for (int j = 0; j < 4; j++)
      s[j] = ((U2F(gv[0][j]) + U2F(gv[1][j])) + (U2F(gv[2][j]) + U2F(gv[3][j]))) + ((U2F(gv[4][j]) + U2F(gv[5][j])) + (U2F(gv[6][j]) + U2F(gv[7][j])));
    *(LAS u32x2*)(lds + A_KVL + (wid >> 1) * 1024 + ((wid & 1) * 16 + (lane & 15) + ((lane >> 4) & 1) * 32) * 16 + hi * 8) = (u32x2){cvtpk(s[0], s[1]), cvtpk(s[2], s[3])};
    float zs = U2F(gz);
    zs += shfl(zs, lane ^ 8); zs += shfl(zs, lane ^ 16); zs = half_add(zs);
    if (lane < 8) *(LAS u16*)(lds + A_ZL + (wid * 8 + lane) * 2) = (u16)cvtpk(zs, zs);
  }
  lgkm_bar();
  STAMP(4 * L, 8);
  {
    f32x16 num = zero, den = zero;
    UNROLL for (int fs = 0; fs < 4; fs++) {
      const bf16x8 kf = lds_b128(lds + A_KVL + fs * 1024 + lane * 16);
      const s16x4 z0 = *(LAS s16x4*)(lds + A_ZL + fs * 32 + hi * 8), z1 = *(LAS s16x4*)(lds + A_ZL + fs * 32 + hi * 8 + 16);
      num = MFMA32(kf, phq[fs], num);
      den = MFMA32(__builtin_shufflevector(z0, z1, 0, 1, 2, 3, 4, 5, 6, 7), phq[fs], den);
    }
    const float rd = 1.f / (den[0] + 1e-6f * qs);
    UNROLL for (int gp = 0; gp < 2; gp++) {
      float v[8];
      gather8(num, gp, v);
      st16(x.rs, (u32x4){cvtpk(v[0] * rd, v[1] * rd), cvtpk(v[2] * rd, v[3] * rd), cvtpk(v[4] * rd, v[5] * rd), cvtpk(v[6] * rd, v[7] * rd)},
           gp * 1024 + lane * 16, act_off(4 * L + 1) + rt * 65536 + h * 2048);
    }
  }
  wait_vm_bar<0>();
  if (wid == 7) set_flag(ws, 1u, 4 * L + 1, tt * 32 + h, lane);
  STAMP(4 * L, 9);
}

constexpr unsigned w64_tbl(int which) { unsigned t = 0; for (int c = 0; c < 8; c++) t |= (unsigned)(w64_at(which, c) >> 14) << (4 * c); return t; }
template <int WHICH, class Hook>
DI void gemm64(const Ctx& x, unsigned abase, f32x16 (&acc)[2], Hook hook) {
  const int lane = x.lane;
  auto aload = [&](unsigned base, int j) AI { return __builtin_bit_cast(bf16x8, ld16(x.rs, lane * 16 + (j & 3) * 1024, base + (j >> 2) * 4096)); };
  bf16x8 a[16], bf[4][2];
  UNROLL for (int j = 0; j < 16; j++) a[j] = aload(abase, j);
  const lds_ptr lb = x.lds + lane * 16;
  lds_ptr b0 = lb + w64_at(WHICH, 0);
  UNROLL for (int j = 0; j < 2; j++) UNROLL for (int n = 0; n < 2; n++) bf[j][n] = lds_b128(b0 + j * 2048 + n * 1024);
  auto trip = [&](int it, auto lastc) AI {
    constexpr bool LAST = decltype(lastc)::value;
    const unsigned t = w64_tbl(WHICH) >> (8 * it);
    const lds_ptr b1 = lb + ((t >> 4) & 15) * 16384, b2 = lb + ((t >> 8) & 15) * 16384;
    abase += 16384;
    static_for<16>([&](auto jc) AI {
      constexpr int j = decltype(jc)::value, r = j + 2;
      hook(it, j);
      if (!LAST || r < 16) UNROLL for (int n = 0; n < 2; n++) bf[r & 3][n] = lds_b128((r < 8 ? b0 : (r < 16 ? b1 : b2)) + (r & 7) * 2048 + n * 1024);
      SB();
      acc[0] = MFMA32(bf[j & 3][0], a[j], acc[0]);
      acc[1] = MFMA32(bf[j & 3][1], a[j], acc[1]);
      SB();
      if (!LAST) a[j] = aload(abase, j);
      SB();
    });
    b0 = b2;
  };
  _Pragma("nounroll") for (int it = 0; it < 3; it++) trip(it, IC<0>{});
  trip(3, IC<1>{});
}

template <bool AFTER_B>
DI void out_compute(const Ctx& x, int L, u32x4 (&res)[4], char* out) {
  char* ws = x.ws;
  const int lane = x.lane, hi = x.hi, ph = 4 * L + (AFTER_B ? 3 : 1);
  const bool last = AFTER_B && L == 1;
  const int pn = x.idx >> 1, rt = (x.grp * 2 + (x.idx & 1)) * 4 + x.wid, r = lane & 31;
  STAMP(ph, 0);
  bar_only();
  STAMP(ph, 3);
  f32x16 acc[2] = {{}, {}};
  gemm64<AFTER_B ? 2 : 0>(x, act_off(ph) + rt * 65536, acc, [&](int it, int j) AI {
    if ((j == 14 && it == 2) || (!AFTER_B && j == 6 && it == 1)) bar_only();
  });
  STAMP(ph, 1);
  float ss = 0.f, sq = 0.f;
  UNROLL for (int i = 0; i < 4; i++) {
    float v[8];
    gather8(acc[i >> 1], i & 1, v);
    UNROLL for (int j = 0; j < 4; j++) res[i][j] = cvtpk(v[2 * j] + bflo(res[i][j]), v[2 * j + 1] + bfhi(res[i][j]));
    if (last) *(GAS u32x4*)(out + (size_t)(rt * 32 + r) * 2048 + pn * 128 + i * 32 + hi * 16) = res[i];
    else st16(x.rs, res[i], i * 1024 + lane * 16, act_off(ph + 1) + rt * 65536 + pn * 4096);
    UNROLL for (int j = 0; j < 4; j++) { ss = dot2(res[i][j], 0x3f803f80u, ss); sq = dot2(res[i][j], res[i][j], sq); }
  }
  if (!last) {
    ss = half_add(ss); sq = half_add(sq);
    const u32x4 sp = stats_pair(ss, sq);
    if (lane < 16) st16(x.rs, sp, lane * 16, OFF_ST + ((ph + 1) >> 1) * 262144 + rt * 4096 + pn * 256);
  }
  bar_only();
  wait_vm_bar<0>();
  STAMP(ph, 2);
}
DI void phase_out_b(const Ctx& x, int L, u32x4 (&res)[4], char* out) {
  if (x.wid < 4) {
    out_compute<true>(x, L, res, out);
    if (x.wid == 0 && L == 0) set_flag(x.ws, 1u, 4, x.grp * 32 + (x.idx & 1) * 16 + (x.idx >> 1), x.lane);
    return;
  }
  const int w4 = x.wid - 4, id0 = x.grp * 32 + (x.idx & 1) * 16;
  if (w4 == 3) wait_flags(x.ws, 1u, 4 * L + 3, id0 + x.lane, x.lane < 16);
  else {
    UNROLL for (int c = 6; c < 8; c++) { dma_w64<2, 3>(x, c, w4 * 5); dma_w64<2, 2>(x, c, w4 * 5 + 3); if (w4 == c - 6) dma_w64<2, 1>(x, c, 15); }
    if (L == 0) UNROLL for (int i = 0; i < 3; i++) { if (w4 * 3 + i < 8) dma_wqkv(x, 0, w4 * 3 + i); }
  }
  bar_only();
  drain_vm();
  bar_only();
  if (L == 0) UNROLL for (int c = 1; c < 4; c++) UNROLL for (int i = 0; i < 2; i++) dma_wqkv(x, c, w4 * 2 + i);
  bar_only();
  bar_only();
}

constexpr int B_QX = 65536, B_PART = 98304;
constexpr int kv_at(int tile) { return (((tile >> 3) + 3) % 5) * 32768 + (tile & 7) * 4096; }
constexpr float QSCALE = 0.17677669529663687f * LOG2E;
DI void phase_out_a_b(const Ctx& x, int L, u32x4 (&res)[4]) {
  char* ws = x.ws;
  const lds_ptr lds = x.lds;
  const int lane = x.lane, wid = x.wid, hi = x.hi, g4 = lane >> 4, c16 = lane & 15, ph = 4 * L + 2;
  const int pm = x.grp * 2 + (x.idx & 1), pj = x.idx >> 1;
  const int rt = pm * 4 + (wid & 3), head = pj * 2 + (wid >> 2);
  const unsigned stb = OFF_ST + (2 * L + 1) * 262144, kvf = OFF_KVF + L * 262144;
  const int id0 = x.grp * 32 + (x.idx & 1) * 16;
  auto kv_dma = [&](int ch, int first) AI {
    gldsN_sc1<4>(ws + kvf + ((ch + x.grp) & 7) * 32768 + first * 1024, lane * 16, x.l0 + kv_at(ch * 8) + first * 1024);
  };
  auto kv_dma2 = [&](int ch, int first) AI {
    gldsN_sc1<2>(ws + kvf + ((ch + x.grp) & 7) * 32768 + first * 1024, lane * 16, x.l0 + kv_at(ch * 8) + first * 1024);
  };
  auto kv_all = [&]() AI { return all_flags(x.rs, 10 + L, lane); };
  bool kv_ok = true;
  bf16x8 qf[2];
  if (wid < 4) {
    out_compute<false>(x, L, res, nullptr);
    STAMP(ph, 0);
    u32x4 csv[4];
    stats_load(x.rs, OFF_CS + pj * 4096, lane, csv);
    bar_only();
    STAMP(ph, 1);
    f32x16 acc[2] = {{}, {}};
    u32x4 stv[4];
    float rstd;
    gemm64<1>(x, act_off(ph) + rt * 65536, acc, [&](int it, int j) AI {
      if (j == 0 && it == 3) stats_load(x.rs, stb + rt * 4096, lane, stv);
      if (j == 13 && it == 3) {
        float mean;
        stats_finish(stv, -1, mean, rstd);
        const bf16x8 mf = mean_frag(mean);
        float t[4];
        UNROLL for (int k = 0; k < 4; k++) t[k] = (U2F(csv[0][k]) + U2F(csv[1][k])) + (U2F(csv[2][k]) + U2F(csv[3][k]));
        UNROLL for (int n = 0; n < 2; n++) acc[n] = MFMA32(cs_frag(half_add(swap16_add(t[2 * n], t[2 * n + 1])), hi), mf, acc[n]);
      }
    });
    STAMP(ph, 2);
    const float sc = rstd * QSCALE;
    UNROLL for (int i = 0; i < 16; i++) { acc[0][i] *= sc; acc[1][i] *= sc; }
    qf[0] = pack8(acc[0], 0); qf[1] = pack8(acc[0], 1);
    bar_only();
    UNROLL for (int s = 0; s < 2; s++) *(LAS bf16x8*)(lds + B_QX + (wid * 2 + s) * 1024 + lane * 16) = pack8(acc[1], s);
    lgkm_bar();
  } else {
    const int w4 = wid - 4, krt = pm * 4 + (pj >> 2), kth = (pj >> 1) & 1, ktype = pj & 1, u = wid & 1;
    bf16x8 kx[8], kw[8][2];
    auto kw_load = [&]() AI {
      UNROLL for (int i = 0; i < 8; i++) UNROLL for (int n = 0; n < 2; n++)
        kw[i][n] = __builtin_bit_cast(bf16x8, ld16(x.rs, lane * 16 + n * 1024, OFF_WKVF + (ktype * 32 + w4 * 8 + i) * 2048));
    };
    if (wid == 7) wait_flags(ws, 1u, ph - 1, x.grp * 32 + lane, lane < 32);
    else {
      UNROLL for (int c = 3; c < 8; c++) { dma_w64<0, 3>(x, c, w4 * 5); dma_w64<0, 2>(x, c, w4 * 5 + 3); if (w4 == c % 3) dma_w64<0, 1>(x, c, 15); }
      UNROLL for (int c = 0; c < 2; c++) { dma_w64<1, 3>(x, c, w4 * 5); dma_w64<1, 2>(x, c, w4 * 5 + 3); if (w4 == c) dma_w64<1, 1>(x, c, 15); }
    }
    bar_only();
    wait_vm_bar<0>();
    if (L == 0) {
      const u32x4 e = {EMPTY, EMPTY, EMPTY, EMPTY};
      const int b = x.grp * 32 + x.idx;
      UNROLL for (int i = 0; i < 2; i++) st16(x.rs, e, lane * 16, OFF_SLAB + (256 + b) * SLAB_B + (w4 * 2 + i) * 1024);
      if (w4 == 0 && lane < 16) st16(x.rs, e, lane * 16, OFF_SLAB + (256 + b) * SLAB_B + 8192);
    }
    u32x4 cp[2] = {};
    if (wid >= 6) UNROLL for (int i = 0; i < 8; i++) cp[i >> 2][i & 3] = ld4(x.rs, i * 256 + lane * 4, OFF_CSKV + (ktype * 2 + u) * 2048);
    bar_only();
    UNROLL for (int c = 2; c < 7; c++) dma_w64<1, 4>(x, c, w4 * 4);
    kw_load();
    bar_only();
    dma_w64<1, 4>(x, 7, w4 * 4);
    volatile LAS unsigned* pcnt = (volatile LAS unsigned*)(lds + B_PART);
    if (wid == 6) { pcnt[0] = 0u; pcnt[16] = 0u; }
    bar_only();
    STAMP(ph, 0);
    if (wid == 5) { set_flag(ws, 1u, ph, id0 + pj, lane); wait_flags(ws, 1u, ph, id0 + lane, lane < 16); }
    f32x4 cs = {};
    if (wid >= 6) {
      const float c = half_add(swap16_add(sum8(cp), sum8(cp)));
      UNROLL for (int j = 0; j < 4; j++) { const float cj = shfl(c, 4 * g4 + j); cs[j] = ktype ? c : cj; }
    }
    asm volatile("s_waitcnt vmcnt(0) lgkmcnt(0)\n\ts_barrier" ::: "memory");
    __builtin_amdgcn_s_setprio(2);
    UNROLL for (int i = 0; i < 8; i++)
      kx[i] = __builtin_bit_cast(bf16x8, ld16(x.rs, (g4 >> 1) * 1024 + (16 * kth + c16 + 32 * (g4 & 1)) * 16 + (i & 1) * 2048,
                                              act_off(ph) + krt * 65536 + w4 * 16384 + (i >> 1) * 4096));
    u32x4 sv[4];
    if (wid >= 6) stats_load(x.rs, stb + krt * 4096, lane, sv);
    f32x4 d[2] = {{}, {}};
    if (ktype == 0) { UNROLL for (int i = 0; i < 8; i++) UNROLL for (int n = 0; n < 2; n++) d[n] = MFMA16(kw[i][n], kx[i], d[n]); }
    else { UNROLL for (int i = 0; i < 8; i++) UNROLL for (int n = 0; n < 2; n++) d[n] = MFMA16(kx[i], kw[i][n], d[n]); }
    UNROLL for (int n = 0; n < 2; n++) *(LAS f32x4*)(lds + B_PART + 1024 + (w4 * 2 + n) * 1024 + lane * 16) = d[n];
    asm volatile("s_waitcnt lgkmcnt(0)" ::: "memory");
    if (lane == 0) __hip_atomic_fetch_add((LAS unsigned*)(lds + B_PART), 1u, __ATOMIC_RELAXED, __HIP_MEMORY_SCOPE_WORKGROUP);
    if (wid >= 6) {
      float mean, rstd;
      stats_finish(sv, kth, mean, rstd);
      f32x4 m4, r4;
      UNROLL for (int j = 0; j < 4; j++) {
        const float mj = shfl(mean, 4 * g4 + j), rj = shfl(rstd, 4 * g4 + j);
        m4[j] = ktype ? mj : mean; r4[j] = ktype ? rj : rstd;
      }
      _Pragma("nounroll") for (unsigned spins = 0; spins < SPIN_CAP * 16 && pcnt[0] != 4u; spins++) __builtin_amdgcn_s_sleep(1);
      asm volatile("" ::: "memory");
      f32x4 e = -m4 * cs;
      UNROLL for (int w = 0; w < 4; w++) e += *(LAS f32x4*)(lds + B_PART + 1024 + (w * 2 + u) * 1024 + lane * 16);
      e *= r4;
      const int frag = ktype ? 2 + kth : u, lp = (ktype ? 16 * u : 16 * kth) + c16 + 32 * (g4 & 1);
      const unsigned e0 = cvtpk(e[0], e[1]), e1 = cvtpk(e[2], e[3]);
      const auto w0 = __builtin_amdgcn_permlane32_swap(e0, e0, false, false), w1 = __builtin_amdgcn_permlane32_swap(e1, e1, false, false);
      if (lane < 32) st16(x.rs, (u32x4){w0[0], w1[0], w0[1], w1[1]}, frag * 1024 + lp * 16, kvf + krt * 4096);
      if (last_arriver(lds + B_PART + 64, 2, lane)) set_flag(ws, 1u, 10 + L, x.grp * 32 + x.idx, lane);
    }
    __builtin_amdgcn_s_setprio(0);
    {
      STAMP(ph, 1);
      if (wid == 7) {
        wait_flags(ws, 1u, 10 + L, x.grp * 32 + lane, lane < 32);
        STAMP(ph, 2);
        UNROLL for (int i = 0; i < 8; i++) kv_dma(0, i * 4);
        kv_ok = kv_all();
        drain_vm();
        STAMP(ph, 7);
      }
    }
    __builtin_amdgcn_s_setprio(0);
    bar_only();
    if (wid == 7) *(LAS unsigned*)(lds + B_QX + 8192) = kv_ok;
    lgkm_bar();
    UNROLL for (int s = 0; s < 2; s++) qf[s] = lds_b128(lds + B_QX + ((wid & 3) * 2 + s) * 1024 + lane * 16);
  }
  __builtin_amdgcn_s_waitcnt(0x0F70);
  const bool kv_early = __builtin_amdgcn_readfirstlane(*(LAS unsigned*)(lds + B_QX + 8192)) != 0;
  STAMP(ph, 3); STAMPC(ph, 14);
  if (kv_early) kv_dma(1, wid * 4);
  const bf16x8 sel = sel_frag(lane);
  const f32x16 zero = {};
  f32x16 O = zero, negm, SA, SB_;
  f32x4 LS = {};
  bf16x8 kf[2], vf[2];
  const LdsLane ll(lds, lane);
  kf[0] = ll.rd(kv_at(0)); kf[1] = ll.rd(kv_at(0) + 1024);
  SA = MFMA32(kf[0], qf[0], zero); SA = MFMA32(kf[1], qf[1], SA);
  kf[0] = ll.rd(kv_at(1)); kf[1] = ll.rd(kv_at(1) + 1024);
  vf[0] = ll.rd(kv_at(0) + 2048); vf[1] = ll.rd(kv_at(0) + 3072);
  {
    float m = SA[0];
    UNROLL for (int i = 1; i < 16; i++) m = __builtin_fmaxf(m, SA[i]);
    m = half_max(m);
    UNROLL for (int i = 0; i < 16; i++) { negm[i] = -m; SA[i] -= m; }
  }
#define EX(i) Sc[i] = __builtin_amdgcn_exp2f(Sc[i])
#define PIN(x) asm volatile("" : "+v"(x))
  u32x4 p0, p1 = {0u, 0u, 0u, 0u};
  auto step = [&](f32x16& Sc, f32x16& Sn, int kp, int vc, int vp) AI {
    SB();
    Sn = MFMA32(kf[0], qf[0], negm);
    SB();
    kf[0] = ll.rd(kp);
    EX(0); EX(1); EX(2); PIN(Sc);
    SB();
    O = MFMA32(vf[1], __builtin_bit_cast(bf16x8, p1), O);
    SB();
    vf[1] = ll.rd(vc + 3072);
    EX(3); EX(4); EX(5); PIN(Sc);
    SB();
    Sn = MFMA32(kf[1], qf[1], Sn);
    SB();
    kf[1] = ll.rd(kp + 1024);
    EX(6); EX(7);
    p0[0] = cvtpk(Sc[0], Sc[1]); p0[1] = cvtpk(Sc[2], Sc[3]); p0[2] = cvtpk(Sc[4], Sc[5]); p0[3] = cvtpk(Sc[6], Sc[7]); PIN(p0);
    SB();
    LS = MFMA16(sel, __builtin_bit_cast(bf16x8, p1), LS);
    SB();
    EX(8); EX(9); PIN(Sc);
    SB();
    O = MFMA32(vf[0], __builtin_bit_cast(bf16x8, p0), O);
    SB();
    vf[0] = ll.rd(vp + 2048);
    EX(10); EX(11); EX(12); PIN(Sc);
    SB();
    LS = MFMA16(sel, __builtin_bit_cast(bf16x8, p0), LS);
    SB();
    EX(13); EX(14); EX(15);
    p1[0] = cvtpk(Sc[8], Sc[9]); p1[1] = cvtpk(Sc[10], Sc[11]); p1[2] = cvtpk(Sc[12], Sc[13]); p1[3] = cvtpk(Sc[14], Sc[15]); PIN(p1);
    SB();
  };
  auto hook = [&](int t) AI {
    if (t < 2) { if (kv_early) kv_dma(2 + t, wid * 4); }
    if (t == 2 && !kv_ok) {
      _Pragma("nounroll") for (unsigned spins = 0; spins < SPIN_CAP && !kv_all(); spins++) asm volatile("s_sleep 4" ::: "memory");
      _Pragma("nounroll") for (int i = 0; i < 8; i++) { kv_dma(1, i * 4); kv_dma(2, i * 4); kv_dma(3, i * 4); }
    }
    if (t == 6) wait_vm_bar<0>();
    if (t == 8 || t == 10) kv_dma2(4, wid * 4 + t - 8);
    if (t == 25) wait_vm_bar<0>();
    if (t >= 25 && t < 31) kv_dma2(5 + (t - 25) / 2, wid * 4 + ((t - 25) & 1) * 2);
    if (t == 38) wait_vm_bar<0>();
    if (t == 39 || t == 41) dma_w64<2, 2>(x, (t - 39) / 2, wid * 2);
    if (t == 49) bar_only();
    if (t >= 50 && t < 58 && !(t & 1)) dma_w64<2, 2>(x, 2 + (t - 50) / 2, wid * 2);
  };
  if (wid >= 4) __builtin_amdgcn_s_setprio(1);
  static_for<32>([&](auto tc) AI {
    constexpr int t = decltype(tc)::value * 2;
    hook(t);
    step(SA, SB_, kv_at((t + 2) & 63), kv_at(t), kv_at(t + 1));
    hook(t + 1);
    step(SB_, SA, kv_at((t + 3) & 63), kv_at(t + 1), kv_at((t + 2) & 63));
  });
  O = MFMA32(vf[1], __builtin_bit_cast(bf16x8, p1), O);
  LS = MFMA16(sel, __builtin_bit_cast(bf16x8, p1), LS);
  STAMP(ph, 4); STAMPC(ph, 15);
  __builtin_amdgcn_s_setprio(0);
  float l = LS[0];
  {
    float chk = l;
    UNROLL for (int i = 0; i < 16; i++) chk = __builtin_fmaxf(chk, __builtin_fabsf(O[i]) * 0x1p-27f);
    bool bad = !(chk < 0x1p100f);
    UNROLL for (int i = 0; i < 16; i++) bad |= (O[i] != O[i]);
    if (any_lane(bad)) {
      O = zero; l = 0.f;
      float m = -__builtin_inff();
      for (int t = 0; t < 64; t++) {
        bf16x8 f[4];
        UNROLL for (int i = 0; i < 4; i++) f[i] = __builtin_bit_cast(bf16x8, ld16(x.rs, i * 1024 + lane * 16, kvf + t * 4096));
        f32x16 s = MFMA32(f[0], qf[0], zero);
        s = MFMA32(f[1], qf[1], s);
        float mt = s[0];
        UNROLL for (int i = 1; i < 16; i++) mt = __builtin_fmaxf(mt, s[i]);
        const float mn = __builtin_fmaxf(m, half_max(mt));
        const float al = __builtin_amdgcn_exp2f(m - mn);
        m = mn;
        UNROLL for (int i = 0; i < 16; i++) { s[i] = __builtin_amdgcn_exp2f(s[i] - mn); O[i] *= al; }
        const bf16x8 p0 = pack8(s, 0), p1 = pack8(s, 1);
        f32x4 lt = MFMA16(sel, p0, ((f32x4){0.f, 0.f, 0.f, 0.f}));
        lt = MFMA16(sel, p1, lt);
        l = l * al + lt[0];
        O = MFMA32(f[2], p0, O);
        O = MFMA32(f[3], p1, O);
      }
    }
  }
  const float rl = 1.f / l;
  UNROLL for (int gp = 0; gp < 2; gp++) {
    float v[8];
    gather8(O, gp, v);
    st16(x.rs, (u32x4){cvtpk(v[0] * rl, v[1] * rl), cvtpk(v[2] * rl, v[3] * rl), cvtpk(v[4] * rl, v[5] * rl), cvtpk(v[6] * rl, v[7] * rl)},
         gp * 1024 + lane * 16, act_off(ph + 1) + rt * 65536 + head * 2048);
  }
  asm volatile("s_waitcnt vmcnt(0) lgkmcnt(0)\n\ts_barrier" ::: "memory");
  if (wid == 7) set_flag(ws, 1u, ph + 1, id0 + pj, lane);
  STAMP(ph, 5);
}

__global__ __launch_bounds__(512, 2) void k_main(char* ws, char* out, Inputs in) {
  extern __shared__ __attribute__((aligned(16))) char lds_raw[];
  Ctx x;
  x.ws = ws;
  x.rs = __builtin_amdgcn_make_buffer_rsrc(ws, 0, WS_USED, 0x00020000);
  x.lds = (lds_ptr)lds_raw;
  x.l0 = (unsigned)(uintptr_t)x.lds;
  x.lane = TID & 63;
  x.wid = __builtin_amdgcn_readfirstlane(TID >> 6);
  const int pg = BID & 7, pi = BID >> 3;
  x.ah = (pg >> 1) * 8 + (pi >> 2); x.at = (pg & 1) * 4 + (pi & 3);
  x.grp = (pg & 6) | (pi & 1); x.idx = (((pg & 1) * 8 + (pi >> 2)) << 1) | ((pi >> 1) & 1);
  x.hi = x.lane >> 5;
  x.tag = 0;
  x.tag = phase_p(fresh(x, 0), in, BID);
  { const Ctx y = fresh(x, 0); UNROLL for (int c = 0; c < 2; c++) dma_wqkv(y, c, y.wid); }
  u32x4 res[4] = {};
  for (int L = 0; L < 2; L++) {
    phase_a(fresh(x, 0), L, res);
    phase_out_a_b(fresh(x, 0), L, res);
    phase_out_b(fresh(x, 0), L, res, out);
  }
  if (BID == 0 && TID == 0) __hip_atomic_store((GAS u64*)(ws + OFF_EPOCH), x.tag, RLX_AGENT);
}

#if defined(__HIP_DEVICE_COMPILE__)
#pragma clang attribute pop
#endif

extern "C" void kernel_launch(void* const* d_in, const int* in_sizes, int n_in, void* d_out, int out_size, void* d_ws, size_t ws_size,
                              hipStream_t stream) {
  char* ws = (char*)d_ws;
  (void)hipFuncSetAttribute((const void*)k_main, hipFuncAttributeMaxDynamicSharedMemorySize, LDS_ALL);
  const Inputs in = {(const char*)d_in[0], (const char*)d_in[1], (const char*)d_in[2], (const char*)d_in[3],
                     (const char*)d_in[5], (const char*)d_in[6], (const char*)d_in[7], (const char*)d_in[8]};
  k_main<<<256, 512, LDS_ALL, stream>>>(ws, (char*)d_out, in);
}
```

```cpp
#include <hip/hip_runtime.h>
#include <hip/hip_bf16.h>
#include <stdint.h>

typedef unsigned short u16;
using bf16x8 = __attribute__((ext_vector_type(8))) short;
using s16x4 = __attribute__((ext_vector_type(4))) short;
using f32x16 = __attribute__((ext_vector_type(16))) float;
using f32x4 = __attribute__((ext_vector_type(4))) float;
using f32x2 = __attribute__((ext_vector_type(2))) float;
using u32x4 = __attribute__((ext_vector_type(4))) unsigned;
using u32x2 = __attribute__((ext_vector_type(2))) unsigned;
typedef __bf16 bf16x2_t __attribute__((ext_vector_type(2)));
#define LAS __attribute__((address_space(3)))
#define GAS __attribute__((address_space(1)))
typedef LAS char* lds_ptr;
typedef short v4i16_t __attribute__((ext_vector_type(4)));
#define DI __device__ __forceinline__
#define MFMA32(a, b, c) __builtin_amdgcn_mfma_f32_32x32x16_bf16((a), (b), (c), 0, 0, 0)
#define MFMA16(a, b, c) __builtin_amdgcn_mfma_f32_16x16x32_bf16((a), (b), (c), 0, 0, 0)
#define SB() __builtin_amdgcn_sched_barrier(0)
#define RLX_AGENT __ATOMIC_RELAXED, __HIP_MEMORY_SCOPE_AGENT
#define UNROLL _Pragma("unroll")
#define AI __attribute__((always_inline))

constexpr float LOG2E = 1.4426950408889634f;
constexpr unsigned SPIN_CAP = 1u << 19;

constexpr unsigned MBy = 1u << 20;
constexpr unsigned OFF_EPOCH = 0;
constexpr unsigned OFF_FL = 768 * 1024;
constexpr unsigned OFF_CSKV = 120 * 1024;
constexpr unsigned OFF_CS = 512 * 1024;
constexpr unsigned OFF_WKVF = 128 * 1024;
constexpr unsigned OFF_AUX = 256 * 1024;
constexpr unsigned AUX_B = 8192;
constexpr unsigned OFF_ST = 1 * MBy;
constexpr unsigned OFF_KVF = 2 * MBy;
constexpr unsigned OFF_SLAB = 4 * MBy;
constexpr unsigned SLAB_B = 8448;
constexpr unsigned OFF_ACT = 16 * MBy;
constexpr unsigned OFF_WTA = 48 * MBy;
constexpr unsigned OFF_WT64 = 54 * MBy;
constexpr unsigned WS_USED = 60 * MBy;

#if defined(__HIP_DEVICE_COMPILE__)
#pragma clang attribute push(__attribute__((target("no-packed-fp32-ops"))), apply_to = function)
#endif
#define U2F(x) __builtin_bit_cast(float, (unsigned)(x))
#define F2U(x) __builtin_bit_cast(unsigned, (float)(x))
#define TID ((int)__builtin_amdgcn_workitem_id_x())
#define BID ((int)__builtin_amdgcn_workgroup_id_x())
DI bool all_lanes(bool p) { return __builtin_amdgcn_ballot_w64(!p) == 0; }
DI bool any_lane(bool p) { return __builtin_amdgcn_ballot_w64(p) != 0; }
DI float shfl(float v, int src) { return U2F(__builtin_amdgcn_ds_bpermute(src * 4, (int)F2U(v))); }
DI void glds16(const void* sbase, unsigned voff, unsigned lds_base) {
  unsigned sv;
  asm volatile("s_mov_b32 %0, m0\n\ts_mov_b32 m0, %3\n\ts_nop 0\n\tglobal_load_lds_dwordx4 %1, %2\n\ts_mov_b32 m0, %0"
               : "=&s"(sv) : "v"(voff), "s"(sbase), "s"(lds_base) : "memory");
}
#define GL_(o, sc) "global_load_lds_dwordx4 %1, %2 offset:" #o sc "\n\t"
#define GLDS_N(NAME, SC)                                                                                                   \
  template <int N> DI void NAME(const void* sbase, unsigned voff, unsigned lds_base) {                                     \
    unsigned sv;                                                                                                           \
    if constexpr (N == 1)                                                                                                  \
      asm volatile("s_mov_b32 %0, m0\n\ts_mov_b32 m0, %3\n\ts_nop 0\n\t" GL_(0, SC) "s_mov_b32 m0, %0"                    \
                   : "=&s"(sv) : "v"(voff), "s"(sbase), "s"(lds_base) : "memory");                                         \
    else if constexpr (N == 2)                                                                                                  \
      asm volatile("s_mov_b32 %0, m0\n\ts_mov_b32 m0, %3\n\ts_nop 0\n\t" GL_(0, SC) GL_(1024, SC) "s_mov_b32 m0, %0"       \
                   : "=&s"(sv) : "v"(voff), "s"(sbase), "s"(lds_base) : "memory");                                         \
    else if constexpr (N == 3)                                                                                             \
      asm volatile("s_mov_b32 %0, m0\n\ts_mov_b32 m0, %3\n\ts_nop 0\n\t" GL_(0, SC) GL_(1024, SC) GL_(2048, SC) "s_mov_b32 m0, %0" \
                   : "=&s"(sv) : "v"(voff), "s"(sbase), "s"(lds_base) : "memory");                                         \
    else                                                                                                                   \
      asm volatile("s_mov_b32 %0, m0\n\ts_mov_b32 m0, %3\n\ts_nop 0\n\t" GL_(0, SC) GL_(1024, SC) GL_(2048, SC) GL_(3072, SC) "s_mov_b32 m0, %0" \
                   : "=&s"(sv) : "v"(voff), "s"(sbase), "s"(lds_base) : "memory");                                         \
  }
GLDS_N(gldsN_sc1, " sc1")
template <int N> DI void wait_vm_bar() { asm volatile("s_waitcnt vmcnt(%0)\n\ts_barrier" ::"n"(N) : "memory"); }
DI void bar_only() { asm volatile("s_barrier" ::: "memory"); }
DI void lgkm_bar() { asm volatile("s_waitcnt lgkmcnt(0)\n\ts_barrier" ::: "memory"); }
DI void drain_vm() { asm volatile("s_waitcnt vmcnt(0)" ::: "memory"); }
DI s16x4 lds_tr(lds_ptr p) { return __builtin_bit_cast(s16x4, __builtin_amdgcn_ds_read_tr16_b64_v4i16((LAS v4i16_t*)p)); }
template <int E> DI bf16x8 lds_trfrag(lds_ptr p) { s16x4 lo = lds_tr(p), hi = lds_tr(p + E); return __builtin_shufflevector(lo, hi, 0, 1, 2, 3, 4, 5, 6, 7); }
DI bf16x8 lds_b128(lds_ptr p) { return *(LAS bf16x8*)p; }
DI unsigned cvtpk(float lo, float hi) {
  bf16x2_t v = __builtin_convertvector((f32x2){lo, hi}, bf16x2_t);
  return __builtin_bit_cast(unsigned, v);
}
DI float bflo(unsigned u) { return U2F(u << 16); }
DI float bfhi(unsigned u) { return U2F(u & 0xffff0000u); }
DI float dot2(unsigned a, unsigned b, float c) {
  return __builtin_amdgcn_fdot2_f32_bf16(__builtin_bit_cast(bf16x2_t, a), __builtin_bit_cast(bf16x2_t, b), c, false);
}
DI float half_add(float x) { auto r = __builtin_amdgcn_permlane32_swap(F2U(x), F2U(x), false, false); return U2F(r[0]) + U2F(r[1]); }
DI float half_max(float x) { auto r = __builtin_amdgcn_permlane32_swap(F2U(x), F2U(x), false, false); return __builtin_fmaxf(U2F(r[0]), U2F(r[1])); }
DI float half_min(float x) { auto r = __builtin_amdgcn_permlane32_swap(F2U(x), F2U(x), false, false); return __builtin_fminf(U2F(r[0]), U2F(r[1])); }
DI bf16x8 pack8(const f32x16& x, int s) {
  u32x4 p = {cvtpk(x[8 * s], x[8 * s + 1]), cvtpk(x[8 * s + 2], x[8 * s + 3]), cvtpk(x[8 * s + 4], x[8 * s + 5]), cvtpk(x[8 * s + 6], x[8 * s + 7])};
  return __builtin_bit_cast(bf16x8, p);
}
DI u32x4 swap_cols(u32x4 p) {
  const auto a = __builtin_amdgcn_permlane32_swap(p[0], p[2], false, false), b = __builtin_amdgcn_permlane32_swap(p[1], p[3], false, false);
  return (u32x4){a[0], b[0], a[1], b[1]};
}
DI u32x4 pack8s(const f32x16& x, int s, float m) {
  return (u32x4){cvtpk(x[8 * s] * m, x[8 * s + 1] * m), cvtpk(x[8 * s + 2] * m, x[8 * s + 3] * m), cvtpk(x[8 * s + 4] * m, x[8 * s + 5] * m), cvtpk(x[8 * s + 6] * m, x[8 * s + 7] * m)};
}
typedef __amdgpu_buffer_rsrc_t rsrc_t;
DI u32x4 ld16(rsrc_t rs, unsigned voff, unsigned soff) { return __builtin_amdgcn_raw_buffer_load_b128(rs, voff, soff, 16); }
DI unsigned ld4(rsrc_t rs, unsigned voff, unsigned soff) { return __builtin_amdgcn_raw_buffer_load_b32(rs, voff, soff, 16); }
DI void st16(rsrc_t rs, u32x4 v, unsigned voff, unsigned soff) { __builtin_amdgcn_raw_buffer_store_b128(v, rs, voff + soff, 0, 16); }
DI void st4(rsrc_t rs, unsigned v, unsigned voff, unsigned soff) { __builtin_amdgcn_raw_buffer_store_b32(v, rs, voff, soff, 16); }
constexpr unsigned EMPTY = 0xffffffffu;
DI u32x4 not_empty(f32x4 v) {
  u32x4 r;
  UNROLL for (int j = 0; j < 4; j++) r[j] = F2U(v[j]) < EMPTY ? F2U(v[j]) : EMPTY - 1;
  return r;
}
typedef unsigned long long u64;
template <class T> DI GAS T* flag_at(char* ws, int slot, int id) { return (GAS T*)(ws + OFF_FL + (id >> 2) * 4096 + slot * 128) + (id & 3); }
template <class T> DI void set_flag(char* ws, T val, int slot, int id, int lane) {
  if (lane == 0) __hip_atomic_store(flag_at<T>(ws, slot, id), val, RLX_AGENT);
}
template <class T> DI void wait_flags(char* ws, T val, int slot, int id, bool on) {
  GAS T* p = flag_at<T>(ws, slot, id);
  __builtin_amdgcn_s_sleep(10);
  _Pragma("nounroll") for (unsigned spins = 0; spins < SPIN_CAP; spins++) {
    if (on) on = __hip_atomic_load(p, RLX_AGENT) != val;
    if (!any_lane(on)) break;
    __builtin_amdgcn_s_sleep(1);
  }
  asm volatile("" ::: "memory");
}
DI bool all_flags(rsrc_t rs, int slot, int lane) {
  const u32x4 v = ld16(rs, lane * 4096, OFF_FL + slot * 128);
  return all_lanes((v[0] & v[1] & v[2] & v[3]) == 1u);
}
DI bool all_flags_asm(char* ws, u64 tag, int slot, int lane) {
  u32x4 a, b;
  asm volatile("global_load_dwordx4 %0, %2, %3 sc1\n\tglobal_load_dwordx4 %1, %2, %3 offset:16 sc1\n\ts_waitcnt vmcnt(0)"
               : "=&v"(a), "=&v"(b) : "v"(lane * 4096), "s"(ws + OFF_FL + slot * 128) : "memory");
  const unsigned lo = (unsigned)tag, hi = (unsigned)(tag >> 32);
  return all_lanes(a[0] == lo && a[2] == lo && b[0] == lo && b[2] == lo && a[1] == hi && a[3] == hi && b[1] == hi && b[3] == hi);
}
DI bool last_arriver(lds_ptr cnt, unsigned n, int lane) {
  drain_vm();
  unsigned old = 0;
  if (lane == 0) old = __hip_atomic_fetch_add((LAS unsigned*)cnt, 1u, __ATOMIC_RELAXED, __HIP_MEMORY_SCOPE_WORKGROUP);
  return (unsigned)__builtin_amdgcn_readfirstlane(old) == n - 1;
}
DI float swap16_add(float a, float b) {
  auto r = __builtin_amdgcn_permlane16_swap(F2U(a), F2U(b), false, false);
  return U2F(r[0]) + U2F(r[1]);
}
DI float sum8(const u32x4 (&v)[2]) {
  return ((U2F(v[0][0]) + U2F(v[0][1])) + (U2F(v[0][2]) + U2F(v[0][3]))) + ((U2F(v[1][0]) + U2F(v[1][1])) + (U2F(v[1][2]) + U2F(v[1][3])));
}
DI void stats_load(rsrc_t rs, unsigned base, int lane, u32x4 (&v)[4]) {
  UNROLL for (int i = 0; i < 4; i++) v[i] = ld16(rs, lane * 16 + i * 1024, base);
}
DI u32x4 stats_pair(float s, float q) {
  auto a = __builtin_amdgcn_permlane16_swap(F2U(s), F2U(s), false, false), b = __builtin_amdgcn_permlane16_swap(F2U(q), F2U(q), false, false);
  return (u32x4){a[0], b[0], a[1], b[1]};
}
DI void stats_finish(const u32x4 (&v)[4], int half, float& mean, float& rstd) {
  float t[4];
  UNROLL for (int j = 0; j < 4; j++) t[j] = (U2F(v[0][j]) + U2F(v[1][j])) + (U2F(v[2][j]) + U2F(v[3][j]));
  if (half == 0) { t[2] = t[0]; t[3] = t[1]; }
  if (half == 1) { t[0] = t[2]; t[1] = t[3]; }
  const float s = half_add(swap16_add(t[0], t[2])), q = half_add(swap16_add(t[1], t[3]));
  mean = s * (1.f / 1024.f);
  rstd = __builtin_amdgcn_rsqf(__builtin_fmaxf(q * (1.f / 1024.f) - mean * mean, 0.f) + 1e-5f);
}
DI void split3(float x, unsigned& a, unsigned& b, unsigned& c) {
  a = cvtpk(x, x) & 0xffffu; x -= bflo(a);
  b = cvtpk(x, x) & 0xffffu; x -= bflo(b);
  c = cvtpk(x, x) & 0xffffu;
}
DI bf16x8 cs_frag(float cs, int hi) {
  unsigned a, b, c; split3(cs, a, b, c);
  u32x4 p = {a | (b << 16), a | (c << 16), b | (a << 16), 0u};
  if (hi) p = (u32x4){0u, 0u, 0u, 0u};
  return __builtin_bit_cast(bf16x8, p);
}
DI bf16x8 mean_frag(float mean) {
  unsigned a, b, c; split3(-mean, a, b, c);
  u32x4 p = {a | (a << 16), b | (a << 16), b | (c << 16), 0u};
  return __builtin_bit_cast(bf16x8, p);
}
constexpr int ramp(int s, int I0, int PD) { int n = I0 + 2 * (s + 1); if (n > s + 1 + PD) n = s + 1 + PD; return n > 64 ? 64 : n; }
template <int V> struct IC { static constexpr int value = V; };
template <int N, int I = 0, class F> DI void static_for(F&& f) {
  if constexpr (I < N) { f(IC<I>{}); static_for<N, I + 1>(f); }
}
template <bool RATIO, class Hook> DI float dual_softmax(const f32x16& p, float c, f32x16& ep, f32x16& em, Hook hook) {
  float mx = p[0], mn = p[0];
  UNROLL for (int i = 1; i < 16; i++) { mx = __builtin_fmaxf(mx, p[i]); mn = __builtin_fminf(mn, p[i]); }
  mx = half_max(mx); mn = half_min(mn);
  const float a = -mx * c, b = mn * c;
  float sp = 0.f, sm = 0.f;
  UNROLL for (int i = 0; i < 16; i++) {
    if (!RATIO) { asm volatile("" : "+v"(sp), "+v"(sm)); hook(i); }
    ep[i] = __builtin_amdgcn_exp2f(__builtin_fmaf(p[i], c, a));
    em[i] = __builtin_amdgcn_exp2f(__builtin_fmaf(p[i], -c, b));
    sp += ep[i]; sm += em[i];
  }
  sp = half_add(sp); sm = __builtin_amdgcn_rcpf(half_add(sm));
  if (RATIO) { sm *= sp; UNROLL for (int i = 0; i < 16; i++) em[i] *= sm; }
  else { const float rp = __builtin_amdgcn_rcpf(sp); UNROLL for (int i = 0; i < 16; i++) { ep[i] *= rp; em[i] *= sm; } }
  return sp;
}
typedef __bf16 bfv8 __attribute__((ext_vector_type(8)));
typedef __bf16 bfv16 __attribute__((ext_vector_type(16)));
using u32x8 = __attribute__((ext_vector_type(8))) unsigned;
DI bfv8 sel_frag(int lane) {
  const unsigned on = (lane & 2) ? 0u : 0x3f803f80u, lo = (lane & 4) ? 0u : on, hi = (lane & 4) ? on : 0u;
  return __builtin_bit_cast(bfv8, (u32x4){lo, lo, hi, hi});
}
DI f32x4 row_sums(bfv8 sel, int lane, u32x8 p, f32x4 c) {
  return __builtin_amdgcn_smfmac_f32_16x16x64_bf16(sel, __builtin_bit_cast(bfv16, p), c, (lane & 1) ? 0xEEEE : 0x4444, 0, 0);
}
DI bf16x8 lo_frag(u32x8 p) { return __builtin_bit_cast(bf16x8, __builtin_shufflevector(p, p, 0, 1, 2, 3)); }
DI bf16x8 hi_frag(u32x8 p) { return __builtin_bit_cast(bf16x8, __builtin_shufflevector(p, p, 4, 5, 6, 7)); }

struct Ctx {
  char* ws; rsrc_t rs; lds_ptr lds; unsigned l0; u64 tag;
  int lane, wid, grp, idx, hi, ah, at;
};
DI Ctx fresh(const Ctx& x) {
  Ctx y = x;
  asm volatile("" : "+s"(y.wid), "+s"(y.grp), "+s"(y.idx), "+s"(y.ah), "+s"(y.at));
  asm volatile("" : "+v"(y.lane));
  y.lane &= 63;
  y.hi = y.lane >> 5;
  return y;
}
struct LdsLane {
  lds_ptr b0, b1, b2;
  DI LdsLane(lds_ptr lds, int lane) : b0(lds + lane * 16), b1(b0 + 65536), b2(b0 + 131072) { asm volatile("" : "+v"(b0), "+v"(b1), "+v"(b2)); }
  DI bf16x8 rd(int off) const { return lds_b128((off < 65536 ? b0 : (off < 131072 ? b1 : b2)) + (off & 65535)); }
};
constexpr int LDS_ALL = 163840;
constexpr int A_SLOT = 24576, A_ROW = 224, A_IMG = 98304, A_KVL = A_IMG + 256 * A_ROW, A_ZL = A_KVL + 4096, A_AUX = 6 * A_SLOT;
static_assert(A_ZL + 128 <= LDS_ALL, "");
DI unsigned act_off(int i) { return OFF_ACT + i * 4 * MBy; }

DI int a_head(const Ctx& x) { return x.ah; }
DI int a_tile(const Ctx& x) { return x.at; }
DI void dma_wqkv(const Ctx& x, int c, int pg) {
  gldsN_sc1<3>(x.ws + OFF_WTA + a_head(x) * 196608 + c * A_SLOT + pg * 3072, x.lane * 16, x.l0 + (c % 6) * A_SLOT + pg * 3072);
}
constexpr int w64_at(int which, int c) {
  constexpr int t[3][8] = {{48, 64, 80, 0, 16, 32, 96, 112}, {128, 144, 48, 64, 80, 0, 16, 32}, {32, 48, 64, 80, 96, 112, 128, 144}};
  return t[which][c] * 1024;
}
template <int WHICH, int N> DI void dma_w64(const Ctx& x, int c, int p) {
  gldsN_sc1<N>(x.ws + OFF_WT64 + WHICH * 2 * MBy + (x.idx >> 1) * 131072 + c * 16384 + p * 1024, x.lane * 16, x.l0 + w64_at(WHICH, c) + p * 1024);
}
struct Inputs { const char *x0, *wqkv, *wphi, *woA, *wq, *wk, *wv, *woB; };
DI u64 phase_p(const Ctx& x, const Inputs& in, int bid) {
  char* ws = x.ws;
  const u64 epoch = __hip_atomic_load((GAS u64*)(ws + OFF_EPOCH), RLX_AGENT);
  const lds_ptr lds = x.lds;
  const int lane = x.lane, wid = x.wid, hi = x.hi, r = lane & 31, tid = wid * 64 + lane;
  const int kb = bid & 15, cu = bid >> 4;
  UNROLL for (int g = 0; g < 3; g++)
    glds16(in.wqkv + (size_t)(kb * 64 + wid * 8) * 6144 + g * 2048 + cu * 128, (lane >> 3) * 6144 + (lane & 7) * 16, x.l0 + g * 8192 + wid * 1024);
  const int rt = bid >> 2, ct = (bid & 3) * 4 + (wid & 3);
  u32x4 xv[4];
  if (wid < 4) UNROLL for (int i = 0; i < 4; i++) xv[i] = *(const GAS u32x4*)(in.x0 + (size_t)(rt * 32 + r) * 2048 + ct * 128 + i * 32 + hi * 16);
  bf16x8 sm;
  if (wid == 7 && bid < 64) UNROLL for (int j = 0; j < 8; j++)
    sm[j] = ((const GAS short*)in.wphi)[(bid >> 1) * 1024 + (16 * (bid & 1) + 8 * (j >> 2) + 4 * hi + (j & 3)) * 32 + r];
  if (wid == 7 && bid >= 128) {
    const GAS short* W = (const GAS short*)(bid >= 192 ? in.wv : in.wk);
    UNROLL for (int j = 0; j < 8; j++) sm[j] = W[(32 * ((bid >> 1) & 31) + 8 * (lane >> 4) + j) * 32 + 16 * (bid & 1) + (lane & 15)];
  }
  UNROLL for (int g = 0; g < 3; g++)
    glds16((g == 0 ? in.woA : (g == 1 ? in.wq : in.woB)) + (size_t)(kb * 64 + wid * 8) * 2048 + cu * 128, (lane >> 3) * 2048 + (lane & 7) * 16,
           x.l0 + 24576 + g * 8192 + wid * 1024);
  wait_vm_bar<3>();
  const int ks = wid >> 1, nh = wid & 1, step = kb * 4 + ks;
  const lds_ptr p = lds + (ks * 16 + 8 * hi + ((lane & 15) >> 2)) * 128 + nh * 64 + ((lane >> 4) & 1) * 32 + (lane & 3) * 8;
  LAS float* red = (LAS float*)(lds + 49152);
  auto colsum = [&](u32x4 fr, int at) AI {
    float c = 0.f;
    UNROLL for (int k = 0; k < 4; k++) c = dot2(fr[k], 0x3f803f80u, c);
    c = half_add(c);
    if (hi == 0) red[ks * 256 + at + r] = c;
  };
  UNROLL for (int g = 0; g < 3; g++) {
    const u32x4 fr = __builtin_bit_cast(u32x4, lds_trfrag<512>(p + g * 8192));
    st16(x.rs, fr, lane * 16, OFF_WTA + ((cu * 2 + nh) * 192 + step * 3 + g) * 1024);
    colsum(fr, nh * 96 + g * 32);
  }
  if (wid < 4) {
    float s = 0, q = 0;
    UNROLL for (int i = 0; i < 4; i++) {
      st16(x.rs, xv[i], lane * 16, OFF_ACT + rt * 65536 + (ct * 4 + i) * 1024);
      UNROLL for (int j = 0; j < 4; j++) { s = dot2(xv[i][j], 0x3f803f80u, s); q = dot2(xv[i][j], xv[i][j], q); }
    }
    const u32x4 sp = stats_pair(half_add(s), half_add(q));
    if (lane < 16) st16(x.rs, sp, lane * 16, OFF_ST + rt * 4096 + ct * 256);
  }
  if (wid == 7 && bid < 64) st16(x.rs, __builtin_bit_cast(u32x4, sm), lane * 16, OFF_AUX + (bid >> 1) * AUX_B + 6144 + (bid & 1) * 1024);
  lgkm_bar();
  if (wid == 6 && lane < 48) {
    const f32x4 t = (*(LAS f32x4*)(red + lane * 4) + *(LAS f32x4*)(red + 256 + lane * 4)) + (*(LAS f32x4*)(red + 512 + lane * 4) + *(LAS f32x4*)(red + 768 + lane * 4));
    st16(x.rs, __builtin_bit_cast(u32x4, t), (lane >= 24 ? AUX_B - 384 : 0) + lane * 16, OFF_AUX + cu * 2 * AUX_B + kb * 384);
  }
  asm volatile("s_waitcnt vmcnt(0) lgkmcnt(0)\n\ts_barrier" ::: "memory");
  const u64 tag = (((u64)(unsigned)__builtin_amdgcn_readfirstlane((unsigned)(epoch >> 32)) << 32) | (unsigned)__builtin_amdgcn_readfirstlane((unsigned)epoch)) *
                      6364136223846793005ull + 1442695040888963407ull;
  if (wid == 0) set_flag(ws, tag, 0, bid, lane);
  UNROLL for (int g = 0; g < 3; g++) {
    const u32x4 fr = __builtin_bit_cast(u32x4, lds_trfrag<512>(p + 24576 + g * 8192));
    st16(x.rs, fr, lane * 16, OFF_WT64 + g * 2 * MBy + (cu * 128 + step * 2 + nh) * 1024);
    if (g == 1) colsum(fr, nh * 32);
  }
  if (wid == 5 && lane < 24) {
    const int slot = (bid & 3) * 3 + (lane >> 3);
    if (slot != 0 && slot != 8) st16(x.rs, (u32x4){0u, 0u, 0u, 0u}, slot * 128 + (lane & 7) * 16, OFF_FL + (bid >> 2) * 4096);
  }
  const u32x4 e = {EMPTY, EMPTY, EMPTY, EMPTY};
  st16(x.rs, e, tid * 16, OFF_SLAB + (x.ah * 8 + x.at) * SLAB_B);
  if (tid < 16) st16(x.rs, e, tid * 16, OFF_SLAB + (x.ah * 8 + x.at) * SLAB_B + 8192);
  if (wid == 7 && bid >= 128) {
    st16(x.rs, __builtin_bit_cast(u32x4, sm), lane * 16, OFF_WKVF + (bid - 128) * 1024);
    float c = 0.f;
    UNROLL for (int k = 0; k < 4; k++) c = dot2(__builtin_bit_cast(u32x4, sm)[k], 0x3f803f80u, c);
    c = half_add(swap16_add(c, c));
    if (lane < 16) st4(x.rs, F2U(c), lane * 4, OFF_CSKV + ((bid >= 192) * 2 + (bid & 1)) * 2048 + ((bid >> 1) & 31) * 64);
  }
  lgkm_bar();
  if (wid == 6 && lane < 16) {
    u32x4 t;
    UNROLL for (int j = 0; j < 4; j++) t[j] = F2U((red[j * 16 + lane] + red[256 + j * 16 + lane]) + (red[512 + j * 16 + lane] + red[768 + j * 16 + lane]));
    st16(x.rs, t, lane * 16, OFF_CS + cu * 4096 + kb * 256);
  }
  if (wid == 0) wait_flags(ws, tag, 0, lane < 16 ? (x.ah >> 1) * 16 + lane : (lane < 48 ? x.at * 32 + lane - 16 : x.ah * 2 + (lane & 1)), lane < 50);
  asm volatile("s_waitcnt vmcnt(0) lgkmcnt(0)\n\ts_barrier" ::: "memory");
  if (wid == 0) set_flag(ws, tag, 8, bid, lane);
  return tag;
}

DI void phase_a(const Ctx& x, int L, u32x4 (&res)[4]) {
  char* ws = x.ws;
  const int lane = x.lane, wid = x.wid, hi = x.hi, h = a_head(x), tt = a_tile(x), r = lane & 31, rt = tt * 8 + wid;
  const lds_ptr lds = x.lds;
  constexpr int PD = 16, I0 = 6;
  auto aux = [&]() AI { gldsN_sc1<1>(ws + OFF_AUX + h * AUX_B + wid * 1024, lane * 16, x.l0 + A_AUX + wid * 1024); };
  float csv[3][8];
  bf16x8 wph[2], csf[3], mf;
  float rstd;
  if (L) {
    if (wid == 0) { wait_flags(ws, 1u, 4 * L, tt * 32 + lane, lane < 32); bar_only(); }
    UNROLL for (int c = 4; c < 6; c++) dma_wqkv(x, c, wid);
    aux();
    if (wid != 0) wait_vm_bar<7>();
  }
  const unsigned abase = act_off(4 * L) + rt * 65536;
  auto aload = [&](int s) AI { return __builtin_bit_cast(bf16x8, ld16(x.rs, lane * 16 + (s & 3) * 1024, abase + (s >> 2) * 4096)); };
  bf16x8 a[PD];
  UNROLL for (int s = 0; s < I0; s++) a[s] = aload(s);
  f32x16 acc[3] = {{}, {}, {}};
  bf16x8 bf[2][3];
  const LdsLane ll(lds, lane);
  auto bread = [&](int s) AI { UNROLL for (int i = 0; i < 3; i++) bf[s & 1][i] = ll.rd((s * 3 + i) * 1024); };
  bf16x8 tw[6];
  u32x4 stv[4];
  if (L == 0) {
    UNROLL for (int c = 2; c < 6; c++) dma_wqkv(x, c, wid);
    aux();
    wait_vm_bar<I0 + 12>();
  }
  bread(0);
  static_for<48>([&](auto sc) AI {
    constexpr int s = decltype(sc)::value;
    if (s == 15) { wait_vm_bar<ramp(14, I0, PD) - I0>(); dma_wqkv(x, 6, wid); }
    if (s == 25) {
      if (L == 0 && wid == 0)
        _Pragma("nounroll") for (unsigned spins = 0; spins < SPIN_CAP && !all_flags_asm(ws, x.tag, 8, lane); spins++) {}
      bar_only(); dma_wqkv(x, 7, wid); dma_w64<0, 2>(x, 0, wid * 2); }
    if (s == 47) {
      asm volatile("s_waitcnt vmcnt(22) lgkmcnt(0)\n\ts_barrier" ::: "memory");
      dma_w64<0, 2>(x, 1, wid * 2); dma_w64<0, 2>(x, 2, wid * 2);
    }
    if (s == 28) stats_load(x.rs, OFF_ST + 2 * L * 262144 + rt * 4096, lane, stv);
    if (s == 42) {
      UNROLL for (int i = 0; i < 3; i++) UNROLL for (int k = 0; k < 8; k++) csv[i][k] = *(LAS float*)(lds + A_AUX + ((hi * 8 + k) * 96 + i * 32 + r) * 4);
      UNROLL for (int i = 0; i < 2; i++) wph[i] = ll.rd(A_AUX + 6144 + i * 1024);
    }
    if (s == 44) {
      float mean;
      stats_finish(stv, -1, mean, rstd);
      mf = mean_frag(mean);
      UNROLL for (int i = 0; i < 3; i++) csf[i] = cs_frag(half_add(((csv[i][0] + csv[i][1]) + (csv[i][2] + csv[i][3])) + ((csv[i][4] + csv[i][5]) + (csv[i][6] + csv[i][7]))), hi);
    }
    if (s == 45) UNROLL for (int i = 0; i < 3; i++) acc[i] = MFMA32(csf[i], mf, acc[i]);
    if (s == 47) {
      if (L == 0) UNROLL for (int i = 0; i < 4; i++)
        res[i] = ld16(x.rs, i * 1024 + lane * 16, act_off(0) + ((x.grp * 2 + (x.idx & 1)) * 4 + (wid & 3)) * 65536 + (x.idx >> 1) * 4096);
    }
    if (s < 47) bread(s + 1);
    else UNROLL for (int j = 0; j < 6; j++) tw[j] = ll.rd(j * 3072 + 1024);
    SB();
    const bf16x8 av = a[s % PD];
    UNROLL for (int i = 0; i < 3; i++) acc[i] = MFMA32(bf[s & 1][i], av, acc[i]);
    if (s == 44) asm volatile("" : "+v"(rstd), "+v"(wph[0]), "+v"(wph[1]));
    SB();
    UNROLL for (int n = ramp(s - 1, I0, PD); n < ramp(s, I0, PD); n++) a[n % PD] = aload(n);
    SB();
  });
  auto tail = [&](int n) AI {
    acc[n < 16 ? 1 : 2] = MFMA32(tw[n % 6], a[n & 15], acc[n < 16 ? 1 : 2]);
    SB();
    if (n + 6 < 32) tw[n % 6] = ll.rd(((n + 6) & 15) * 3072 + (n + 6 < 16 ? 1024 : 2048));
    SB();
  };
  static_for<16>([&](auto nc) AI { tail(decltype(nc)::value); });
  asm volatile("" : "+v"(acc[0]));
  const f32x16 zero = {};
  {
    tail(16); tail(17);
    const bf16x8 k0 = pack8(acc[1], 0), k1 = pack8(acc[1], 1);
    SB();
    f32x16 p = MFMA32(wph[0], k0, zero);
    p = MFMA32(wph[1], k1, p);
    SB();
    tail(18); tail(19);
    const lds_ptr rowb = lds + A_IMG + (wid * 32 + r) * A_ROW;
    const lds_ptr row[2] = {rowb + ((hi ^ (r >> 2)) & 3) * 8, rowb + (((2 + hi) ^ (r >> 2)) & 3) * 8};
    f32x16 ep, em;
    dual_softmax<false>(p, rstd * LOG2E, ep, em, [&](int i) AI { if (i < 12) tail(20 + i); });
    UNROLL for (int g = 0; g < 4; g++)
      *(LAS u32x2*)(row[g & 1] + 128 + (g >> 1) * 32) = (u32x2){cvtpk(acc[2][4 * g] * rstd, acc[2][4 * g + 1] * rstd), cvtpk(acc[2][4 * g + 2] * rstd, acc[2][4 * g + 3] * rstd)};
    UNROLL for (int g = 0; g < 4; g++) {
      *(LAS u32x2*)(row[g & 1] + (g >> 1) * 32) = (u32x2){cvtpk(ep[4 * g], ep[4 * g + 1]), cvtpk(ep[4 * g + 2], ep[4 * g + 3])};
      *(LAS u32x2*)(row[g & 1] + 64 + (g >> 1) * 32) = (u32x2){cvtpk(em[4 * g], em[4 * g + 1]), cvtpk(em[4 * g + 2], em[4 * g + 3])};
    }
  }
  lgkm_bar();
  const unsigned slab = OFF_SLAB + (L * 256 + h * 8) * SLAB_B;
  {
    __builtin_amdgcn_s_setprio(3);
    const int ft = wid >> 1, dt = wid & 1;
    const lds_ptr ip = lds + A_IMG + (lane >> 2) * A_ROW + ((lane ^ (lane >> 4)) & 3) * 8;
    bf16x8 pf[8], vf[8];
    UNROLL for (int u = 0; u < 8; u++) { pf[u] = lds_trfrag<16 * A_ROW>(ip + u * 32 * A_ROW + ft * 32); vf[u] = lds_trfrag<16 * A_ROW>(ip + u * 32 * A_ROW + 128 + dt * 32); }
    SB();
    f32x4 kv = {}, z = {};
    UNROLL for (int u = 0; u < 8; u++) kv = MFMA16(pf[u], vf[u], kv);
    st16(x.rs, not_empty(kv), lane * 16, slab + tt * SLAB_B + wid * 1024);
    if (dt == 0) {
      const short one = (lane & 15) == 0 ? (short)0x3f80 : (short)0;
      const bf16x8 ones = {one, one, one, one, one, one, one, one};
      UNROLL for (int u = 0; u < 8; u++) z = MFMA16(pf[u], ones, z);
      if ((lane & 15) == 0) st16(x.rs, not_empty(z), lane, slab + tt * SLAB_B + 8192 + ft * 64);
    }
  }
  __builtin_amdgcn_s_setprio(0);
  u32x4 gv[8];
  unsigned gz;
  auto gather = [&]() AI {
    UNROLL for (int t = 0; t < 8; t++) gv[t] = ld16(x.rs, lane * 16, slab + t * SLAB_B + wid * 1024);
    gz = ld4(x.rs, (lane >> 3) * SLAB_B + (lane & 7) * 4, slab + 8192 + wid * 32);
    asm volatile("" ::: "memory");
  };
  bf16x8 phq[4];
  float qs;
  {
    bf16x8 qw[16];
    UNROLL for (int s = 0; s < 16; s++) qw[s] = ll.rd(s * 3072);
    SB();
    UNROLL for (int s = 0; s < 16; s++) acc[0] = MFMA32(qw[s], a[s], acc[0]);
    asm volatile("" : "+v"(acc[0]));
    gather();
    SB();
    f32x16 p = MFMA32(wph[0], pack8(acc[0], 0), zero);
    p = MFMA32(wph[1], pack8(acc[0], 1), p);
    f32x16 ep, em;
    qs = dual_softmax<true>(p, rstd * LOG2E, ep, em, [](int) {});
    phq[0] = pack8(ep, 0); phq[1] = pack8(ep, 1); phq[2] = pack8(em, 0); phq[3] = pack8(em, 1);
    asm volatile("" : "+v"(phq[0]), "+v"(phq[1]), "+v"(phq[2]), "+v"(phq[3]));
  }
  _Pragma("nounroll") for (unsigned spins = 0; spins < SPIN_CAP; spins++) {
    unsigned m = gz;
    UNROLL for (int t = 0; t < 8; t++) UNROLL for (int j = 0; j < 4; j++) m = m > gv[t][j] ? m : gv[t][j];
    if (all_lanes(m != EMPTY)) break;
    asm volatile("s_sleep 2" ::: "memory");
    gather();
    }
  {
    f32x4 s;
    UNROLL for (int j = 0; j < 4; j++)
      s[j] = ((U2F(gv[0][j]) + U2F(gv[1][j])) + (U2F(gv[2][j]) + U2F(gv[3][j]))) + ((U2F(gv[4][j]) + U2F(gv[5][j])) + (U2F(gv[6][j]) + U2F(gv[7][j])));
    *(LAS u32x2*)(lds + A_KVL + (wid >> 1) * 1024 + ((wid & 1) * 16 + (lane & 15) + ((lane >> 4) & 1) * 32) * 16 + hi * 8) = (u32x2){cvtpk(s[0], s[1]), cvtpk(s[2], s[3])};
    float zs = U2F(gz);
    zs += U2F(__builtin_amdgcn_mov_dpp((int)F2U(zs), 0x128, 0xf, 0xf, true));
    zs = half_add(swap16_add(zs, zs));
    if (lane < 8) *(LAS u16*)(lds + A_ZL + (wid * 8 + lane) * 2) = (u16)cvtpk(zs, zs);
  }
  lgkm_bar();
  UNROLL for (int c = 3; c < 6; c++) dma_w64<0, 2>(x, c, wid * 2);
  {
    f32x16 num = zero, den = zero;
    UNROLL for (int fs = 0; fs < 4; fs++) {
      const bf16x8 kf = lds_b128(lds + A_KVL + fs * 1024 + lane * 16);
      const s16x4 z0 = *(LAS s16x4*)(lds + A_ZL + fs * 32 + hi * 8), z1 = *(LAS s16x4*)(lds + A_ZL + fs * 32 + hi * 8 + 16);
      num = MFMA32(kf, phq[fs], num);
      den = MFMA32(__builtin_shufflevector(z0, z1, 0, 1, 2, 3, 4, 5, 6, 7), phq[fs], den);
    }
    const float rd = __builtin_amdgcn_rcpf(den[0] + 1e-6f * qs);
    UNROLL for (int gp = 0; gp < 2; gp++) st16(x.rs, swap_cols(pack8s(num, gp, rd)), gp * 1024 + lane * 16, act_off(4 * L + 1) + rt * 65536 + h * 2048);
  }
  wait_vm_bar<0>();
  if (wid == 7) set_flag(ws, 1u, 4 * L + 1, tt * 32 + h, lane);
}

constexpr int GPD = 12;
template <int WHICH, class Hook>
DI void gemm64(const Ctx& x, unsigned abase, f32x16 (&acc)[2], Hook hook) {
  const int lane = x.lane;
  auto aload = [&](int j) AI { return __builtin_bit_cast(bf16x8, ld16(x.rs, lane * 16 + (j & 3) * 1024, abase + (j >> 2) * 4096)); };
  bf16x8 a[GPD], bf[4][2];
  UNROLL for (int j = 0; j < GPD; j++) a[j] = aload(j);
  const LdsLane ll(x.lds, lane);
  auto bread = [&](int s) AI { UNROLL for (int n = 0; n < 2; n++) bf[s & 3][n] = ll.rd(w64_at(WHICH, s >> 3) + (s & 7) * 2048 + n * 1024); };
  bread(0); bread(1);
  static_for<64>([&](auto jc) AI {
    constexpr int s = decltype(jc)::value;
    hook(s);
    if (s + 2 < 64) bread(s + 2);
    SB();
    acc[0] = MFMA32(bf[s & 3][0], a[s % GPD], acc[0]);
    acc[1] = MFMA32(bf[s & 3][1], a[s % GPD], acc[1]);
    SB();
    if (s + GPD < 64) a[s % GPD] = aload(s + GPD);
    SB();
  });
}

template <bool AFTER_B>
DI void out_compute(const Ctx& x, int L, u32x4 (&res)[4], char* out) {
  const int lane = x.lane, hi = x.hi, ph = 4 * L + (AFTER_B ? 3 : 1);
  const bool last = AFTER_B && L == 1;
  const int pn = x.idx >> 1, rt = (x.grp * 2 + (x.idx & 1)) * 4 + x.wid, r = lane & 31;
  f32x16 acc[2];
  UNROLL for (int i = 0; i < 4; i++) {
    const u32x4 p = swap_cols(res[i]);
    UNROLL for (int j = 0; j < 4; j++) { acc[i >> 1][8 * (i & 1) + 2 * j] = bflo(p[j]); acc[i >> 1][8 * (i & 1) + 2 * j + 1] = bfhi(p[j]); }
  }
  asm volatile("" : "+v"(acc[0]), "+v"(acc[1]));
  bar_only();
  gemm64<AFTER_B ? 2 : 0>(x, act_off(ph) + rt * 65536, acc, [&](int s) AI { if (s == 46) bar_only(); });
  float ss = 0.f, sq = 0.f;
  UNROLL for (int i = 0; i < 4; i++) {
    res[i] = swap_cols(__builtin_bit_cast(u32x4, pack8(acc[i >> 1], i & 1)));
    if (last) *(LAS u32x4*)(x.lds + x.wid * 4608 + r * 144 + i * 32 + hi * 16) = res[i];
    else st16(x.rs, res[i], i * 1024 + lane * 16, act_off(ph + 1) + rt * 65536 + pn * 4096);
  }
  if (last) UNROLL for (int i = 0; i < 4; i++) {
    const u32x4 t = *(LAS u32x4*)(x.lds + x.wid * 4608 + (i * 8 + (lane >> 3)) * 144 + (lane & 7) * 16);
    char* po = out + (size_t)(rt * 32 + i * 8 + (lane >> 3)) * 2048 + pn * 128 + (lane & 7) * 16;
    asm volatile("global_store_dwordx4 %0, %1, off sc1\n\ts_nop 1" ::"v"(po), "v"(t) : "memory");
  }
  if (!last) {
    SB();
    float s2 = 0.f, q2 = 0.f;
    UNROLL for (int i = 0; i < 2; i++) UNROLL for (int j = 0; j < 4; j++) {
      ss = dot2(res[i][j], 0x3f803f80u, ss); sq = dot2(res[i][j], res[i][j], sq);
      s2 = dot2(res[i + 2][j], 0x3f803f80u, s2); q2 = dot2(res[i + 2][j], res[i + 2][j], q2);
    }
    ss = half_add(ss + s2); sq = half_add(sq + q2);
    const u32x4 sp = stats_pair(ss, sq);
    if (lane < 16) st16(x.rs, sp, lane * 16, OFF_ST + ((ph + 1) >> 1) * 262144 + rt * 4096 + pn * 256);
  }
  wait_vm_bar<0>();
}
DI void phase_out_b(const Ctx& x, int L, u32x4 (&res)[4], char* out) {
  if (x.wid < 4) {
    out_compute<true>(x, L, res, out);
    if (x.wid == 0 && L == 0) set_flag(x.ws, 1u, 4, x.grp * 32 + (x.idx & 1) * 16 + (x.idx >> 1), x.lane);
    return;
  }
  const int w4 = x.wid - 4, id0 = x.grp * 32 + (x.idx & 1) * 16;
  if (w4 == 3) wait_flags(x.ws, 1u, 4 * L + 3, id0 + x.lane, x.lane < 16);
  else {
    UNROLL for (int c = 6; c < 8; c++) { dma_w64<2, 3>(x, c, w4 * 5); dma_w64<2, 2>(x, c, w4 * 5 + 3); if (w4 == c - 6) dma_w64<2, 1>(x, c, 15); }
    if (L == 0) UNROLL for (int i = 0; i < 3; i++) { if (w4 * 3 + i < 8) dma_wqkv(x, 0, w4 * 3 + i); }
  }
  bar_only();
  if (L == 1 && w4 == 3) { set_flag(x.ws, ~x.tag, 0, BID, x.lane); set_flag(x.ws, ~x.tag, 8, BID, x.lane); }
  drain_vm();
  bar_only();
  if (L == 0) UNROLL for (int c = 1; c < 4; c++) UNROLL for (int i = 0; i < 2; i++) dma_wqkv(x, c, w4 * 2 + i);
  bar_only();
}

constexpr int B_QX = 65536, B_PART = 98304;
constexpr int kv_at(int tile) { return (((tile >> 3) + 3) % 5) * 32768 + (tile & 7) * 4096; }
constexpr float QSCALE = 0.17677669529663687f * LOG2E;
DI void phase_out_a_b(const Ctx& x, int L, u32x4 (&res)[4]) {
  char* ws = x.ws;
  const lds_ptr lds = x.lds;
  const int lane = x.lane, wid = x.wid, hi = x.hi, g4 = lane >> 4, c16 = lane & 15, ph = 4 * L + 2;
  const int pm = x.grp * 2 + (x.idx & 1), pj = x.idx >> 1;
  const int rt = pm * 4 + (wid & 3), head = pj * 2 + (wid >> 2);
  const unsigned stb = OFF_ST + (2 * L + 1) * 262144, kvf = OFF_KVF + L * 262144;
  const int id0 = x.grp * 32 + (x.idx & 1) * 16;
  auto kv_dma = [&](int ch, int first) AI {
    gldsN_sc1<4>(ws + kvf + ((ch + x.grp) & 7) * 32768 + first * 1024, lane * 16, x.l0 + kv_at(ch * 8) + first * 1024);
  };
  auto kv_dma2 = [&](int ch, int first) AI {
    gldsN_sc1<2>(ws + kvf + ((ch + x.grp) & 7) * 32768 + first * 1024, lane * 16, x.l0 + kv_at(ch * 8) + first * 1024);
  };
  auto kv_all = [&]() AI { return all_flags(x.rs, 10 + L, lane); };
  bool kv_ok = true;
  bf16x8 qf[2];
  if (wid < 4) {
    out_compute<false>(x, L, res, nullptr);
    u32x4 csv[4];
    stats_load(x.rs, OFF_CS + pj * 4096, lane, csv);
    bar_only();
    f32x16 acc[2] = {{}, {}};
    u32x4 stv[4];
    float rstd;
    gemm64<1>(x, act_off(ph) + rt * 65536, acc, [&](int s) AI {
      if (s == 48) stats_load(x.rs, stb + rt * 4096, lane, stv);
      if (s == 61) {
        float mean;
        stats_finish(stv, -1, mean, rstd);
        const bf16x8 mf = mean_frag(mean);
        float t[4];
        UNROLL for (int k = 0; k < 4; k++) t[k] = (U2F(csv[0][k]) + U2F(csv[1][k])) + (U2F(csv[2][k]) + U2F(csv[3][k]));
        UNROLL for (int n = 0; n < 2; n++) acc[n] = MFMA32(cs_frag(half_add(swap16_add(t[2 * n], t[2 * n + 1])), hi), mf, acc[n]);
      }
    });
    const float sc = rstd * QSCALE;
    UNROLL for (int i = 0; i < 16; i++) { acc[0][i] *= sc; acc[1][i] *= sc; }
    qf[0] = pack8(acc[0], 0); qf[1] = pack8(acc[0], 1);
    bar_only();
    UNROLL for (int s = 0; s < 2; s++) *(LAS bf16x8*)(lds + B_QX + (wid * 2 + s) * 1024 + lane * 16) = pack8(acc[1], s);
    lgkm_bar();
  } else {
    const int w4 = wid - 4, krt = pm * 4 + (pj >> 2), kth = (pj >> 1) & 1, ktype = pj & 1, u = wid & 1;
    bf16x8 kx[8], kw[8][2];
    auto kw_load = [&]() AI {
      UNROLL for (int i = 0; i < 8; i++) UNROLL for (int n = 0; n < 2; n++)
        kw[i][n] = __builtin_bit_cast(bf16x8, ld16(x.rs, lane * 16 + n * 1024, OFF_WKVF + (ktype * 32 + w4 * 8 + i) * 2048));
    };
    if (wid == 7) wait_flags(ws, 1u, ph - 1, x.grp * 32 + lane, lane < 32);
    else {
      UNROLL for (int c = 6; c < 8; c++) { dma_w64<0, 3>(x, c, w4 * 5); dma_w64<0, 2>(x, c, w4 * 5 + 3); if (w4 == c % 3) dma_w64<0, 1>(x, c, 15); }
      UNROLL for (int c = 0; c < 2; c++) { dma_w64<1, 3>(x, c, w4 * 5); dma_w64<1, 2>(x, c, w4 * 5 + 3); if (w4 == c) dma_w64<1, 1>(x, c, 15); }
    }
    bar_only();
    if (L == 0) {
      const u32x4 e = {EMPTY, EMPTY, EMPTY, EMPTY};
      const int b = x.grp * 32 + x.idx;
      UNROLL for (int i = 0; i < 2; i++) st16(x.rs, e, lane * 16, OFF_SLAB + (256 + b) * SLAB_B + (w4 * 2 + i) * 1024);
      if (w4 == 0 && lane < 16) st16(x.rs, e, lane * 16, OFF_SLAB + (256 + b) * SLAB_B + 8192);
    }
    u32x4 cp[2] = {};
    if (wid >= 6) UNROLL for (int i = 0; i < 8; i++) cp[i >> 2][i & 3] = ld4(x.rs, i * 256 + lane * 4, OFF_CSKV + (ktype * 2 + u) * 2048);
    wait_vm_bar<0>();
    UNROLL for (int c = 2; c < 7; c++) dma_w64<1, 4>(x, c, w4 * 4);
    kw_load();
    bar_only();
    volatile LAS unsigned* pcnt = (volatile LAS unsigned*)(lds + B_PART);
    if (wid == 5) { set_flag(ws, 1u, ph, id0 + pj, lane); wait_flags(ws, 1u, ph, id0 + lane, lane < 16); }
    else { dma_w64<1, 4>(x, 7, w4 * 4); if (wid == 4) dma_w64<1, 4>(x, 7, 4); }
    if (wid == 6) { pcnt[0] = 0u; pcnt[16] = 0u; }
    f32x4 cs = {};
    if (wid >= 6) {
      const float c = half_add(swap16_add(sum8(cp), sum8(cp)));
      UNROLL for (int j = 0; j < 4; j++) { const float cj = shfl(c, 4 * g4 + j); cs[j] = ktype ? c : cj; }
    }
    asm volatile("s_waitcnt vmcnt(0) lgkmcnt(0)\n\ts_barrier" ::: "memory");
    __builtin_amdgcn_s_setprio(2);
    UNROLL for (int i = 0; i < 8; i++)
      kx[i] = __builtin_bit_cast(bf16x8, ld16(x.rs, (g4 >> 1) * 1024 + (16 * kth + c16 + 32 * (g4 & 1)) * 16 + (i & 1) * 2048,
                                              act_off(ph) + krt * 65536 + w4 * 16384 + (i >> 1) * 4096));
    u32x4 sv[4];
    if (wid >= 6) stats_load(x.rs, stb + krt * 4096, lane, sv);
    f32x4 d[2] = {{}, {}};
    if (ktype == 0) { UNROLL for (int i = 0; i < 8; i++) UNROLL for (int n = 0; n < 2; n++) d[n] = MFMA16(kw[i][n], kx[i], d[n]); }
    else { UNROLL for (int i = 0; i < 8; i++) UNROLL for (int n = 0; n < 2; n++) d[n] = MFMA16(kx[i], kw[i][n], d[n]); }
    UNROLL for (int n = 0; n < 2; n++) *(LAS f32x4*)(lds + B_PART + 1024 + (w4 * 2 + n) * 1024 + lane * 16) = d[n];
    asm volatile("s_waitcnt lgkmcnt(0)" ::: "memory");
    if (lane == 0) __hip_atomic_fetch_add((LAS unsigned*)(lds + B_PART), 1u, __ATOMIC_RELAXED, __HIP_MEMORY_SCOPE_WORKGROUP);
    if (wid >= 6) {
      float mean, rstd;
      stats_finish(sv, kth, mean, rstd);
      f32x4 m4, r4;
      UNROLL for (int j = 0; j < 4; j++) {
        const float mj = shfl(mean, 4 * g4 + j), rj = shfl(rstd, 4 * g4 + j);
        m4[j] = ktype ? mj : mean; r4[j] = ktype ? rj : rstd;
      }
      _Pragma("nounroll") for (unsigned spins = 0; spins < SPIN_CAP * 16 && pcnt[0] != 4u; spins++) __builtin_amdgcn_s_sleep(1);
      asm volatile("" ::: "memory");
      f32x4 e = -m4 * cs;
      UNROLL for (int w = 0; w < 4; w++) e += *(LAS f32x4*)(lds + B_PART + 1024 + (w * 2 + u) * 1024 + lane * 16);
      e *= r4;
      const int frag = ktype ? 2 + kth : u, lp = (ktype ? 16 * u : 16 * kth) + c16 + 32 * (g4 & 1);
      const unsigned e0 = cvtpk(e[0], e[1]), e1 = cvtpk(e[2], e[3]);
      const auto w0 = __builtin_amdgcn_permlane32_swap(e0, e0, false, false), w1 = __builtin_amdgcn_permlane32_swap(e1, e1, false, false);
      if (lane < 32) st16(x.rs, (u32x4){w0[0], w1[0], w0[1], w1[1]}, frag * 1024 + lp * 16, kvf + krt * 4096);
      if (last_arriver(lds + B_PART + 64, 2, lane)) set_flag(ws, 1u, 10 + L, x.grp * 32 + x.idx, lane);
    }
    __builtin_amdgcn_s_setprio(0);
    {
      if (wid == 7) {
        wait_flags(ws, 1u, 10 + L, x.grp * 32 + lane, lane < 32);
        UNROLL for (int i = 0; i < 8; i++) kv_dma(0, i * 4);
        kv_ok = kv_all();
        drain_vm();
      }
    }
    __builtin_amdgcn_s_setprio(0);
    bar_only();
    if (wid == 7) *(LAS unsigned*)(lds + B_QX + 8192) = kv_ok;
    lgkm_bar();
    UNROLL for (int s = 0; s < 2; s++) qf[s] = lds_b128(lds + B_QX + ((wid & 3) * 2 + s) * 1024 + lane * 16);
  }
  __builtin_amdgcn_s_waitcnt(0x0F70);
  const unsigned kv_word = *(LAS unsigned*)(lds + B_QX + 8192);
  const bfv8 sel = sel_frag(lane);
  const f32x16 zero = {};
  f32x16 O = zero, negm, SA, SB_;
  f32x4 LS = {};
  bf16x8 kf[2], vf[2];
  const LdsLane ll(lds, lane);
  kf[0] = ll.rd(kv_at(0)); kf[1] = ll.rd(kv_at(0) + 1024);
  SA = MFMA32(kf[0], qf[0], zero); SA = MFMA32(kf[1], qf[1], SA);
  kf[0] = ll.rd(kv_at(1)); kf[1] = ll.rd(kv_at(1) + 1024);
  vf[0] = ll.rd(kv_at(0) + 2048); vf[1] = ll.rd(kv_at(0) + 3072);
  SB();
  const bool kv_early = __builtin_amdgcn_readfirstlane(kv_word) != 0;
  if (kv_early) kv_dma(1, wid * 4);
  {
    float m = SA[0];
    UNROLL for (int i = 1; i < 16; i++) m = __builtin_fmaxf(m, SA[i]);
    m = half_max(m) + 60.f;
    UNROLL for (int i = 0; i < 16; i++) { negm[i] = -m; SA[i] -= m; }
  }
#define EX(i) Sc[i] = __builtin_amdgcn_exp2f(Sc[i])
#define PIN(x) asm volatile("" : "+v"(x))
#define CV(i) pp[i] = cvtpk(Sc[2 * (i)], Sc[2 * (i) + 1])
  u32x8 pp = {};
  auto step = [&](f32x16& Sc, f32x16& Sn, int kp, int vc, int vp) AI {
    SB();
    Sn = MFMA32(kf[0], qf[0], negm);
    SB();
    kf[0] = ll.rd(kp);
    EX(0); EX(1); EX(2); EX(3); PIN(Sc);
    SB();
    Sn = MFMA32(kf[1], qf[1], Sn);
    SB();
    kf[1] = ll.rd(kp + 1024);
    EX(4); EX(5); EX(6); EX(7); PIN(Sc);
    SB();
    LS = row_sums(sel, lane, pp, LS);
    SB();
    EX(8); EX(9); PIN(Sc);
    SB();
    O = MFMA32(vf[1], hi_frag(pp), O);
    SB();
    vf[1] = ll.rd(vc + 3072);
    CV(0); CV(1); CV(2); CV(3); PIN(pp);
    SB();
    EX(10); EX(11); PIN(Sc);
    SB();
    O = MFMA32(vf[0], lo_frag(pp), O);
    SB();
    vf[0] = ll.rd(vp + 2048);
    EX(12); EX(13); EX(14); EX(15);
    CV(4); CV(5); CV(6); CV(7); PIN(pp);
    SB();
  };
  auto hook = [&](int t) AI {
    if (t < 2) { if (kv_early) kv_dma(2 + t, wid * 4); }
    if (t == 2 && !kv_ok) {
      _Pragma("nounroll") for (unsigned spins = 0; spins < SPIN_CAP && !kv_all(); spins++) asm volatile("s_sleep 4" ::: "memory");
      _Pragma("nounroll") for (int i = 0; i < 8; i++) { kv_dma(1, i * 4); kv_dma(2, i * 4); kv_dma(3, i * 4); }
    }
    if (t == 6) wait_vm_bar<0>();
    if (t == 8 || t == 10) kv_dma2(4, wid * 4 + t - 8);
    if (t == 25) wait_vm_bar<0>();
    if (t >= 25 && t < 31) kv_dma2(5 + (t - 25) / 2, wid * 4 + ((t - 25) & 1) * 2);
    if (t == 38) wait_vm_bar<0>();
    if (t == 39 || t == 41) dma_w64<2, 2>(x, (t - 39) / 2, wid * 2);
    if (t == 49) bar_only();
    if (t >= 50 && t < 58 && !(t & 1)) dma_w64<2, 2>(x, 2 + (t - 50) / 2, wid * 2);
  };
  if (wid >= 4) __builtin_amdgcn_s_setprio(1);
  static_for<32>([&](auto tc) AI {
    constexpr int t = decltype(tc)::value * 2;
    hook(t);
    step(SA, SB_, kv_at((t + 2) & 63), kv_at(t), kv_at(t + 1));
    hook(t + 1);
    step(SB_, SA, kv_at((t + 3) & 63), kv_at(t + 1), kv_at((t + 2) & 63));
  });
  O = MFMA32(vf[1], hi_frag(pp), O);
  LS = row_sums(sel, lane, pp, LS);
  __builtin_amdgcn_s_setprio(0);
  float l = LS[0] + LS[1];
  {
    const float so = (((O[0] + O[1]) + (O[2] + O[3])) + ((O[4] + O[5]) + (O[6] + O[7]))) + (((O[8] + O[9]) + (O[10] + O[11])) + ((O[12] + O[13]) + (O[14] + O[15])));
    const bool bad = !(l < 0x1p100f) || !(__builtin_fabsf(so) < __builtin_inff());
    if (any_lane(bad)) {
      O = zero; l = 0.f;
      float m = -__builtin_inff();
      for (int t = 0; t < 64; t++) {
        bf16x8 f[4];
        UNROLL for (int i = 0; i < 4; i++) f[i] = __builtin_bit_cast(bf16x8, ld16(x.rs, i * 1024 + lane * 16, kvf + t * 4096));
        f32x16 s = MFMA32(f[0], qf[0], zero);
        s = MFMA32(f[1], qf[1], s);
        float mt = s[0];
        UNROLL for (int i = 1; i < 16; i++) mt = __builtin_fmaxf(mt, s[i]);
        const float mn = __builtin_fmaxf(m, half_max(mt));
        const float al = __builtin_amdgcn_exp2f(m - mn);
        m = mn;
        UNROLL for (int i = 0; i < 16; i++) { s[i] = __builtin_amdgcn_exp2f(s[i] - mn); O[i] *= al; }
        UNROLL for (int i = 0; i < 8; i++) pp[i] = cvtpk(s[2 * i], s[2 * i + 1]);
        const f32x4 lt = row_sums(sel, lane, pp, (f32x4){0.f, 0.f, 0.f, 0.f});
        l = l * al + (lt[0] + lt[1]);
        O = MFMA32(f[2], lo_frag(pp), O);
        O = MFMA32(f[3], hi_frag(pp), O);
      }
    }
  }
  const float rl = __builtin_amdgcn_rcpf(l);
  UNROLL for (int gp = 0; gp < 2; gp++) st16(x.rs, swap_cols(pack8s(O, gp, rl)), gp * 1024 + lane * 16, act_off(ph + 1) + rt * 65536 + head * 2048);
  asm volatile("s_waitcnt vmcnt(0) lgkmcnt(0)\n\ts_barrier" ::: "memory");
  if (wid == 7) set_flag(ws, 1u, ph + 1, id0 + pj, lane);
}

__global__ __launch_bounds__(512, 2) void k_main(char* ws, char* out, Inputs in) {
  extern __shared__ __attribute__((aligned(16))) char lds_raw[];
  Ctx x;
  x.ws = ws;
  x.rs = __builtin_amdgcn_make_buffer_rsrc(ws, 0, WS_USED, 0x00020000);
  x.lds = (lds_ptr)lds_raw;
  x.l0 = (unsigned)(uintptr_t)x.lds;
  x.lane = TID & 63;
  x.wid = __builtin_amdgcn_readfirstlane(TID >> 6);
  const int pg = BID & 7, pi = BID >> 3;
  x.ah = (pg >> 1) * 8 + (pi >> 2); x.at = (pg & 1) * 4 + (pi & 3);
  x.grp = (pg & 6) | (pi & 1); x.idx = (((pg & 1) * 8 + (pi >> 2)) << 1) | ((pi >> 1) & 1);
  x.hi = x.lane >> 5;
  x.tag = 0;
  x.tag = phase_p(fresh(x), in, BID);
  { const Ctx y = fresh(x); UNROLL for (int c = 0; c < 2; c++) dma_wqkv(y, c, y.wid); }
  u32x4 res[4] = {};
  for (int L = 0; L < 2; L++) {
    phase_a(fresh(x), L, res);
    phase_out_a_b(fresh(x), L, res);
    phase_out_b(fresh(x), L, res, out);
  }
  if (BID == 0 && TID == 0) __hip_atomic_store((GAS u64*)(ws + OFF_EPOCH), x.tag, RLX_AGENT);
}

#if defined(__HIP_DEVICE_COMPILE__)
#pragma clang attribute pop
#endif

extern "C" void kernel_launch(void* const* d_in, const int* in_sizes, int n_in, void* d_out, int out_size, void* d_ws, size_t ws_size,
                              hipStream_t stream) {
  char* ws = (char*)d_ws + ((ws_size - WS_USED - 2 * MBy) & ~(size_t)(2 * MBy - 1));
  (void)hipFuncSetAttribute((const void*)k_main, hipFuncAttributeMaxDynamicSharedMemorySize, LDS_ALL);
  const Inputs in = {(const char*)d_in[0], (const char*)d_in[1], (const char*)d_in[2], (const char*)d_in[3],
                     (const char*)d_in[5], (const char*)d_in[6], (const char*)d_in[7], (const char*)d_in[8]};
  k_main<<<256, 512, LDS_ALL, stream>>>(ws, (char*)d_out, in);
}
```
